```python
import torch

def _softmax(x, axis=-1):
    return torch.softmax(x, dim=axis)

def _gelu(x):
    return torch.nn.functional.gelu(x, approximate='tanh')

def _silu(x):
    return torch.nn.functional.silu(x)

def _sigmoid(x):
    return torch.sigmoid(x)

def _layernorm(x, axis=-1, eps=1e-05):
    m = x.mean(dim=axis, keepdim=True)
    v = x.var(dim=axis, keepdim=True, unbiased=False)
    return (x - m) / torch.sqrt(v + eps)

def _scatter_add(tgt, idx, src, axis=0):
    return tgt.clone().index_add_(axis, idx, src)

def _segment_sum(src, seg, n):
    out = torch.zeros((n,) + src.shape[1:], dtype=src.dtype)
    return out.index_add_(0, seg, src)

def _pad(x, pw):
    flat = tuple((v for pair in reversed(pw) for v in pair))
    return torch.nn.functional.pad(x, flat)
kernel_name = 'adaLN_cond__gqa_block_cached__swiglu_ffn__prompt'

def setup_inputs():

    _w = lambda a: (lambda t: t.to(torch.bfloat16) if t.is_floating_point() else t)(torch.as_tensor(a))

    def _A_setup():
        B, Sq, D = (1, 2048, 448)
        Hq, Hkv, Sk, Dh = (16, 8, 2048, 256)
        assert Hq % Hkv == 0, (Hq, Hkv)
        return {'x': _w((lambda *s: torch.randn(*s, dtype=torch.bfloat16) * 0.05)(B, Sq, D)), 'k_cache': _w((lambda *s: torch.randn(*s, dtype=torch.bfloat16) * 3.2)(B, Hkv, Sk, Dh)), 'v_cache': _w((lambda *s: torch.randn(*s, dtype=torch.bfloat16) * 0.4)(B, Hkv, Sk, Dh)), 'wq': _w((lambda *s: torch.randn(*s, dtype=torch.bfloat16) * 3.2)(D, Hq * Dh) * D ** (-0.5)), 'wo': _w((lambda *s: torch.randn(*s, dtype=torch.bfloat16) * 0.4)(Hq * Dh, D) * (Hq * Dh) ** (-0.5))}
    _A_in = _A_setup()

    def _B_setup():
        B, S, D, F = (1, 2048, 448, 1792)
        return {'x': _w((lambda *s: torch.randn(*s, dtype=torch.bfloat16) * 0.05)(B, S, D)), 'w_gate': _w((lambda *s: torch.randn(*s, dtype=torch.bfloat16) * 0.05)(D, F)), 'w_up': _w((lambda *s: torch.randn(*s, dtype=torch.bfloat16) * 0.05)(D, F)), 'w_down': _w((lambda *s: torch.randn(*s, dtype=torch.bfloat16) * 0.05)(F, D))}
    _B_in = _B_setup()
    return {'A_x': _A_in['x'], 'A_k_cache': _A_in['k_cache'], 'A_v_cache': _A_in['v_cache'], 'A_wq': _A_in['wq'], 'A_wo': _A_in['wo'], 'B_x': _B_in['x'], 'B_w_gate': _B_in['w_gate'], 'B_w_up': _B_in['w_up'], 'B_w_down': _B_in['w_down'], 'c': _w((lambda *s: torch.randn(*s, dtype=torch.bfloat16) * 0.2)(1, 448) * 8.0), 'w_mod': _w((lambda *s: torch.randn(*s, dtype=torch.bfloat16) * 0.2)(448, 6 * 448) * 448 ** (-0.5) * 8.0)}

def reference(A_x, A_k_cache, A_v_cache, A_wq, A_wo, B_x, B_w_gate, B_w_up, B_w_down, c, w_mod):

    def _A_ref(x, k_cache, v_cache, wq, wo):
        B, Sq, D = x.shape
        Hq, Dh = (16, 256)
        Hkv = k_cache.shape[1]
        rep = Hq // Hkv
        q = (lambda x, *d: x.permute(*d))((x @ wq).reshape(B, Sq, Hq, Dh), 0, 2, 1, 3)
        k = (lambda x, n, axis: torch.repeat_interleave(x, n, dim=axis))(k_cache, rep, axis=1)
        v = (lambda x, n, axis: torch.repeat_interleave(x, n, dim=axis))(v_cache, rep, axis=1)
        scores = torch.einsum('bhqd,bhkd->bhqk', q, k) * Dh ** (-0.5)
        p = _softmax(scores, axis=-1)
        o = torch.einsum('bhqk,bhkd->bhqd', p, v)
        return (lambda x, *d: x.permute(*d))(o, 0, 2, 1, 3).reshape(B, Sq, Hq * Dh) @ wo

    def _B_ref(x, w_gate, w_up, w_down):
        g = _silu(x @ w_gate)
        u = x @ w_up
        return g * u @ w_down
    x = A_x
    mod = (_silu(c) @ w_mod).reshape(c.shape[0], 6, 1, -1)
    sa, ca, ga, sb, cb, gb = (mod[:, 0].clone(), mod[:, 1].clone(), mod[:, 2].clone(), mod[:, 3].clone(), mod[:, 4].clone(), mod[:, 5].clone())
    h = _layernorm(x, axis=-1) * (1 + ca) + sa
    x = x + ga * _A_ref(h, A_k_cache, A_v_cache, A_wq, A_wo)
    h = _layernorm(x, axis=-1) * (1 + cb) + sb
    x = x + gb * _B_ref(h, B_w_gate, B_w_up, B_w_down)
    return x
```

```cpp
#include <hip/hip_runtime.h>
#include <hip/hip_bf16.h>
#define EMUL 0
typedef __hip_bfloat16 bf16;
__device__ __forceinline__ float b2f(bf16 x) { return __bfloat162float(x); }
__device__ __forceinline__ bf16 f2b(float x) { return __float2bfloat16(x); }
__device__ __forceinline__ float rb(float x) { return __bfloat162float(__float2bfloat16(x)); }

constexpr int S = 2048, D = 448, HQ = 16, HKV = 8, DH = 256, F = 1792;

__global__ void k_mod(const bf16* c, const bf16* wmod, float* mod) {
  int j = blockIdx.x * 256 + threadIdx.x;
  if (j >= 6 * D) return;
  float acc = 0;
  for (int k = 0; k < D; ++k) { float cv = b2f(c[k]); float s = rb(cv / (1.f + __expf(-cv))); acc += s * b2f(wmod[k * 6 * D + j]); }
  mod[j] = rb(acc);
}
__global__ void k_lnmod(const float* xf, const bf16* xb, const float* shift, const float* scale, bf16* h) {
  int row = blockIdx.x; int lane = threadIdx.x;
  float v[7]; float s = 0;
  for (int i = 0; i < 7; ++i) { v[i] = xf ? xf[row * D + lane + 64 * i] : b2f(xb[row * D + lane + 64 * i]); s += v[i]; }
  for (int o = 32; o; o >>= 1) s += __shfl_xor(s, o);
  float mu = s / D; float q = 0;
  for (int i = 0; i < 7; ++i) { float d = v[i] - mu; q += d * d; }
  for (int o = 32; o; o >>= 1) q += __shfl_xor(q, o);
  float var = q / D;
  float rs = rsqrtf(var + 1e-5f);
  for (int i = 0; i < 7; ++i) { int c = lane + 64 * i; h[row * D + c] = f2b((v[i] - mu) * rs * (1.f + scale[c]) + shift[c]); }
}
__global__ void k_gemm(const bf16* A, const bf16* B, float* C, int M, int N, int K) {
  __shared__ float As[16][65], Bs[16][65];
  int tx = threadIdx.x & 15, ty = threadIdx.x >> 4;
  int m0 = blockIdx.y * 64, n0 = blockIdx.x * 64;
  float acc[4][4] = {};
  for (int k0 = 0; k0 < K; k0 += 16) {
    for (int i = threadIdx.x; i < 1024; i += 256) { int r = i >> 4, c = i & 15; As[c][r] = b2f(A[(long)(m0 + r) * K + k0 + c]); }
    for (int i = threadIdx.x; i < 1024; i += 256) { int r = i >> 6, c = i & 63; Bs[r][c] = b2f(B[(long)(k0 + r) * N + n0 + c]); }
    __syncthreads();
    for (int k = 0; k < 16; ++k) {
      float a[4], b[4];
      for (int i = 0; i < 4; ++i) a[i] = As[k][ty * 4 + i];
      for (int i = 0; i < 4; ++i) b[i] = Bs[k][tx * 4 + i];
      for (int i = 0; i < 4; ++i) for (int j = 0; j < 4; ++j) acc[i][j] += a[i] * b[j];
    }
    __syncthreads();
  }
  for (int i = 0; i < 4; ++i) for (int j = 0; j < 4; ++j) C[(long)(m0 + ty * 4 + i) * N + n0 + tx * 4 + j] = acc[i][j];
}
__global__ __launch_bounds__(256) void k_attn(const float* q, const bf16* K, const bf16* V, bf16* o) {
  __shared__ float sc[8][2048];
  __shared__ float qs[8][256];
  __shared__ float red[8];
  int h = blockIdx.y, r0 = blockIdx.x * 8, kv = h >> 1, t = threadIdx.x;
  for (int i = t; i < 8 * 256; i += 256) qs[i >> 8][i & 255] = rb(q[(long)(r0 + (i >> 8)) * 4096 + h * 256 + (i & 255)]);
  __syncthreads();
  for (int key = t; key < 2048; key += 256) {
    const bf16* kp = K + ((long)kv * 2048 + key) * 256;
    float a[8] = {};
    for (int d = 0; d < 256; d += 8) {
      uint4 raw = *(const uint4*)(kp + d); const bf16* kb = (const bf16*)&raw;
      for (int e = 0; e < 8; ++e) { float kf = b2f(kb[e]); for (int r = 0; r < 8; ++r) a[r] += qs[r][d + e] * kf; }
    }
    for (int r = 0; r < 8; ++r) {
      sc[r][key] = a[r] * 0.0625f;
    }
  }
  __syncthreads();
  int w = t >> 6, lane = t & 63;
  for (int r = w; r < 8; r += 4) {
    float mx = -1e30f; for (int k = lane; k < 2048; k += 64) mx = fmaxf(mx, sc[r][k]);
    for (int of = 32; of; of >>= 1) mx = fmaxf(mx, __shfl_xor(mx, of));
    float s = 0; for (int k = lane; k < 2048; k += 64) { float p = __expf(sc[r][k] - mx); s += p; sc[r][k] = p; }
    for (int of = 32; of; of >>= 1) s += __shfl_xor(s, of);
    for (int k = lane; k < 2048; k += 64) sc[r][k] = rb(sc[r][k]);
    if (lane == 0) red[r] = 1.f / s;
  }
  __syncthreads();
  float a[8] = {};
  const bf16* vp = V + (long)kv * 2048 * 256 + t;
  for (int key = 0; key < 2048; ++key) { float vf = b2f(vp[(long)key * 256]); for (int r = 0; r < 8; ++r) a[r] += sc[r][key] * vf; }
  for (int r = 0; r < 8; ++r) o[(long)(r0 + r) * 4096 + h * 256 + t] = f2b(a[r] * red[r]);
}
__global__ void k_res1(const bf16* x, const float* ao, const float* ga, float* x1) {
  int i = blockIdx.x * 256 + threadIdx.x; int c = i % D;
  x1[i] = b2f(x[i]) + ga[c] * ao[i];
}
__global__ void k_act(const float* g, const float* u, bf16* a) {
  int i = blockIdx.x * 256 + threadIdx.x;
  float gg = g[i]; float s = gg / (1.f + __expf(-gg)); a[i] = f2b(s * u[i]);
}
__global__ void k_res2(const float* x1, const float* d, const float* gb, bf16* out) {
  int i = blockIdx.x * 256 + threadIdx.x; int c = i % D;
  out[i] = f2b(x1[i] + gb[c] * d[i]);
}
extern "C" void kernel_launch(void* const* d_in, const int* in_sizes, int n_in, void* d_out, int out_size, void* d_ws, size_t ws_size, hipStream_t stream) {
  const bf16* x = (const bf16*)d_in[0]; const bf16* Kc = (const bf16*)d_in[1]; const bf16* Vc = (const bf16*)d_in[2];
  const bf16* wq = (const bf16*)d_in[3]; const bf16* wo = (const bf16*)d_in[4];
  const bf16* wg = (const bf16*)d_in[6]; const bf16* wu = (const bf16*)d_in[7]; const bf16* wd = (const bf16*)d_in[8];
  const bf16* c = (const bf16*)d_in[9]; const bf16* wmod = (const bf16*)d_in[10];
  char* ws = (char*)d_ws;
  float* mod = (float*)ws; ws += 16384;
  bf16* h = (bf16*)ws; ws += (size_t)S * D * 2;
  float* q = (float*)ws; ws += (size_t)S * 4096 * 4;
  bf16* o = (bf16*)ws; ws += (size_t)S * 4096 * 2;
  float* ao = (float*)ws; ws += (size_t)S * D * 4;
  float* x1 = (float*)ws; ws += (size_t)S * D * 4;
  float* g = (float*)ws; ws += (size_t)S * F * 4;
  float* u = (float*)ws; ws += (size_t)S * F * 4;
  bf16* a = (bf16*)ws; ws += (size_t)S * F * 2;
  k_mod<<<(6 * D + 255) / 256, 256, 0, stream>>>(c, wmod, mod);
  k_lnmod<<<S, 64, 0, stream>>>(nullptr, x, mod + 0 * D, mod + 1 * D, h);
  k_gemm<<<dim3(4096 / 64, S / 64), 256, 0, stream>>>(h, wq, q, S, 4096, D);
  k_attn<<<dim3(S / 8, HQ), 256, 0, stream>>>(q, Kc, Vc, o);
  k_gemm<<<dim3(D / 64, S / 64), 256, 0, stream>>>(o, wo, ao, S, D, 4096);
  k_res1<<<S * D / 256, 256, 0, stream>>>(x, ao, mod + 2 * D, x1);
  k_lnmod<<<S, 64, 0, stream>>>(x1, nullptr, mod + 3 * D, mod + 4 * D, h);
  k_gemm<<<dim3(F / 64, S / 64), 256, 0, stream>>>(h, wg, g, S, F, D);
  k_gemm<<<dim3(F / 64, S / 64), 256, 0, stream>>>(h, wu, u, S, F, D);
  k_act<<<S * F / 256, 256, 0, stream>>>(g, u, a);
  k_gemm<<<dim3(D / 64, S / 64), 256, 0, stream>>>(a, wd, ao, S, D, F);
  k_res2<<<S * D / 256, 256, 0, stream>>>(x1, ao, mod + 5 * D, (bf16*)d_out);
}
```

```cpp
#include <hip/hip_runtime.h>
#include <hip/hip_bf16.h>
#include <stdint.h>

typedef __hip_bfloat16 bf16;
typedef __attribute__((ext_vector_type(8))) short bf16x8;
typedef __attribute__((ext_vector_type(4))) short s16x4;
typedef __attribute__((ext_vector_type(16))) float f32x16;
typedef __attribute__((ext_vector_type(4))) float f32x4;
typedef __attribute__((ext_vector_type(2))) float f32x2;
typedef __attribute__((ext_vector_type(4))) unsigned u32x4;
typedef __attribute__((ext_vector_type(2))) __bf16 bf16x2v;
#define DI __device__ __forceinline__

constexpr int S = 2048, D = 448, HQ = 16, HKV = 8, DH = 256, F = 1792;
constexpr int NS = D / 16;
constexpr float CS = 1.4426950408889634f / 16.0f;
constexpr float THR = 64.0f;

DI float b2f(bf16 x) { return __bfloat162float(x); }
DI float rb(float x) { return __bfloat162float(__float2bfloat16(x)); }
DI unsigned cvtpk(float lo, float hi) { f32x2 v = {lo, hi}; bf16x2v b = __builtin_convertvector(v, bf16x2v); return __builtin_bit_cast(unsigned, b); }
DI float lo16(unsigned u) { return __uint_as_float(u << 16); }
DI float hi16(unsigned u) { return __uint_as_float(u & 0xffff0000u); }

constexpr size_t WS_MOD = 0;
constexpr size_t WS_HF = 16384;
constexpr size_t SZ_HF = (size_t)S * D * 2;
constexpr size_t WS_H2F = WS_HF + SZ_HF;
constexpr size_t WS_X1 = WS_H2F + SZ_HF;
constexpr size_t WS_YP = WS_X1 + SZ_HF * 2;
constexpr size_t WS_Y2P = WS_YP + SZ_HF * 16;
constexpr size_t WS_END = WS_Y2P + SZ_HF * 14;

__global__ __launch_bounds__(1024) void k_mod(const bf16* __restrict__ c, const bf16* __restrict__ wmod, float* __restrict__ mod) {
  __shared__ float red[128][64];
  __shared__ float sc[D];
  const int t = threadIdx.x, col0 = blockIdx.x * 64;
  if (t < D) { float cv = b2f(c[t]); sc[t] = rb(cv / (1.f + __expf(-cv))); }
  const int cc = t & 7, rr = t >> 3;
  u32x4 w[4];
#pragma unroll
  for (int i = 0; i < 4; ++i) { int row = rr + 128 * i; w[i] = (row < D) ? *(const u32x4*)(wmod + (size_t)row * (6 * D) + col0 + cc * 8) : u32x4{0, 0, 0, 0}; }
  __syncthreads();
  float a[8] = {};
#pragma unroll
  for (int i = 0; i < 4; ++i) { int row = rr + 128 * i; float s = row < D ? sc[row] : 0.f;
#pragma unroll
    for (int e = 0; e < 4; ++e) { a[2 * e] += s * lo16(w[i][e]); a[2 * e + 1] += s * hi16(w[i][e]); } }
#pragma unroll
  for (int e = 0; e < 8; ++e) red[rr][cc * 8 + e] = a[e];
  __syncthreads();
  { int col = t & 63, g = t >> 6; float s = 0;
#pragma unroll
    for (int i = 0; i < 8; ++i) s += red[g * 8 + i][col];
    __syncthreads();
    red[g][col] = s; }
  __syncthreads();
  if (t < 64) { float s = 0;
#pragma unroll
    for (int i = 0; i < 16; ++i) s += red[i][t];
    float m = rb(s); int j = col0 + t; int which = j / D, cidx = j % D;
    if (which == 0) mod[1 * D + cidx] = m * CS;
    else if (which == 1) mod[0 * D + cidx] = rb(1.f + m) * CS;
    else if (which == 2) mod[2 * D + cidx] = m;
    else if (which == 3) mod[4 * D + cidx] = m;
    else if (which == 4) mod[3 * D + cidx] = rb(1.f + m);
    else mod[5 * D + cidx] = m;
  }
}

template <bool SECOND>
__global__ __launch_bounds__(448) void k_ln(const bf16* __restrict__ x, const float* __restrict__ mod, const bf16* __restrict__ yp,
                                            float* __restrict__ x1, bf16* __restrict__ hf) {
  __shared__ float red[2][56][8];
  const int t = threadIdx.x, r8 = t & 7, hh = (t >> 3) & 1, s = t >> 4;
  const int row = blockIdx.x * 8 + r8, qb = row >> 5, r = row & 31, c0 = 16 * s + 8 * hh;
  const size_t fo = (((size_t)qb * NS + s) * 64 + hh * 32 + r) * 8;
  u32x4 xr = *(const u32x4*)(x + (size_t)row * D + c0);
  float v[8];
#pragma unroll
  for (int e = 0; e < 4; ++e) { v[2 * e] = lo16(xr[e]); v[2 * e + 1] = hi16(xr[e]); }
  if constexpr (SECOND) {
    u32x4 p[16];
#pragma unroll
    for (int h = 0; h < 16; ++h) p[h] = *(const u32x4*)(yp + (size_t)h * (S * D) + fo);
    float a[8] = {};
#pragma unroll
    for (int h = 0; h < 16; ++h)
#pragma unroll
      for (int e = 0; e < 4; ++e) { a[2 * e] += lo16(p[h][e]); a[2 * e + 1] += hi16(p[h][e]); }
    f32x4 g0 = *(const f32x4*)(mod + 2 * D + c0), g1 = *(const f32x4*)(mod + 2 * D + c0 + 4);
#pragma unroll
    for (int e = 0; e < 4; ++e) { v[e] += g0[e] * a[e]; v[4 + e] += g1[e] * a[4 + e]; }
    *(f32x4*)(x1 + fo) = f32x4{v[0], v[1], v[2], v[3]}; *(f32x4*)(x1 + fo + 4) = f32x4{v[4], v[5], v[6], v[7]};
  }
  float sum = 0;
#pragma unroll
  for (int e = 0; e < 8; ++e) sum += v[e];
  red[0][t >> 3][r8] = sum;
  __syncthreads();
  float tot = 0;
#pragma unroll
  for (int i = 0; i < 56; ++i) tot += red[0][i][r8];
  const float mu = tot * (1.f / D);
  float sq = 0;
#pragma unroll
  for (int e = 0; e < 8; ++e) { v[e] -= mu; sq += v[e] * v[e]; }
  red[1][t >> 3][r8] = sq;
  __syncthreads();
  float tq = 0;
#pragma unroll
  for (int i = 0; i < 56; ++i) tq += red[1][i][r8];
  const float rs = rsqrtf(tq * (1.f / D) + 1e-5f);
  const float* A = mod + (SECOND ? 3 : 0) * D + c0; const float* B = mod + (SECOND ? 4 : 1) * D + c0;
  f32x4 a0 = *(const f32x4*)A, a1 = *(const f32x4*)(A + 4), b0 = *(const f32x4*)B, b1 = *(const f32x4*)(B + 4);
  u32x4 o;
  o[0] = cvtpk(v[0] * rs * a0[0] + b0[0], v[1] * rs * a0[1] + b0[1]); o[1] = cvtpk(v[2] * rs * a0[2] + b0[2], v[3] * rs * a0[3] + b0[3]);
  o[2] = cvtpk(v[4] * rs * a1[0] + b1[0], v[5] * rs * a1[1] + b1[1]); o[3] = cvtpk(v[6] * rs * a1[2] + b1[2], v[7] * rs * a1[3] + b1[3]);
  *(u32x4*)(hf + fo) = o;
}

__global__ __launch_bounds__(448) void k_out(const float* __restrict__ mod, const bf16* __restrict__ y2p, const float* __restrict__ x1, bf16* __restrict__ out) {
  const int t = threadIdx.x, r8 = t & 7, hh = (t >> 3) & 1, s = t >> 4;
  const int row = blockIdx.x * 8 + r8, qb = row >> 5, r = row & 31, c0 = 16 * s + 8 * hh;
  const size_t fo = (((size_t)qb * NS + s) * 64 + hh * 32 + r) * 8;
  u32x4 p[14];
#pragma unroll
  for (int h = 0; h < 14; ++h) p[h] = *(const u32x4*)(y2p + (size_t)h * (S * D) + fo);
  f32x4 x0 = *(const f32x4*)(x1 + fo), xx1 = *(const f32x4*)(x1 + fo + 4);
  f32x4 g0 = *(const f32x4*)(mod + 5 * D + c0), g1 = *(const f32x4*)(mod + 5 * D + c0 + 4);
  float a[8] = {};
#pragma unroll
  for (int h = 0; h < 14; ++h)
#pragma unroll
    for (int e = 0; e < 4; ++e) { a[2 * e] += lo16(p[h][e]); a[2 * e + 1] += hi16(p[h][e]); }
  u32x4 o;
  o[0] = cvtpk(x0[0] + g0[0] * a[0], x0[1] + g0[1] * a[1]); o[1] = cvtpk(x0[2] + g0[2] * a[2], x0[3] + g0[3] * a[3]);
  o[2] = cvtpk(xx1[0] + g1[0] * a[4], xx1[1] + g1[1] * a[5]); o[3] = cvtpk(xx1[2] + g1[2] * a[6], xx1[3] + g1[3] * a[7]);
  *(u32x4*)(out + (size_t)row * D + c0) = o;
}

typedef __attribute__((address_space(3))) void* ldsp;
typedef __attribute__((address_space(1))) const void* gp;
DI void glds16(const void* g, unsigned ldsoff, char* lds) {
  __builtin_amdgcn_global_load_lds((gp)g, (ldsp)(lds + ldsoff), 16, 0, 0);
}
DI s16x4 trread(unsigned addr) { s16x4 r; asm volatile("ds_read_b64_tr_b16 %0, %1\n\ts_waitcnt lgkmcnt(0)" : "=&v"(r) : "v"(addr) : "memory"); return r; }
DI bf16x8 cat(s16x4 a, s16x4 b) { return bf16x8{a[0], a[1], a[2], a[3], b[0], b[1], b[2], b[3]}; }
DI bf16x8 packfrag(const f32x16& a, int s, float sc) {
  u32x4 w; w[0] = cvtpk(a[8 * s + 0] * sc, a[8 * s + 1] * sc); w[1] = cvtpk(a[8 * s + 2] * sc, a[8 * s + 3] * sc);
  w[2] = cvtpk(a[8 * s + 4] * sc, a[8 * s + 5] * sc); w[3] = cvtpk(a[8 * s + 6] * sc, a[8 * s + 7] * sc);
  return __builtin_bit_cast(bf16x8, w);
}
#define MFMA(a, b, c) __builtin_amdgcn_mfma_f32_32x32x16_bf16(a, b, c, 0, 0, 0)

template <int KS>
DI void proj_out(const bf16* __restrict__ W, const bf16x8* bfr, bf16* __restrict__ yout  , char* lds, unsigned ldsbase, int w, int lane) {
  constexpr int KROWS = KS * 16;
  const int hh = lane >> 5, g1 = (lane >> 4) & 1, q = (lane >> 2) & 3, p = lane & 3, pp = 2 * (p & 1) + (p >> 1);
  for (int nt = 0; nt < 7; ++nt) {
    __syncthreads();
    for (int pc = w; pc < KROWS / 8; pc += 4) {
      int row = pc * 8 + (lane >> 3), cpos = lane & 7, c = cpos ^ (((row >> 1) & 1) << 2);
      glds16(W + (size_t)row * D + nt * 64 + c * 8, ldsbase + pc * 1024, lds);
    }
    asm volatile("s_waitcnt vmcnt(0)" ::: "memory");
    __syncthreads();
    f32x16 acc[2] = {};
#pragma unroll
    for (int ks = 0; ks < KS; ++ks)
#pragma unroll
      for (int nbl = 0; nbl < 2; ++nbl) {
        unsigned a0 = (unsigned)(uintptr_t)lds + ldsbase + (16 * ks + 4 * hh + q) * 128 + ((nbl ^ (q >> 1)) * 64) + g1 * 32 + pp * 8;
        bf16x8 A = cat(trread(a0), trread(a0 + 8 * 128));
        acc[nbl] = MFMA(A, bfr[ks], acc[nbl]);
      }
#pragma unroll
    for (int nbl = 0; nbl < 2; ++nbl)
#pragma unroll
      for (int s = 0; s < 2; ++s) *(bf16x8*)(yout + ((size_t)(nt * 4 + nbl * 2 + s) * 64 + lane) * 8) = packfrag(acc[nbl], s, 1.f);
  }
}

template <bool PI, typename SRC>
DI void proj_in(SRC src, const bf16* __restrict__ hf  , f32x16* acc, char* lds, int w, int lane) {
  const int hh = lane >> 5, g1 = (lane >> 4) & 1, q = (lane >> 2) & 3, p = lane & 3, pp = PI ? 2 * (p & 1) + (p >> 1) : p;
  for (int u = 0; u < 14; ++u) {
    __syncthreads();
#pragma unroll
    for (int i = 0; i < 4; ++i) { int pc = 4 * w + i, row = 2 * pc + hh, cpos = lane & 31, c = cpos ^ ((row & 3) << 2);
      glds16(src(u * 32 + row, c), pc * 1024, lds); }
    bf16x8 b0 = *(const bf16x8*)(hf + ((size_t)(2 * u) * 64 + lane) * 8), b1 = *(const bf16x8*)(hf + ((size_t)(2 * u + 1) * 64 + lane) * 8);
    asm volatile("s_waitcnt vmcnt(0)" ::: "memory");
    __syncthreads();
#pragma unroll
    for (int s = 0; s < 2; ++s)
#pragma unroll
      for (int db = 0; db < 8; ++db) {
        unsigned a0 = (unsigned)(uintptr_t)lds + (16 * s + 8 * hh + q) * 512 + ((db ^ q) * 64) + g1 * 32 + pp * 8;
        bf16x8 A = cat(trread(a0), trread(a0 + 4 * 512));
        acc[db] = MFMA(A, s ? b1 : b0, acc[db]);
      }
  }
}

__global__ __launch_bounds__(256, 1) void k_attn(const bf16* __restrict__ hf, const bf16* __restrict__ wq, const bf16* __restrict__ Kc, const bf16* __restrict__ Vc,
                                                 const bf16* __restrict__ wo, bf16* __restrict__ yp) {
  extern __shared__ __attribute__((aligned(16))) char lds[];
  const int b = blockIdx.x, kvh = b & 7, j = b >> 3, head = 2 * kvh + (j & 1), qblk = j >> 1;
  const int w = __builtin_amdgcn_readfirstlane(threadIdx.x >> 6), lane = threadIdx.x & 63, qb = qblk * 4 + w;
  const int r = lane & 31, hh = lane >> 5, g1 = (lane >> 4) & 1, q = (lane >> 2) & 3, p = lane & 3;
  f32x16 acc[8] = {};
  proj_in<true>([&](int row, int c) { return wq + (size_t)row * 4096 + head * 256 + c * 8; }, hf + (size_t)qb * NS * 512, acc, lds, w, lane);
  bf16x8 qf[16];
#pragma unroll
  for (int db = 0; db < 8; ++db) { qf[2 * db] = packfrag(acc[db], 0, 1.f); qf[2 * db + 1] = packfrag(acc[db], 1, 1.f); }
#pragma unroll
  for (int db = 0; db < 8; ++db) acc[db] = f32x16{};
  float mref = 0.f, lsum = 0.f;
  const bf16* Kh = Kc + (size_t)kvh * S * DH; const bf16* Vh = Vc + (size_t)kvh * S * DH;
  for (int u = 0; u < 64; ++u) {
    __syncthreads();
#pragma unroll
    for (int i = 0; i < 4; ++i) { int pc = 4 * w + i, row = 2 * pc + hh, cpos = lane & 31;
      glds16(Kh + (size_t)(u * 32 + row) * DH + (cpos ^ (row & 15)) * 8, pc * 1024, lds);
      glds16(Vh + (size_t)(u * 32 + row) * DH + (cpos ^ ((row & 3) << 2)) * 8, 16384 + pc * 1024, lds); }
    asm volatile("s_waitcnt vmcnt(0)" ::: "memory");
    __syncthreads();
    f32x16 s = {};
#pragma unroll
    for (int ks = 0; ks < 16; ++ks) {
      bf16x8 kf = *(const bf16x8*)(lds + r * 512 + (((2 * ks + hh) ^ (r & 15)) * 16));
      s = MFMA(kf, qf[ks], s);
    }
    float mx = s[0];
#pragma unroll
    for (int i = 1; i < 16; ++i) mx = fmaxf(mx, s[i]);
    mx -= mref;
    if (u == 0 || __any(mx > THR)) {
      mx = fmaxf(mx, __shfl_xor(mx, 32));
      float dl = u == 0 ? mx : fmaxf(mx, 0.f);
      float f = u == 0 ? 1.f : __builtin_amdgcn_exp2f(-dl);
      mref += dl; lsum *= f;
#pragma unroll
      for (int db = 0; db < 8; ++db)
#pragma unroll
        for (int i = 0; i < 16; ++i) acc[db][i] *= f;
    }
#pragma unroll
    for (int i = 0; i < 16; ++i) { s[i] = __builtin_amdgcn_exp2f(s[i] - mref); lsum += s[i]; }
    bf16x8 pf0 = packfrag(s, 0, 1.f), pf1 = packfrag(s, 1, 1.f);
#pragma unroll
    for (int ks = 0; ks < 2; ++ks)
#pragma unroll
      for (int db = 0; db < 8; ++db) {
        unsigned a0 = (unsigned)(uintptr_t)lds + 16384 + (16 * ks + 4 * hh + q) * 512 + ((db ^ q) * 64) + g1 * 32 + p * 8;
        bf16x8 A = cat(trread(a0), trread(a0 + 8 * 512));
        acc[db] = MFMA(A, ks ? pf1 : pf0, acc[db]);
      }
  }
  lsum += __shfl_xor(lsum, 32);
  const float inv = 1.f / lsum;
#pragma unroll
  for (int db = 0; db < 8; ++db) { qf[2 * db] = packfrag(acc[db], 0, inv); qf[2 * db + 1] = packfrag(acc[db], 1, inv); }
  proj_out<16>(wo + (size_t)head * 256 * D, qf, yp + (size_t)head * (S * D) + (size_t)qb * NS * 512, lds, 0, w, lane);
}

__global__ __launch_bounds__(256, 1) void k_ffn(const bf16* __restrict__ h2f, const bf16* __restrict__ wg, const bf16* __restrict__ wu, const bf16* __restrict__ wd,
                                                bf16* __restrict__ y2p) {
  extern __shared__ __attribute__((aligned(16))) char lds[];
  const int b = blockIdx.x, id = (b & 7) * 28 + (b >> 3), fc = id >> 4, qblk = id & 15;
  const int w = __builtin_amdgcn_readfirstlane(threadIdx.x >> 6), lane = threadIdx.x & 63, qb = qblk * 4 + w;
  f32x16 acc[8] = {};
  proj_in<false>([&](int row, int c) { return (c < 16 ? wg : wu) + (size_t)row * F + fc * 128 + (c & 15) * 8; }, h2f + (size_t)qb * NS * 512, acc, lds, w, lane);
  bf16x8 af[8];
#pragma unroll
  for (int fb = 0; fb < 4; ++fb) {
    f32x16 a;
#pragma unroll
    for (int i = 0; i < 16; ++i) { float g = acc[fb][i]; a[i] = g / (1.f + __expf(-g)) * acc[fb + 4][i]; }
    af[2 * fb] = packfrag(a, 0, 1.f); af[2 * fb + 1] = packfrag(a, 1, 1.f);
  }
  proj_out<8>(wd + (size_t)fc * 128 * D, af, y2p + (size_t)fc * (S * D) + (size_t)qb * NS * 512, lds, 0, w, lane);
}

extern "C" void kernel_launch(void* const* d_in, const int* in_sizes, int n_in, void* d_out, int out_size, void* d_ws, size_t ws_size, hipStream_t stream) {
  const bf16* x = (const bf16*)d_in[0]; const bf16* Kc = (const bf16*)d_in[1]; const bf16* Vc = (const bf16*)d_in[2];
  const bf16* wq = (const bf16*)d_in[3]; const bf16* wo = (const bf16*)d_in[4];
  const bf16* wg = (const bf16*)d_in[6]; const bf16* wu = (const bf16*)d_in[7]; const bf16* wd = (const bf16*)d_in[8];
  const bf16* c = (const bf16*)d_in[9]; const bf16* wmod = (const bf16*)d_in[10];
  char* ws = (char*)d_ws;
  float* mod = (float*)(ws + WS_MOD); bf16* hf = (bf16*)(ws + WS_HF); bf16* h2f = (bf16*)(ws + WS_H2F); float* x1 = (float*)(ws + WS_X1);
  bf16* yp = (bf16*)(ws + WS_YP); bf16* y2p = (bf16*)(ws + WS_Y2P);
  k_mod<<<42, 1024, 0, stream>>>(c, wmod, mod);
  k_ln<false><<<256, 448, 0, stream>>>(x, mod, nullptr, nullptr, hf);
  k_attn<<<256, 256, 65536, stream>>>(hf, wq, Kc, Vc, wo, yp);
  k_ln<true><<<256, 448, 0, stream>>>(x, mod, yp, x1, h2f);
  k_ffn<<<224, 256, 65536, stream>>>(h2f, wg, wu, wd, y2p);
  k_out<<<256, 448, 0, stream>>>(mod, y2p, x1, (bf16*)d_out);
}
```

```cpp
#define FST " sc1"
#define REL2 1
#define FUSE_LN 2
#include <hip/hip_runtime.h>
#include <hip/hip_bf16.h>
#include <stdint.h>

typedef __hip_bfloat16 bf16;
typedef __attribute__((ext_vector_type(8))) short bf16x8;
typedef __attribute__((ext_vector_type(4))) short s16x4;
typedef __attribute__((ext_vector_type(16))) float f32x16;
typedef __attribute__((ext_vector_type(4))) float f32x4;
typedef __attribute__((ext_vector_type(2))) float f32x2;
typedef __attribute__((ext_vector_type(4))) unsigned u32x4;
typedef __attribute__((ext_vector_type(2))) __bf16 bf16x2v;
#define DI __device__ __forceinline__

constexpr int S = 2048, D = 448, HQ = 16, HKV = 8, DH = 256, F = 1792;
constexpr int NS = D / 16;
constexpr float CS = 1.4426950408889634f / 16.0f;
constexpr float THR = 64.0f;

DI float b2f(bf16 x) { return __bfloat162float(x); }
DI float rb(float x) { return __bfloat162float(__float2bfloat16(x)); }
DI unsigned cvtpk(float lo, float hi) { f32x2 v = {lo, hi}; bf16x2v b = __builtin_convertvector(v, bf16x2v); return __builtin_bit_cast(unsigned, b); }
DI float lo16(unsigned u) { return __uint_as_float(u << 16); }
DI float hi16(unsigned u) { return __uint_as_float(u & 0xffff0000u); }

constexpr size_t WS_MOD = 0;
constexpr size_t WS_AB = 12288;
constexpr size_t WS_HF = 16384;
constexpr size_t SZ_HF = (size_t)S * D * 2;
constexpr size_t WS_H2F = WS_HF + SZ_HF;
constexpr size_t WS_X1 = WS_H2F + SZ_HF;
constexpr size_t WS_YP = WS_X1 + SZ_HF * 2;
constexpr size_t WS_ACT = WS_YP + SZ_HF * 16;
constexpr size_t WS_WGU = WS_ACT + SZ_HF * 4;
constexpr size_t WS_WD = WS_WGU + (size_t)D * F * 4;
constexpr size_t WS_SYNC = WS_WD + (size_t)D * F * 2;
constexpr size_t WS_END = WS_SYNC + 112 * 256;

DI void tr8(const u32x4* in, u32x4* o) {
#pragma unroll
  for (int e = 0; e < 4; ++e)
#pragma unroll
    for (int kk = 0; kk < 4; ++kk) {
      o[2 * e][kk] = __builtin_amdgcn_perm(in[2 * kk + 1][e], in[2 * kk][e], 0x05040100u);
      o[2 * e + 1][kk] = __builtin_amdgcn_perm(in[2 * kk + 1][e], in[2 * kk][e], 0x07060302u);
    }
}
DI void st16(void* p, u32x4 v) { asm volatile("global_store_dwordx4 %0, %1, off " FST "\n s_nop 2" :: "v"(p), "v"(v) : "memory"); }
constexpr int NREL = 154 / REL2;
constexpr int NMODB = 84;
constexpr int NLNB = 64, G0 = 2;
__global__ __launch_bounds__(256) void k_front(const bf16* __restrict__ c, const bf16* __restrict__ wmod, float* __restrict__ mod,
                                               const bf16* __restrict__ wg, const bf16* __restrict__ wu, const bf16* __restrict__ wd,
                                               bf16* __restrict__ wgu, bf16* __restrict__ wdf, const bf16* __restrict__ x, bf16* __restrict__ hf, unsigned* __restrict__ sync
                                               ) {
  __shared__ float red[64][33];
  __shared__ u32x4 stage[4 * 648];
  const int t = threadIdx.x;
  if (blockIdx.x == NMODB && t < 112) sync[t * 64] = 0u;
  if (blockIdx.x >= NMODB && blockIdx.x < NMODB + NLNB) {
    const int qb = blockIdx.x - NMODB, lane = t & 63, r = (t >> 6) * 8 + (lane & 7), j = lane >> 3;
    const bf16* xr = x + (size_t)(qb * 32 + r) * D + j * 8;
    u32x4 u[7];
#pragma unroll
    for (int i = 0; i < 7; ++i) u[i] = *(const u32x4*)(xr + i * 64);
    float v[7][8]; float sum = 0;
#pragma unroll
    for (int i = 0; i < 7; ++i)
#pragma unroll
      for (int e = 0; e < 4; ++e) { v[i][2 * e] = lo16(u[i][e]); v[i][2 * e + 1] = hi16(u[i][e]); sum += v[i][2 * e] + v[i][2 * e + 1]; }
    sum += __shfl_xor(sum, 8); sum += __shfl_xor(sum, 16); sum += __shfl_xor(sum, 32);
    const float mu = sum * (1.f / D);
    float sq = 0;
#pragma unroll
    for (int i = 0; i < 7; ++i)
#pragma unroll
      for (int e = 0; e < 8; ++e) { v[i][e] -= mu; sq += v[i][e] * v[i][e]; }
    sq += __shfl_xor(sq, 8); sq += __shfl_xor(sq, 16); sq += __shfl_xor(sq, 32);
    const float rs = rsqrtf(sq * (1.f / D) + 1e-5f);
    bf16* op = hf + ((size_t)qb * NS * 64 + j * 32 + r) * 8;
#pragma unroll
    for (int i = 0; i < 7; ++i) { u32x4 o;
#pragma unroll
      for (int e = 0; e < 4; ++e) o[e] = cvtpk(v[i][2 * e] * rs, v[i][2 * e + 1] * rs);
      st16(op + i * 2048, o); }
    return;
  }
  if (blockIdx.x >= NMODB) {
    const int wv0 = __builtin_amdgcn_readfirstlane(((blockIdx.x - NMODB - NLNB) * 4 + (t >> 6)) * REL2), lane = t & 63, hh = lane >> 5, i5 = lane & 31;
    const bool gu = wv0 < 392;
    const int stride = gu ? F : D;
    u32x4 in[REL2][8], o[8];
#pragma unroll
    for (int u = 0; u < REL2; ++u) {
      const int wv = wv0 + u, which = wv / 196, rem = gu ? wv % 196 : wv - 392, ks = gu ? rem / 7 : rem >> 1, g = gu ? rem % 7 : rem & 1;
      const bf16* src = (gu ? (which ? wu : wg) : wd) + (size_t)(2 * ks + hh) * 8 * stride + (gu ? 32 * g + i5 : 28 * g + (i5 < 28 ? i5 : 27)) * 8;
#pragma unroll
      for (int i = 0; i < 8; ++i) in[u][i] = *(const u32x4*)(src + (size_t)i * stride);
    }
    u32x4* sl = stage + (t >> 6) * 648;
    const int a = i5 & 3, mb = gu ? (a & 1) * 4 + (a >> 1) * 18 : (a >> 1) * 4 + (a & 1) * 18;
#pragma unroll
    for (int u = 0; u < REL2; ++u) {
      const int wv = wv0 + u, which = wv / 196, rem = gu ? wv % 196 : wv - 392, ks = gu ? rem / 7 : rem >> 1, g = gu ? rem % 7 : rem & 1;
      tr8(in[u], o);
      __builtin_amdgcn_wave_barrier();
#pragma unroll
      for (int e = 0; e < 8; ++e) sl[(i5 >> 2) * 81 + hh * 40 + mb + (e >> 2) * 8 + (e & 3)] = o[e];
      __builtin_amdgcn_wave_barrier();
#pragma unroll
      for (int q = 0; q < 8; ++q) o[q] = sl[q * 81 + hh * 40 + i5 + (i5 >> 4) * 2];
#pragma unroll
      for (int q = 0; q < 8; ++q) {
        const int fblk = 8 * g + q, fb = fblk & 3, sp = fb < G0 ? (ks * G0 + fb) * 2 + which : fb * 56 + ks * 2 + which;
        bf16* dst = gu ? wgu + ((size_t)(fblk >> 2) * 224 + sp) * 512 : wdf + ((size_t)(7 * g + q) * 112 + ks) * 512;
        if (gu || q < 7) st16(dst + lane * 8, o[q]);
      }
    }
    return;
    return;
  }
  const int col0 = blockIdx.x * 32, cc = t & 3, rr = t >> 2;
  u32x4 w[7]; float sv[7];
#pragma unroll
  for (int i = 0; i < 7; ++i) w[i] = *(const u32x4*)(wmod + (size_t)(rr + 64 * i) * (6 * D) + col0 + cc * 8);
#pragma unroll
  for (int i = 0; i < 7; ++i) { const float cv = b2f(c[rr + 64 * i]); sv[i] = rb(cv / (1.f + __expf(-cv))); }
  float a[8] = {};
#pragma unroll
  for (int i = 0; i < 7; ++i)
#pragma unroll
    for (int e = 0; e < 4; ++e) { a[2 * e] += sv[i] * lo16(w[i][e]); a[2 * e + 1] += sv[i] * hi16(w[i][e]); }
#pragma unroll
  for (int e = 0; e < 8; ++e) red[rr][cc * 8 + e] = a[e];
  __syncthreads();
  const int col = t & 31, g = t >> 5;
  float sacc = 0;
#pragma unroll
  for (int i = 0; i < 8; ++i) sacc += red[g * 8 + i][col];
  __syncthreads();
  red[g][col] = sacc;
  __syncthreads();
  if (t < 32) { float s = 0;
#pragma unroll
    for (int i = 0; i < 8; ++i) s += red[i][t];
    float m = rb(s); int j = col0 + t; int which = j / D, cidx = j % D;
    float* ab = mod + WS_AB / 4 + (cidx >> 2) * 8 + (cidx & 3);
    if (which == 0) ab[4] = m * CS;
    else if (which == 1) ab[0] = rb(1.f + m) * CS;
    else if (which == 2) mod[2 * D + cidx] = m;
    else if (which == 3) mod[4 * D + cidx] = m;
    else if (which == 4) mod[3 * D + cidx] = rb(1.f + m);
    else mod[5 * D + cidx] = m;
  }
}

__global__ __launch_bounds__(256, 1) void k_attn(const bf16* __restrict__ hf, const bf16* __restrict__ wq, const bf16* __restrict__ Kc, const bf16* __restrict__ Vc,
                                                 const bf16* __restrict__ wo, bf16* yp, const float* __restrict__ ab, const bf16* x_, char* ws_, bf16* out_) {
  __shared__ __attribute__((aligned(1024))) char lds[131072 + 4096];
  const int b = blockIdx.x, kvh = b & 7, j = b >> 3, head = 2 * kvh + (j & 1), qblk = j >> 1;
  const int w = __builtin_amdgcn_readfirstlane(threadIdx.x >> 6), qb = qblk * 4 + w;
  const bf16* hfp = hf + (size_t)qb * NS * 512;
  const bf16* wqp = wq + head * 256;
  const bf16* kp = Kc + (size_t)kvh * S * DH;
  const bf16* vp = Vc + (size_t)kvh * S * DH;
  const bf16* wop = wo + (size_t)head * 256 * D;
  bf16* ypp = yp + (size_t)head * (S * D) + (size_t)qb * NS * 512;
  const unsigned lb = (unsigned)(uintptr_t)lds;
  const unsigned wqn = 448u * 8192u - (unsigned)head * 512u;
  asm volatile(
    "v_mbcnt_lo_u32_b32 v0, -1, 0\n"
    "v_mbcnt_hi_u32_b32 v0, -1, v0\n"
    "s_mov_b64 s[56:57], %[ab]\n"
    "s_mov_b32 s58, 0x1000\n"
    "s_mov_b32 s59, 0x20000\n"
    "s_mov_b64 s[36:37], %[hf]\n"
    "s_mov_b32 s38, 0x7000\n"
    "s_mov_b32 s39, 0x20000\n"
    "s_mov_b64 s[40:41], %[wq]\n"
    "s_mov_b32 s42, %[wqn]\n"
    "s_mov_b32 s43, 0x20000\n"
    "v_lshlrev_b32 v1, 4, v0\n"
    "s_lshl_b32 s70, %[w], 10\n"
    "s_add_u32 s72, s70, %[lb]\n"
    "s_add_u32 m0, s72, 0x20000\n"
    "s_lshl_b32 s60, %[w], 12\n"
    "buffer_load_dwordx4 v1, s[56:59], s70 offen offset:0 lds\n"
    "s_add_u32 s60, s60, %[lb]\n"
    "s_add_u32 s61, s60, 0x4000\n"
    "s_add_u32 s62, s60, 0x8000\n"
    "s_add_u32 s63, s60, 0xc000\n"
    "s_add_u32 s64, s60, 0x10000\n"
    "s_add_u32 s65, s60, 0x14000\n"
    "s_add_u32 s66, s60, 0x18000\n"
    "s_add_u32 s67, s60, 0x1c000\n"
    "v_lshrrev_b32 v2, 5, v0\n"
    "v_and_b32 v3, 31, v0\n"
    "v_lshlrev_b32 v4, 2, v2\n"
    "v_xor_b32 v4, v4, v3\n"
    "v_xor_b32 v5, 8, v4\n"
    "v_lshlrev_b32 v4, 4, v4\n"
    "v_lshlrev_b32 v5, 4, v5\n"
    "s_lshl_b32 s70, %[w], 16\n"
    "v_lshl_add_u32 v6, v2, 13, s70\n"
    "v_add_u32 v28, v6, v4\n"
    "v_add_u32 v29, v6, v5\n"
    "v_add_u32 v29, 0x3c00, v29\n"
    "v_add_u32 v30, v6, v4\n"
    "v_add_u32 v30, 0x7800, v30\n"
    "v_add_u32 v31, v6, v5\n"
    "v_add_u32 v31, 0xb400, v31\n"
    "s_mov_b32 s73, 0\n"
    "buffer_load_dwordx4 v[32:35], v1, s[36:39], s73 offen offset:0\n"
    "s_mov_b32 s73, 0\n"
    "buffer_load_dwordx4 v[36:39], v1, s[36:39], s73 offen offset:1024\n"
    "s_mov_b32 m0, s60\n"
    "s_mov_b32 s70, 0\n"
    "buffer_load_dwordx4 v28, s[40:43], s70 offen offset:0 lds\n"
    "buffer_load_dwordx4 v29, s[40:43], s70 offen offset:1024 lds\n"
    "buffer_load_dwordx4 v30, s[40:43], s70 offen offset:2048 lds\n"
    "buffer_load_dwordx4 v31, s[40:43], s70 offen offset:3072 lds\n"
    "s_mov_b32 s73, 0\n"
    "buffer_load_dwordx4 v[40:43], v1, s[36:39], s73 offen offset:2048\n"
    "s_mov_b32 s73, 0\n"
    "buffer_load_dwordx4 v[44:47], v1, s[36:39], s73 offen offset:3072\n"
    "s_mov_b32 m0, s64\n"
    "s_mov_b32 s70, 0x40000\n"
    "buffer_load_dwordx4 v28, s[40:43], s70 offen offset:0 lds\n"
    "buffer_load_dwordx4 v29, s[40:43], s70 offen offset:1024 lds\n"
    "buffer_load_dwordx4 v30, s[40:43], s70 offen offset:2048 lds\n"
    "buffer_load_dwordx4 v31, s[40:43], s70 offen offset:3072 lds\n"
    "s_mov_b32 m0, s61\n"
    "s_mov_b32 s70, 0x80000\n"
    "buffer_load_dwordx4 v28, s[40:43], s70 offen offset:0 lds\n"
    "buffer_load_dwordx4 v29, s[40:43], s70 offen offset:1024 lds\n"
    "buffer_load_dwordx4 v30, s[40:43], s70 offen offset:2048 lds\n"
    "buffer_load_dwordx4 v31, s[40:43], s70 offen offset:3072 lds\n"
    "v_bfe_u32 v4, v0, 2, 2\n"
    "v_bfe_u32 v5, v0, 4, 1\n"
    "v_and_b32 v6, 1, v0\n"
    "v_bfe_u32 v7, v0, 1, 1\n"
    "v_lshlrev_b32 v86, 5, v5\n"
    "v_lshl_add_u32 v86, v6, 4, v86\n"
    "v_lshl_add_u32 v86, v7, 3, v86\n"
    "v_lshlrev_b32 v6, 5, v6\n"
    "v_lshl_add_u32 v6, v5, 4, v6\n"
    "v_lshl_add_u32 v6, v7, 3, v6\n"
    "v_add_u32 v6, %[lb], v6\n"
    "v_lshl_add_u32 v5, v2, 3, v4\n"
    "v_lshl_add_u32 v6, v5, 9, v6\n"
    "v_xor_b32 v7, 0, v4\n"
    "v_lshl_add_u32 v88, v7, 6, v6\n"
    "v_add_u32 v92, 0x10000, v88\n"
    "v_xor_b32 v7, 1, v4\n"
    "v_lshl_add_u32 v89, v7, 6, v6\n"
    "v_add_u32 v93, 0x10000, v89\n"
    "v_xor_b32 v7, 2, v4\n"
    "v_lshl_add_u32 v90, v7, 6, v6\n"
    "v_add_u32 v94, 0x10000, v90\n"
    "v_xor_b32 v7, 3, v4\n"
    "v_lshl_add_u32 v91, v7, 6, v6\n"
    "v_add_u32 v95, 0x10000, v91\n"
    "v_lshlrev_b32 v7, 6, v2\n"
    "v_add_u32 v7, 0x20000, v7\n"
    "v_add_u32 v85, %[lb], v7\n"
    "s_waitcnt vmcnt(10) lgkmcnt(0)\n"
    "s_barrier\n"
    "ds_read_b128 v[112:115], v85 offset:0\n"
    "ds_read_b128 v[116:119], v85 offset:16\n"
    "ds_read_b128 v[120:123], v85 offset:32\n"
    "ds_read_b128 v[124:127], v85 offset:48\n"
    "s_mov_b32 s73, 0x1000\n"
    "buffer_load_dwordx4 v[48:51], v1, s[36:39], s73 offen offset:0\n"
    "ds_read_b64_tr_b16 a[64:65], v88 offset:0\n"
    "ds_read_b64_tr_b16 a[66:67], v88 offset:2048\n"
    "ds_read_b64_tr_b16 a[68:69], v89 offset:0\n"
    "ds_read_b64_tr_b16 a[70:71], v89 offset:2048\n"
    "s_mov_b32 s73, 0x1000\n"
    "buffer_load_dwordx4 v[52:55], v1, s[36:39], s73 offen offset:1024\n"
    "ds_read_b64_tr_b16 a[72:73], v90 offset:0\n"
    "ds_read_b64_tr_b16 a[74:75], v90 offset:2048\n"
    "ds_read_b64_tr_b16 a[76:77], v91 offset:0\n"
    "ds_read_b64_tr_b16 a[78:79], v91 offset:2048\n"
    "s_mov_b32 m0, s65\n"
    "s_mov_b32 s70, 0xc0000\n"
    "buffer_load_dwordx4 v28, s[40:43], s70 offen offset:0 lds\n"
    "ds_read_b64_tr_b16 a[80:81], v88 offset:256\n"
    "ds_read_b64_tr_b16 a[82:83], v88 offset:2304\n"
    "ds_read_b64_tr_b16 a[84:85], v89 offset:256\n"
    "ds_read_b64_tr_b16 a[86:87], v89 offset:2304\n"
    "buffer_load_dwordx4 v29, s[40:43], s70 offen offset:1024 lds\n"
    "ds_read_b64_tr_b16 a[88:89], v90 offset:256\n"
    "ds_read_b64_tr_b16 a[90:91], v90 offset:2304\n"
    "ds_read_b64_tr_b16 a[92:93], v91 offset:256\n"
    "ds_read_b64_tr_b16 a[94:95], v91 offset:2304\n"
    "buffer_load_dwordx4 v30, s[40:43], s70 offen offset:2048 lds\n"
    "ds_read_b64_tr_b16 a[96:97], v88 offset:8192\n"
    "ds_read_b64_tr_b16 a[98:99], v88 offset:10240\n"
    "ds_read_b64_tr_b16 a[100:101], v89 offset:8192\n"
    "ds_read_b64_tr_b16 a[102:103], v89 offset:10240\n"
    "buffer_load_dwordx4 v31, s[40:43], s70 offen offset:3072 lds\n"
    "ds_read_b64_tr_b16 a[104:105], v90 offset:8192\n"
    "ds_read_b64_tr_b16 a[106:107], v90 offset:10240\n"
    "ds_read_b64_tr_b16 a[108:109], v91 offset:8192\n"
    "ds_read_b64_tr_b16 a[110:111], v91 offset:10240\n"
    "s_mov_b32 s73, 0x1000\n"
    "buffer_load_dwordx4 v[56:59], v1, s[36:39], s73 offen offset:2048\n"
    "ds_read_b64_tr_b16 a[112:113], v88 offset:8448\n"
    "ds_read_b64_tr_b16 a[114:115], v88 offset:10496\n"
    "ds_read_b64_tr_b16 a[116:117], v89 offset:8448\n"
    "ds_read_b64_tr_b16 a[118:119], v89 offset:10496\n"
    "s_mov_b32 s73, 0x1000\n"
    "buffer_load_dwordx4 v[60:63], v1, s[36:39], s73 offen offset:3072\n"
    "ds_read_b64_tr_b16 a[120:121], v90 offset:8448\n"
    "ds_read_b64_tr_b16 a[122:123], v90 offset:10496\n"
    "ds_read_b64_tr_b16 a[124:125], v91 offset:8448\n"
    "ds_read_b64_tr_b16 a[126:127], v91 offset:10496\n"
    "s_mov_b32 m0, s62\n"
    "s_mov_b32 s70, 0x100000\n"
    "buffer_load_dwordx4 v28, s[40:43], s70 offen offset:0 lds\n"
    "v_lshlrev_b32 v80, 16, v32\n"
    "v_and_b32 v81, 0xffff0000, v32\n"
    "s_waitcnt lgkmcnt(4)\n"
    "v_fma_f32 v80, v80, v112, v116\n"
    "v_fma_f32 v81, v81, v113, v117\n"
    "buffer_load_dwordx4 v29, s[40:43], s70 offen offset:1024 lds\n"
    "v_cvt_pk_bf16_f32 v32, v80, v81\n"
    "v_lshlrev_b32 v82, 16, v33\n"
    "v_and_b32 v83, 0xffff0000, v33\n"
    "v_fma_f32 v82, v82, v114, v118\n"
    "buffer_load_dwordx4 v30, s[40:43], s70 offen offset:2048 lds\n"
    "v_fma_f32 v83, v83, v115, v119\n"
    "v_cvt_pk_bf16_f32 v33, v82, v83\n"
    "ds_read_b128 v[112:115], v85 offset:128\n"
    "ds_read_b128 v[116:119], v85 offset:144\n"
    "buffer_load_dwordx4 v31, s[40:43], s70 offen offset:3072 lds\n"
    "v_lshlrev_b32 v2, 16, v34\n"
    "v_and_b32 v3, 0xffff0000, v34\n"
    "v_fma_f32 v2, v2, v120, v124\n"
    "v_fma_f32 v3, v3, v121, v125\n"
    "v_cvt_pk_bf16_f32 v34, v2, v3\n"
    "v_lshlrev_b32 v4, 16, v35\n"
    "v_and_b32 v5, 0xffff0000, v35\n"
    "v_fma_f32 v4, v4, v122, v126\n"
    "v_fma_f32 v5, v5, v123, v127\n"
    "v_cvt_pk_bf16_f32 v35, v4, v5\n"
    "ds_read_b128 v[120:123], v85 offset:160\n"
    "ds_read_b128 v[124:127], v85 offset:176\n"
    "s_waitcnt vmcnt(12) lgkmcnt(0)\n"
    "s_barrier\n"
    "v_mfma_f32_32x32x16_bf16 v[128:143], a[64:67], v[32:35], 0\n"
    "ds_read_b64_tr_b16 a[128:129], v92 offset:0\n"
    "v_lshlrev_b32 v80, 16, v36\n"
    "v_and_b32 v81, 0xffff0000, v36\n"
    "v_fma_f32 v80, v80, v112, v116\n"
    "v_fma_f32 v81, v81, v113, v117\n"
    "v_cvt_pk_bf16_f32 v36, v80, v81\n"
    "v_lshlrev_b32 v82, 16, v37\n"
    "ds_read_b64_tr_b16 a[130:131], v92 offset:2048\n"
    "ds_read_b64_tr_b16 a[64:65], v88 offset:16384\n"
    "v_and_b32 v83, 0xffff0000, v37\n"
    "v_fma_f32 v82, v82, v114, v118\n"
    "v_mfma_f32_32x32x16_bf16 v[144:159], a[68:71], v[32:35], 0\n"
    "s_mov_b32 m0, s66\n"
    "s_mov_b32 s70, 0x140000\n"
    "buffer_load_dwordx4 v28, s[40:43], s70 offen offset:0 lds\n"
    "v_fma_f32 v83, v83, v115, v119\n"
    "ds_read_b64_tr_b16 a[66:67], v88 offset:18432\n"
    "ds_read_b64_tr_b16 a[132:133], v93 offset:0\n"
    "v_cvt_pk_bf16_f32 v37, v82, v83\n"
    "ds_read_b128 v[112:115], v85 offset:256\n"
    "v_mfma_f32_32x32x16_bf16 v[160:175], a[72:75], v[32:35], 0\n"
    "ds_read_b64_tr_b16 a[134:135], v93 offset:2048\n"
    "ds_read_b128 v[116:119], v85 offset:272\n"
    "s_mov_b32 s73, 0x2000\n"
    "buffer_load_dwordx4 v[64:67], v1, s[36:39], s73 offen offset:0\n"
    "ds_read_b64_tr_b16 a[68:69], v89 offset:16384\n"
    "v_lshlrev_b32 v2, 16, v38\n"
    "v_and_b32 v3, 0xffff0000, v38\n"
    "v_mfma_f32_32x32x16_bf16 v[176:191], a[76:79], v[32:35], 0\n"
    "ds_read_b64_tr_b16 a[70:71], v89 offset:18432\n"
    "v_fma_f32 v2, v2, v120, v124\n"
    "ds_read_b64_tr_b16 a[136:137], v94 offset:0\n"
    "ds_read_b64_tr_b16 a[138:139], v94 offset:2048\n"
    "v_fma_f32 v3, v3, v121, v125\n"
    "v_cvt_pk_bf16_f32 v38, v2, v3\n"
    "v_mfma_f32_32x32x16_bf16 v[192:207], a[80:83], v[32:35], 0\n"
    "ds_read_b64_tr_b16 a[72:73], v90 offset:16384\n"
    "v_lshlrev_b32 v4, 16, v39\n"
    "buffer_load_dwordx4 v29, s[40:43], s70 offen offset:1024 lds\n"
    "ds_read_b64_tr_b16 a[74:75], v90 offset:18432\n"
    "v_and_b32 v5, 0xffff0000, v39\n"
    "v_fma_f32 v4, v4, v122, v126\n"
    "v_mfma_f32_32x32x16_bf16 v[208:223], a[84:87], v[32:35], 0\n"
    "ds_read_b64_tr_b16 a[140:141], v95 offset:0\n"
    "v_fma_f32 v5, v5, v123, v127\n"
    "ds_read_b64_tr_b16 a[142:143], v95 offset:2048\n"
    "ds_read_b64_tr_b16 a[76:77], v91 offset:16384\n"
    "v_cvt_pk_bf16_f32 v39, v4, v5\n"
    "ds_read_b128 v[120:123], v85 offset:288\n"
    "v_mfma_f32_32x32x16_bf16 v[224:239], a[88:91], v[32:35], 0\n"
    "ds_read_b64_tr_b16 a[78:79], v91 offset:18432\n"
    "ds_read_b128 v[124:127], v85 offset:304\n"
    "ds_read_b64_tr_b16 a[144:145], v92 offset:256\n"
    "ds_read_b64_tr_b16 a[146:147], v92 offset:2304\n"
    "v_lshlrev_b32 v80, 16, v40\n"
    "v_and_b32 v81, 0xffff0000, v40\n"
    "v_mfma_f32_32x32x16_bf16 v[240:255], a[92:95], v[32:35], 0\n"
    "ds_read_b64_tr_b16 a[80:81], v88 offset:16640\n"
    "s_waitcnt lgkmcnt(5)\n"
    "v_fma_f32 v80, v80, v112, v116\n"
    "ds_read_b64_tr_b16 a[82:83], v88 offset:18688\n"
    "ds_read_b64_tr_b16 a[148:149], v93 offset:256\n"
    "v_fma_f32 v81, v81, v113, v117\n"
    "v_cvt_pk_bf16_f32 v40, v80, v81\n"
    "v_mfma_f32_32x32x16_bf16 v[128:143], a[96:99], v[36:39], v[128:143]\n"
    "buffer_load_dwordx4 v30, s[40:43], s70 offen offset:2048 lds\n"
    "v_lshlrev_b32 v82, 16, v41\n"
    "ds_read_b64_tr_b16 a[150:151], v93 offset:2304\n"
    "ds_read_b64_tr_b16 a[84:85], v89 offset:16640\n"
    "v_and_b32 v83, 0xffff0000, v41\n"
    "v_fma_f32 v82, v82, v114, v118\n"
    "v_mfma_f32_32x32x16_bf16 v[144:159], a[100:103], v[36:39], v[144:159]\n"
    "ds_read_b64_tr_b16 a[86:87], v89 offset:18688\n"
    "v_fma_f32 v83, v83, v115, v119\n"
    "s_mov_b32 s73, 0x2000\n"
    "buffer_load_dwordx4 v[68:71], v1, s[36:39], s73 offen offset:1024\n"
    "ds_read_b64_tr_b16 a[152:153], v94 offset:256\n"
    "v_cvt_pk_bf16_f32 v41, v82, v83\n"
    "ds_read_b128 v[112:115], v85 offset:384\n"
    "v_mfma_f32_32x32x16_bf16 v[160:175], a[104:107], v[36:39], v[160:175]\n"
    "ds_read_b64_tr_b16 a[154:155], v94 offset:2304\n"
    "ds_read_b128 v[116:119], v85 offset:400\n"
    "ds_read_b64_tr_b16 a[88:89], v90 offset:16640\n"
    "ds_read_b64_tr_b16 a[90:91], v90 offset:18688\n"
    "v_lshlrev_b32 v2, 16, v42\n"
    "v_and_b32 v3, 0xffff0000, v42\n"
    "v_mfma_f32_32x32x16_bf16 v[176:191], a[108:111], v[36:39], v[176:191]\n"
    "ds_read_b64_tr_b16 a[156:157], v95 offset:256\n"
    "s_waitcnt lgkmcnt(5)\n"
    "v_fma_f32 v2, v2, v120, v124\n"
    "buffer_load_dwordx4 v31, s[40:43], s70 offen offset:3072 lds\n"
    "ds_read_b64_tr_b16 a[158:159], v95 offset:2304\n"
    "v_fma_f32 v3, v3, v121, v125\n"
    "v_cvt_pk_bf16_f32 v42, v2, v3\n"
    "v_mfma_f32_32x32x16_bf16 v[192:207], a[112:115], v[36:39], v[192:207]\n"
    "ds_read_b64_tr_b16 a[92:93], v91 offset:16640\n"
    "v_lshlrev_b32 v4, 16, v43\n"
    "ds_read_b64_tr_b16 a[94:95], v91 offset:18688\n"
    "ds_read_b64_tr_b16 a[160:161], v92 offset:8192\n"
    "v_and_b32 v5, 0xffff0000, v43\n"
    "v_fma_f32 v4, v4, v122, v126\n"
    "v_mfma_f32_32x32x16_bf16 v[208:223], a[116:119], v[36:39], v[208:223]\n"
    "ds_read_b64_tr_b16 a[162:163], v92 offset:10240\n"
    "v_fma_f32 v5, v5, v123, v127\n"
    "ds_read_b64_tr_b16 a[96:97], v88 offset:24576\n"
    "ds_read_b64_tr_b16 a[98:99], v88 offset:26624\n"
    "v_cvt_pk_bf16_f32 v43, v4, v5\n"
    "ds_read_b128 v[120:123], v85 offset:416\n"
    "v_mfma_f32_32x32x16_bf16 v[224:239], a[120:123], v[36:39], v[224:239]\n"
    "ds_read_b64_tr_b16 a[164:165], v93 offset:8192\n"
    "ds_read_b128 v[124:127], v85 offset:432\n"
    "ds_read_b64_tr_b16 a[166:167], v93 offset:10240\n"
    "ds_read_b64_tr_b16 a[100:101], v89 offset:24576\n"
    "v_lshlrev_b32 v80, 16, v44\n"
    "v_and_b32 v81, 0xffff0000, v44\n"
    "v_mfma_f32_32x32x16_bf16 v[240:255], a[124:127], v[36:39], v[240:255]\n"
    "s_mov_b32 m0, s63\n"
    "s_mov_b32 s70, 0x180000\n"
    "buffer_load_dwordx4 v28, s[40:43], s70 offen offset:0 lds\n"
    "s_waitcnt lgkmcnt(3)\n"
    "v_fma_f32 v80, v80, v112, v116\n"
    "ds_read_b64_tr_b16 a[102:103], v89 offset:26624\n"
    "ds_read_b64_tr_b16 a[168:169], v94 offset:8192\n"
    "v_fma_f32 v81, v81, v113, v117\n"
    "v_cvt_pk_bf16_f32 v44, v80, v81\n"
    "v_mfma_f32_32x32x16_bf16 v[128:143], a[128:131], v[40:43], v[128:143]\n"
    "ds_read_b64_tr_b16 a[170:171], v94 offset:10240\n"
    "v_lshlrev_b32 v82, 16, v45\n"
    "s_mov_b32 s73, 0x2000\n"
    "buffer_load_dwordx4 v[72:75], v1, s[36:39], s73 offen offset:2048\n"
    "ds_read_b64_tr_b16 a[104:105], v90 offset:24576\n"
    "v_and_b32 v83, 0xffff0000, v45\n"
    "v_fma_f32 v82, v82, v114, v118\n"
    "v_mfma_f32_32x32x16_bf16 v[144:159], a[132:135], v[40:43], v[144:159]\n"
    "ds_read_b64_tr_b16 a[106:107], v90 offset:26624\n"
    "v_fma_f32 v83, v83, v115, v119\n"
    "ds_read_b64_tr_b16 a[172:173], v95 offset:8192\n"
    "ds_read_b64_tr_b16 a[174:175], v95 offset:10240\n"
    "v_cvt_pk_bf16_f32 v45, v82, v83\n"
    "ds_read_b128 v[112:115], v85 offset:512\n"
    "v_mfma_f32_32x32x16_bf16 v[160:175], a[136:139], v[40:43], v[160:175]\n"
    "ds_read_b64_tr_b16 a[108:109], v91 offset:24576\n"
    "ds_read_b128 v[116:119], v85 offset:528\n"
    "buffer_load_dwordx4 v29, s[40:43], s70 offen offset:1024 lds\n"
    "ds_read_b64_tr_b16 a[110:111], v91 offset:26624\n"
    "v_lshlrev_b32 v2, 16, v46\n"
    "v_and_b32 v3, 0xffff0000, v46\n"
    "v_mfma_f32_32x32x16_bf16 v[176:191], a[140:143], v[40:43], v[176:191]\n"
    "ds_read_b64_tr_b16 a[176:177], v92 offset:8448\n"
    "s_waitcnt lgkmcnt(4)\n"
    "v_fma_f32 v2, v2, v120, v124\n"
    "ds_read_b64_tr_b16 a[178:179], v92 offset:10496\n"
    "ds_read_b64_tr_b16 a[112:113], v88 offset:24832\n"
    "v_fma_f32 v3, v3, v121, v125\n"
    "v_cvt_pk_bf16_f32 v46, v2, v3\n"
    "v_mfma_f32_32x32x16_bf16 v[192:207], a[144:147], v[40:43], v[192:207]\n"
    "ds_read_b64_tr_b16 a[114:115], v88 offset:26880\n"
    "v_lshlrev_b32 v4, 16, v47\n"
    "ds_read_b64_tr_b16 a[180:181], v93 offset:8448\n"
    "ds_read_b64_tr_b16 a[182:183], v93 offset:10496\n"
    "v_and_b32 v5, 0xffff0000, v47\n"
    "v_fma_f32 v4, v4, v122, v126\n"
    "v_mfma_f32_32x32x16_bf16 v[208:223], a[148:151], v[40:43], v[208:223]\n"
    "ds_read_b64_tr_b16 a[116:117], v89 offset:24832\n"
    "v_fma_f32 v5, v5, v123, v127\n"
    "ds_read_b64_tr_b16 a[118:119], v89 offset:26880\n"
    "ds_read_b64_tr_b16 a[184:185], v94 offset:8448\n"
    "v_cvt_pk_bf16_f32 v47, v4, v5\n"
    "ds_read_b128 v[120:123], v85 offset:544\n"
    "v_mfma_f32_32x32x16_bf16 v[224:239], a[152:155], v[40:43], v[224:239]\n"
    "buffer_load_dwordx4 v30, s[40:43], s70 offen offset:2048 lds\n"
    "ds_read_b128 v[124:127], v85 offset:560\n"
    "ds_read_b64_tr_b16 a[186:187], v94 offset:10496\n"
    "ds_read_b64_tr_b16 a[120:121], v90 offset:24832\n"
    "s_waitcnt vmcnt(21)\n"
    "v_lshlrev_b32 v80, 16, v48\n"
    "v_and_b32 v81, 0xffff0000, v48\n"
    "v_mfma_f32_32x32x16_bf16 v[240:255], a[156:159], v[40:43], v[240:255]\n"
    "ds_read_b64_tr_b16 a[122:123], v90 offset:26880\n"
    "s_waitcnt lgkmcnt(4)\n"
    "v_fma_f32 v80, v80, v112, v116\n"
    "s_mov_b32 s73, 0x2000\n"
    "buffer_load_dwordx4 v[76:79], v1, s[36:39], s73 offen offset:3072\n"
    "ds_read_b64_tr_b16 a[188:189], v95 offset:8448\n"
    "v_fma_f32 v81, v81, v113, v117\n"
    "v_cvt_pk_bf16_f32 v48, v80, v81\n"
    "v_mfma_f32_32x32x16_bf16 v[128:143], a[160:163], v[44:47], v[128:143]\n"
    "ds_read_b64_tr_b16 a[190:191], v95 offset:10496\n"
    "v_lshlrev_b32 v82, 16, v49\n"
    "ds_read_b64_tr_b16 a[124:125], v91 offset:24832\n"
    "ds_read_b64_tr_b16 a[126:127], v91 offset:26880\n"
    "v_and_b32 v83, 0xffff0000, v49\n"
    "v_fma_f32 v82, v82, v114, v118\n"
    "v_mfma_f32_32x32x16_bf16 v[144:159], a[164:167], v[44:47], v[144:159]\n"
    "v_fma_f32 v83, v83, v115, v119\n"
    "v_cvt_pk_bf16_f32 v49, v82, v83\n"
    "buffer_load_dwordx4 v31, s[40:43], s70 offen offset:3072 lds\n"
    "ds_read_b128 v[112:115], v85 offset:640\n"
    "ds_read_b128 v[116:119], v85 offset:656\n"
    "v_mfma_f32_32x32x16_bf16 v[160:175], a[168:171], v[44:47], v[160:175]\n"
    "v_lshlrev_b32 v2, 16, v50\n"
    "v_and_b32 v3, 0xffff0000, v50\n"
    "s_waitcnt lgkmcnt(2)\n"
    "v_fma_f32 v2, v2, v120, v124\n"
    "v_fma_f32 v3, v3, v121, v125\n"
    "v_cvt_pk_bf16_f32 v50, v2, v3\n"
    "v_lshlrev_b32 v4, 16, v51\n"
    "v_mfma_f32_32x32x16_bf16 v[176:191], a[172:175], v[44:47], v[176:191]\n"
    "v_and_b32 v5, 0xffff0000, v51\n"
    "v_fma_f32 v4, v4, v122, v126\n"
    "v_fma_f32 v5, v5, v123, v127\n"
    "v_cvt_pk_bf16_f32 v51, v4, v5\n"
    "ds_read_b128 v[120:123], v85 offset:672\n"
    "ds_read_b128 v[124:127], v85 offset:688\n"
    "v_mfma_f32_32x32x16_bf16 v[192:207], a[176:179], v[44:47], v[192:207]\n"
    "s_waitcnt vmcnt(22)\n"
    "v_lshlrev_b32 v80, 16, v52\n"
    "v_and_b32 v81, 0xffff0000, v52\n"
    "s_waitcnt vmcnt(12) lgkmcnt(0)\n"
    "s_barrier\n"
    "ds_read_b64_tr_b16 a[128:129], v92 offset:16384\n"
    "ds_read_b64_tr_b16 a[130:131], v92 offset:18432\n"
    "v_fma_f32 v80, v80, v112, v116\n"
    "v_mfma_f32_32x32x16_bf16 v[208:223], a[180:183], v[44:47], v[208:223]\n"
    "ds_read_b64_tr_b16 a[132:133], v93 offset:16384\n"
    "v_fma_f32 v81, v81, v113, v117\n"
    "ds_read_b64_tr_b16 a[134:135], v93 offset:18432\n"
    "ds_read_b64_tr_b16 a[136:137], v94 offset:16384\n"
    "v_cvt_pk_bf16_f32 v52, v80, v81\n"
    "v_mfma_f32_32x32x16_bf16 v[224:239], a[184:187], v[44:47], v[224:239]\n"
    "ds_read_b64_tr_b16 a[138:139], v94 offset:18432\n"
    "ds_read_b64_tr_b16 a[140:141], v95 offset:16384\n"
    "ds_read_b64_tr_b16 a[142:143], v95 offset:18432\n"
    "v_mfma_f32_32x32x16_bf16 v[240:255], a[188:191], v[44:47], v[240:255]\n"
    "ds_read_b64_tr_b16 a[144:145], v92 offset:16640\n"
    "ds_read_b64_tr_b16 a[146:147], v92 offset:18688\n"
    "ds_read_b64_tr_b16 a[148:149], v93 offset:16640\n"
    "v_mfma_f32_32x32x16_bf16 v[128:143], a[64:67], v[48:51], v[128:143]\n"
    "ds_read_b64_tr_b16 a[150:151], v93 offset:18688\n"
    "v_lshlrev_b32 v82, 16, v53\n"
    "s_mov_b64 s[44:45], %[k]\n"
    "s_mov_b32 s46, 0x100000\n"
    "s_mov_b32 s47, 0x20000\n"
    "s_mov_b64 s[48:49], %[v]\n"
    "s_mov_b32 s50, 0x100000\n"
    "s_mov_b32 s51, 0x20000\n"
    "s_mov_b64 s[52:53], %[wo]\n"
    "s_mov_b32 s54, 0x38000\n"
    "s_mov_b32 s55, 0x20000\n"
    "ds_read_b64_tr_b16 a[64:65], v88 offset:32768\n"
    "ds_read_b64_tr_b16 a[66:67], v88 offset:34816\n"
    "v_and_b32 v83, 0xffff0000, v53\n"
    "v_fma_f32 v82, v82, v114, v118\n"
    "v_mfma_f32_32x32x16_bf16 v[144:159], a[68:71], v[48:51], v[144:159]\n"
    "s_mov_b32 m0, s67\n"
    "s_mov_b32 s70, 0x1c0000\n"
    "buffer_load_dwordx4 v28, s[40:43], s70 offen offset:0 lds\n"
    "v_fma_f32 v83, v83, v115, v119\n"
    "ds_read_b64_tr_b16 a[68:69], v89 offset:32768\n"
    "ds_read_b64_tr_b16 a[70:71], v89 offset:34816\n"
    "v_cvt_pk_bf16_f32 v53, v82, v83\n"
    "ds_read_b128 v[112:115], v85 offset:768\n"
    "v_mfma_f32_32x32x16_bf16 v[160:175], a[72:75], v[48:51], v[160:175]\n"
    "ds_read_b64_tr_b16 a[152:153], v94 offset:16640\n"
    "ds_read_b128 v[116:119], v85 offset:784\n"
    "s_mov_b32 s73, 0x3000\n"
    "buffer_load_dwordx4 v[96:99], v1, s[36:39], s73 offen offset:0\n"
    "ds_read_b64_tr_b16 a[72:73], v90 offset:32768\n"
    "v_lshlrev_b32 v2, 16, v54\n"
    "v_and_b32 v3, 0xffff0000, v54\n"
    "v_mfma_f32_32x32x16_bf16 v[176:191], a[76:79], v[48:51], v[176:191]\n"
    "ds_read_b64_tr_b16 a[74:75], v90 offset:34816\n"
    "v_fma_f32 v2, v2, v120, v124\n"
    "ds_read_b64_tr_b16 a[76:77], v91 offset:32768\n"
    "ds_read_b64_tr_b16 a[78:79], v91 offset:34816\n"
    "v_fma_f32 v3, v3, v121, v125\n"
    "v_cvt_pk_bf16_f32 v54, v2, v3\n"
    "v_mfma_f32_32x32x16_bf16 v[192:207], a[80:83], v[48:51], v[192:207]\n"
    "ds_read_b64_tr_b16 a[154:155], v94 offset:18688\n"
    "v_lshlrev_b32 v4, 16, v55\n"
    "buffer_load_dwordx4 v29, s[40:43], s70 offen offset:1024 lds\n"
    "ds_read_b64_tr_b16 a[80:81], v88 offset:33024\n"
    "v_and_b32 v5, 0xffff0000, v55\n"
    "v_fma_f32 v4, v4, v122, v126\n"
    "v_mfma_f32_32x32x16_bf16 v[208:223], a[84:87], v[48:51], v[208:223]\n"
    "ds_read_b64_tr_b16 a[82:83], v88 offset:35072\n"
    "v_fma_f32 v5, v5, v123, v127\n"
    "ds_read_b64_tr_b16 a[84:85], v89 offset:33024\n"
    "ds_read_b64_tr_b16 a[86:87], v89 offset:35072\n"
    "v_cvt_pk_bf16_f32 v55, v4, v5\n"
    "ds_read_b128 v[120:123], v85 offset:800\n"
    "v_mfma_f32_32x32x16_bf16 v[224:239], a[88:91], v[48:51], v[224:239]\n"
    "ds_read_b64_tr_b16 a[156:157], v95 offset:16640\n"
    "ds_read_b128 v[124:127], v85 offset:816\n"
    "ds_read_b64_tr_b16 a[88:89], v90 offset:33024\n"
    "ds_read_b64_tr_b16 a[90:91], v90 offset:35072\n"
    "v_lshlrev_b32 v80, 16, v56\n"
    "v_and_b32 v81, 0xffff0000, v56\n"
    "v_mfma_f32_32x32x16_bf16 v[240:255], a[92:95], v[48:51], v[240:255]\n"
    "ds_read_b64_tr_b16 a[158:159], v95 offset:18688\n"
    "s_waitcnt lgkmcnt(5)\n"
    "v_fma_f32 v80, v80, v112, v116\n"
    "ds_read_b64_tr_b16 a[92:93], v91 offset:33024\n"
    "ds_read_b64_tr_b16 a[94:95], v91 offset:35072\n"
    "v_fma_f32 v81, v81, v113, v117\n"
    "v_cvt_pk_bf16_f32 v56, v80, v81\n"
    "v_mfma_f32_32x32x16_bf16 v[128:143], a[96:99], v[52:55], v[128:143]\n"
    "buffer_load_dwordx4 v30, s[40:43], s70 offen offset:2048 lds\n"
    "v_lshlrev_b32 v82, 16, v57\n"
    "ds_read_b64_tr_b16 a[160:161], v92 offset:24576\n"
    "ds_read_b64_tr_b16 a[162:163], v92 offset:26624\n"
    "v_and_b32 v83, 0xffff0000, v57\n"
    "v_fma_f32 v82, v82, v114, v118\n"
    "v_mfma_f32_32x32x16_bf16 v[144:159], a[100:103], v[52:55], v[144:159]\n"
    "ds_read_b64_tr_b16 a[96:97], v88 offset:40960\n"
    "v_fma_f32 v83, v83, v115, v119\n"
    "s_mov_b32 s73, 0x3000\n"
    "buffer_load_dwordx4 v[100:103], v1, s[36:39], s73 offen offset:1024\n"
    "ds_read_b64_tr_b16 a[98:99], v88 offset:43008\n"
    "v_cvt_pk_bf16_f32 v57, v82, v83\n"
    "ds_read_b128 v[112:115], v85 offset:896\n"
    "v_mfma_f32_32x32x16_bf16 v[160:175], a[104:107], v[52:55], v[160:175]\n"
    "ds_read_b64_tr_b16 a[164:165], v93 offset:24576\n"
    "ds_read_b128 v[116:119], v85 offset:912\n"
    "ds_read_b64_tr_b16 a[166:167], v93 offset:26624\n"
    "ds_read_b64_tr_b16 a[100:101], v89 offset:40960\n"
    "v_lshlrev_b32 v2, 16, v58\n"
    "v_and_b32 v3, 0xffff0000, v58\n"
    "v_mfma_f32_32x32x16_bf16 v[176:191], a[108:111], v[52:55], v[176:191]\n"
    "ds_read_b64_tr_b16 a[102:103], v89 offset:43008\n"
    "s_waitcnt lgkmcnt(5)\n"
    "v_fma_f32 v2, v2, v120, v124\n"
    "buffer_load_dwordx4 v31, s[40:43], s70 offen offset:3072 lds\n"
    "ds_read_b64_tr_b16 a[168:169], v94 offset:24576\n"
    "v_fma_f32 v3, v3, v121, v125\n"
    "v_cvt_pk_bf16_f32 v58, v2, v3\n"
    "v_mfma_f32_32x32x16_bf16 v[192:207], a[112:115], v[52:55], v[192:207]\n"
    "ds_read_b64_tr_b16 a[170:171], v94 offset:26624\n"
    "v_lshlrev_b32 v4, 16, v59\n"
    "ds_read_b64_tr_b16 a[104:105], v90 offset:40960\n"
    "ds_read_b64_tr_b16 a[106:107], v90 offset:43008\n"
    "v_and_b32 v5, 0xffff0000, v59\n"
    "v_fma_f32 v4, v4, v122, v126\n"
    "v_mfma_f32_32x32x16_bf16 v[208:223], a[116:119], v[52:55], v[208:223]\n"
    "ds_read_b64_tr_b16 a[172:173], v95 offset:24576\n"
    "v_fma_f32 v5, v5, v123, v127\n"
    "ds_read_b64_tr_b16 a[174:175], v95 offset:26624\n"
    "ds_read_b64_tr_b16 a[108:109], v91 offset:40960\n"
    "v_cvt_pk_bf16_f32 v59, v4, v5\n"
    "ds_read_b128 v[120:123], v85 offset:928\n"
    "v_mfma_f32_32x32x16_bf16 v[224:239], a[120:123], v[52:55], v[224:239]\n"
    "ds_read_b64_tr_b16 a[110:111], v91 offset:43008\n"
    "ds_read_b128 v[124:127], v85 offset:944\n"
    "ds_read_b64_tr_b16 a[176:177], v92 offset:24832\n"
    "ds_read_b64_tr_b16 a[178:179], v92 offset:26880\n"
    "v_lshlrev_b32 v80, 16, v60\n"
    "v_and_b32 v81, 0xffff0000, v60\n"
    "v_mfma_f32_32x32x16_bf16 v[240:255], a[124:127], v[52:55], v[240:255]\n"
    "s_mov_b32 m0, s60\n"
    "s_mov_b32 s70, 0x200000\n"
    "buffer_load_dwordx4 v28, s[40:43], s70 offen offset:0 lds\n"
    "s_waitcnt lgkmcnt(3)\n"
    "v_fma_f32 v80, v80, v112, v116\n"
    "ds_read_b64_tr_b16 a[112:113], v88 offset:41216\n"
    "ds_read_b64_tr_b16 a[114:115], v88 offset:43264\n"
    "v_fma_f32 v81, v81, v113, v117\n"
    "v_cvt_pk_bf16_f32 v60, v80, v81\n"
    "v_mfma_f32_32x32x16_bf16 v[128:143], a[128:131], v[56:59], v[128:143]\n"
    "ds_read_b64_tr_b16 a[180:181], v93 offset:24832\n"
    "v_lshlrev_b32 v82, 16, v61\n"
    "s_mov_b32 s73, 0x3000\n"
    "buffer_load_dwordx4 v[104:107], v1, s[36:39], s73 offen offset:2048\n"
    "ds_read_b64_tr_b16 a[182:183], v93 offset:26880\n"
    "v_and_b32 v83, 0xffff0000, v61\n"
    "v_fma_f32 v82, v82, v114, v118\n"
    "v_mfma_f32_32x32x16_bf16 v[144:159], a[132:135], v[56:59], v[144:159]\n"
    "ds_read_b64_tr_b16 a[116:117], v89 offset:41216\n"
    "v_fma_f32 v83, v83, v115, v119\n"
    "ds_read_b64_tr_b16 a[118:119], v89 offset:43264\n"
    "ds_read_b64_tr_b16 a[184:185], v94 offset:24832\n"
    "v_cvt_pk_bf16_f32 v61, v82, v83\n"
    "ds_read_b128 v[112:115], v85 offset:1024\n"
    "v_mfma_f32_32x32x16_bf16 v[160:175], a[136:139], v[56:59], v[160:175]\n"
    "ds_read_b64_tr_b16 a[186:187], v94 offset:26880\n"
    "ds_read_b128 v[116:119], v85 offset:1040\n"
    "buffer_load_dwordx4 v29, s[40:43], s70 offen offset:1024 lds\n"
    "ds_read_b64_tr_b16 a[120:121], v90 offset:41216\n"
    "v_lshlrev_b32 v2, 16, v62\n"
    "v_and_b32 v3, 0xffff0000, v62\n"
    "v_mfma_f32_32x32x16_bf16 v[176:191], a[140:143], v[56:59], v[176:191]\n"
    "ds_read_b64_tr_b16 a[122:123], v90 offset:43264\n"
    "s_waitcnt lgkmcnt(4)\n"
    "v_fma_f32 v2, v2, v120, v124\n"
    "ds_read_b64_tr_b16 a[188:189], v95 offset:24832\n"
    "ds_read_b64_tr_b16 a[190:191], v95 offset:26880\n"
    "v_fma_f32 v3, v3, v121, v125\n"
    "v_cvt_pk_bf16_f32 v62, v2, v3\n"
    "v_mfma_f32_32x32x16_bf16 v[192:207], a[144:147], v[56:59], v[192:207]\n"
    "ds_read_b64_tr_b16 a[124:125], v91 offset:41216\n"
    "v_lshlrev_b32 v4, 16, v63\n"
    "ds_read_b64_tr_b16 a[126:127], v91 offset:43264\n"
    "v_and_b32 v5, 0xffff0000, v63\n"
    "v_fma_f32 v4, v4, v122, v126\n"
    "v_fma_f32 v5, v5, v123, v127\n"
    "v_mfma_f32_32x32x16_bf16 v[208:223], a[148:151], v[56:59], v[208:223]\n"
    "v_cvt_pk_bf16_f32 v63, v4, v5\n"
    "ds_read_b128 v[120:123], v85 offset:1056\n"
    "ds_read_b128 v[124:127], v85 offset:1072\n"
    "s_waitcnt vmcnt(19)\n"
    "v_lshlrev_b32 v80, 16, v64\n"
    "v_and_b32 v81, 0xffff0000, v64\n"
    "s_waitcnt lgkmcnt(2)\n"
    "v_fma_f32 v80, v80, v112, v116\n"
    "v_mfma_f32_32x32x16_bf16 v[224:239], a[152:155], v[56:59], v[224:239]\n"
    "buffer_load_dwordx4 v30, s[40:43], s70 offen offset:2048 lds\n"
    "v_fma_f32 v81, v81, v113, v117\n"
    "v_cvt_pk_bf16_f32 v64, v80, v81\n"
    "v_lshlrev_b32 v82, 16, v65\n"
    "v_and_b32 v83, 0xffff0000, v65\n"
    "v_mfma_f32_32x32x16_bf16 v[240:255], a[156:159], v[56:59], v[240:255]\n"
    "v_fma_f32 v82, v82, v114, v118\n"
    "v_fma_f32 v83, v83, v115, v119\n"
    "s_mov_b32 s73, 0x3000\n"
    "buffer_load_dwordx4 v[108:111], v1, s[36:39], s73 offen offset:3072\n"
    "v_cvt_pk_bf16_f32 v65, v82, v83\n"
    "ds_read_b128 v[112:115], v85 offset:1152\n"
    "v_mfma_f32_32x32x16_bf16 v[128:143], a[160:163], v[60:63], v[128:143]\n"
    "ds_read_b128 v[116:119], v85 offset:1168\n"
    "v_lshlrev_b32 v2, 16, v66\n"
    "v_and_b32 v3, 0xffff0000, v66\n"
    "s_waitcnt lgkmcnt(2)\n"
    "v_fma_f32 v2, v2, v120, v124\n"
    "v_fma_f32 v3, v3, v121, v125\n"
    "v_cvt_pk_bf16_f32 v66, v2, v3\n"
    "v_mfma_f32_32x32x16_bf16 v[144:159], a[164:167], v[60:63], v[144:159]\n"
    "v_lshlrev_b32 v4, 16, v67\n"
    "v_and_b32 v5, 0xffff0000, v67\n"
    "buffer_load_dwordx4 v31, s[40:43], s70 offen offset:3072 lds\n"
    "v_fma_f32 v4, v4, v122, v126\n"
    "v_fma_f32 v5, v5, v123, v127\n"
    "v_mfma_f32_32x32x16_bf16 v[160:175], a[168:171], v[60:63], v[160:175]\n"
    "v_cvt_pk_bf16_f32 v67, v4, v5\n"
    "ds_read_b128 v[120:123], v85 offset:1184\n"
    "ds_read_b128 v[124:127], v85 offset:1200\n"
    "v_lshrrev_b32 v12, 5, v0\n"
    "v_and_b32 v13, 31, v0\n"
    "s_lshl_b32 s68, %[w], 12\n"
    "s_lshl_b32 s69, %[w], 3\n"
    "s_and_b32 s69, s69, 8\n"
    "v_lshl_add_u32 v14, v12, 9, s68\n"
    "v_mfma_f32_32x32x16_bf16 v[176:191], a[172:175], v[60:63], v[176:191]\n"
    "v_xor_b32 v15, s69, v13\n"
    "v_xor_b32 v15, v15, v12\n"
    "v_xor_b32 v87, 0, v15\n"
    "v_lshl_add_u32 v20, v87, 4, v14\n"
    "v_xor_b32 v87, 2, v15\n"
    "v_lshl_add_u32 v21, v87, 4, v14\n"
    "v_mfma_f32_32x32x16_bf16 v[192:207], a[176:179], v[60:63], v[192:207]\n"
    "v_xor_b32 v87, 4, v15\n"
    "v_lshl_add_u32 v22, v87, 4, v14\n"
    "s_waitcnt vmcnt(12) lgkmcnt(0)\n"
    "s_barrier\n"
    "ds_read_b64_tr_b16 a[128:129], v92 offset:32768\n"
    "ds_read_b64_tr_b16 a[130:131], v92 offset:34816\n"
    "v_xor_b32 v87, 6, v15\n"
    "v_mfma_f32_32x32x16_bf16 v[208:223], a[180:183], v[60:63], v[208:223]\n"
    "ds_read_b64_tr_b16 a[132:133], v93 offset:32768\n"
    "v_lshl_add_u32 v23, v87, 4, v14\n"
    "ds_read_b64_tr_b16 a[134:135], v93 offset:34816\n"
    "ds_read_b64_tr_b16 a[136:137], v94 offset:32768\n"
    "v_lshlrev_b32 v15, 1, v12\n"
    "v_xor_b32 v15, v15, v13\n"
    "v_mfma_f32_32x32x16_bf16 v[224:239], a[184:187], v[60:63], v[224:239]\n"
    "ds_read_b64_tr_b16 a[138:139], v94 offset:34816\n"
    "v_xor_b32 v87, 0, v15\n"
    "v_lshl_add_u32 v24, v87, 4, v14\n"
    "v_xor_b32 v87, 4, v15\n"
    "v_lshl_add_u32 v25, v87, 4, v14\n"
    "v_xor_b32 v87, 8, v15\n"
    "v_lshl_add_u32 v26, v87, 4, v14\n"
    "v_xor_b32 v87, 12, v15\n"
    "v_lshl_add_u32 v27, v87, 4, v14\n"
    "v_and_b32 v12, 15, v0\n"
    "v_lshrrev_b32 v13, 4, v0\n"
    "v_xor_b32 v14, v13, v12\n"
    "v_lshlrev_b32 v15, 9, v12\n"
    "v_add_u32 v15, %[lb], v15\n"
    "v_xor_b32 v87, 0, v14\n"
    "v_lshl_add_u32 v8, v87, 4, v15\n"
    "v_xor_b32 v87, 4, v14\n"
    "v_lshl_add_u32 v9, v87, 4, v15\n"
    "v_xor_b32 v87, 8, v14\n"
    "v_lshl_add_u32 v10, v87, 4, v15\n"
    "v_xor_b32 v87, 12, v14\n"
    "v_lshl_add_u32 v11, v87, 4, v15\n"
    "v_lshrrev_b32 v18, 2, v0\n"
    "v_and_b32 v87, 3, v0\n"
    "v_lshlrev_b32 v87, 3, v87\n"
    "v_lshl_add_u32 v19, v18, 9, v87\n"
    "v_add_u32 v19, 0x10000, v19\n"
    "v_add_u32 v19, %[lb], v19\n"
    "v_and_b32 v18, 7, v18\n"
    "v_xor_b32 v87, 0, v18\n"
    "v_lshl_add_u32 v12, v87, 5, v19\n"
    "v_xor_b32 v87, 1, v18\n"
    "v_lshl_add_u32 v13, v87, 5, v19\n"
    "v_xor_b32 v87, 2, v18\n"
    "v_lshl_add_u32 v14, v87, 5, v19\n"
    "v_xor_b32 v87, 3, v18\n"
    "v_lshl_add_u32 v15, v87, 5, v19\n"
    "v_xor_b32 v87, 4, v18\n"
    "v_lshl_add_u32 v16, v87, 5, v19\n"
    "v_xor_b32 v87, 5, v18\n"
    "v_lshl_add_u32 v17, v87, 5, v19\n"
    "v_xor_b32 v87, 7, v18\n"
    "v_xor_b32 v18, 6, v18\n"
    "v_lshl_add_u32 v18, v18, 5, v19\n"
    "v_lshl_add_u32 v19, v87, 5, v19\n"
    "s_mov_b32 s71, 0x42800000\n"
    "ds_read_b64_tr_b16 a[140:141], v95 offset:32768\n"
    "ds_read_b64_tr_b16 a[142:143], v95 offset:34816\n"
    "v_lshlrev_b32 v80, 16, v68\n"
    "v_and_b32 v81, 0xffff0000, v68\n"
    "v_mfma_f32_32x32x16_bf16 v[240:255], a[188:191], v[60:63], v[240:255]\n"
    "ds_read_b64_tr_b16 a[144:145], v92 offset:33024\n"
    "v_fma_f32 v80, v80, v112, v116\n"
    "ds_read_b64_tr_b16 a[146:147], v92 offset:35072\n"
    "ds_read_b64_tr_b16 a[148:149], v93 offset:33024\n"
    "v_fma_f32 v81, v81, v113, v117\n"
    "v_cvt_pk_bf16_f32 v68, v80, v81\n"
    "v_mfma_f32_32x32x16_bf16 v[128:143], a[64:67], v[64:67], v[128:143]\n"
    "ds_read_b64_tr_b16 a[150:151], v93 offset:35072\n"
    "v_lshlrev_b32 v82, 16, v69\n"
    "ds_read_b64_tr_b16 a[64:65], v88 offset:49152\n"
    "ds_read_b64_tr_b16 a[66:67], v88 offset:51200\n"
    "v_and_b32 v83, 0xffff0000, v69\n"
    "v_fma_f32 v82, v82, v114, v118\n"
    "v_mfma_f32_32x32x16_bf16 v[144:159], a[68:71], v[64:67], v[144:159]\n"
    "s_mov_b32 m0, s64\n"
    "s_mov_b32 s70, 0x240000\n"
    "buffer_load_dwordx4 v28, s[40:43], s70 offen offset:0 lds\n"
    "v_fma_f32 v83, v83, v115, v119\n"
    "ds_read_b64_tr_b16 a[68:69], v89 offset:49152\n"
    "ds_read_b64_tr_b16 a[70:71], v89 offset:51200\n"
    "v_cvt_pk_bf16_f32 v69, v82, v83\n"
    "ds_read_b128 v[112:115], v85 offset:1280\n"
    "v_mfma_f32_32x32x16_bf16 v[160:175], a[72:75], v[64:67], v[160:175]\n"
    "ds_read_b64_tr_b16 a[152:153], v94 offset:33024\n"
    "ds_read_b128 v[116:119], v85 offset:1296\n"
    "s_mov_b32 s73, 0x4000\n"
    "buffer_load_dwordx4 v[32:35], v1, s[36:39], s73 offen offset:0\n"
    "ds_read_b64_tr_b16 a[72:73], v90 offset:49152\n"
    "v_lshlrev_b32 v2, 16, v70\n"
    "v_and_b32 v3, 0xffff0000, v70\n"
    "v_mfma_f32_32x32x16_bf16 v[176:191], a[76:79], v[64:67], v[176:191]\n"
    "ds_read_b64_tr_b16 a[74:75], v90 offset:51200\n"
    "v_fma_f32 v2, v2, v120, v124\n"
    "ds_read_b64_tr_b16 a[76:77], v91 offset:49152\n"
    "ds_read_b64_tr_b16 a[78:79], v91 offset:51200\n"
    "v_fma_f32 v3, v3, v121, v125\n"
    "v_cvt_pk_bf16_f32 v70, v2, v3\n"
    "v_mfma_f32_32x32x16_bf16 v[192:207], a[80:83], v[64:67], v[192:207]\n"
    "ds_read_b64_tr_b16 a[154:155], v94 offset:35072\n"
    "v_lshlrev_b32 v4, 16, v71\n"
    "buffer_load_dwordx4 v29, s[40:43], s70 offen offset:1024 lds\n"
    "ds_read_b64_tr_b16 a[80:81], v88 offset:49408\n"
    "v_and_b32 v5, 0xffff0000, v71\n"
    "v_fma_f32 v4, v4, v122, v126\n"
    "v_mfma_f32_32x32x16_bf16 v[208:223], a[84:87], v[64:67], v[208:223]\n"
    "ds_read_b64_tr_b16 a[82:83], v88 offset:51456\n"
    "v_fma_f32 v5, v5, v123, v127\n"
    "ds_read_b64_tr_b16 a[84:85], v89 offset:49408\n"
    "ds_read_b64_tr_b16 a[86:87], v89 offset:51456\n"
    "v_cvt_pk_bf16_f32 v71, v4, v5\n"
    "ds_read_b128 v[120:123], v85 offset:1312\n"
    "v_mfma_f32_32x32x16_bf16 v[224:239], a[88:91], v[64:67], v[224:239]\n"
    "ds_read_b64_tr_b16 a[156:157], v95 offset:33024\n"
    "ds_read_b128 v[124:127], v85 offset:1328\n"
    "ds_read_b64_tr_b16 a[88:89], v90 offset:49408\n"
    "ds_read_b64_tr_b16 a[90:91], v90 offset:51456\n"
    "v_lshlrev_b32 v80, 16, v72\n"
    "v_and_b32 v81, 0xffff0000, v72\n"
    "v_mfma_f32_32x32x16_bf16 v[240:255], a[92:95], v[64:67], v[240:255]\n"
    "ds_read_b64_tr_b16 a[158:159], v95 offset:35072\n"
    "s_waitcnt lgkmcnt(5)\n"
    "v_fma_f32 v80, v80, v112, v116\n"
    "ds_read_b64_tr_b16 a[92:93], v91 offset:49408\n"
    "ds_read_b64_tr_b16 a[94:95], v91 offset:51456\n"
    "v_fma_f32 v81, v81, v113, v117\n"
    "v_cvt_pk_bf16_f32 v72, v80, v81\n"
    "v_mfma_f32_32x32x16_bf16 v[128:143], a[96:99], v[68:71], v[128:143]\n"
    "buffer_load_dwordx4 v30, s[40:43], s70 offen offset:2048 lds\n"
    "v_lshlrev_b32 v82, 16, v73\n"
    "ds_read_b64_tr_b16 a[160:161], v92 offset:40960\n"
    "ds_read_b64_tr_b16 a[162:163], v92 offset:43008\n"
    "v_and_b32 v83, 0xffff0000, v73\n"
    "v_fma_f32 v82, v82, v114, v118\n"
    "v_mfma_f32_32x32x16_bf16 v[144:159], a[100:103], v[68:71], v[144:159]\n"
    "ds_read_b64_tr_b16 a[96:97], v88 offset:57344\n"
    "v_fma_f32 v83, v83, v115, v119\n"
    "s_mov_b32 s73, 0x4000\n"
    "buffer_load_dwordx4 v[36:39], v1, s[36:39], s73 offen offset:1024\n"
    "ds_read_b64_tr_b16 a[98:99], v88 offset:59392\n"
    "v_cvt_pk_bf16_f32 v73, v82, v83\n"
    "ds_read_b128 v[112:115], v85 offset:1408\n"
    "v_mfma_f32_32x32x16_bf16 v[160:175], a[104:107], v[68:71], v[160:175]\n"
    "ds_read_b64_tr_b16 a[164:165], v93 offset:40960\n"
    "ds_read_b128 v[116:119], v85 offset:1424\n"
    "ds_read_b64_tr_b16 a[166:167], v93 offset:43008\n"
    "ds_read_b64_tr_b16 a[100:101], v89 offset:57344\n"
    "v_lshlrev_b32 v2, 16, v74\n"
    "v_and_b32 v3, 0xffff0000, v74\n"
    "v_mfma_f32_32x32x16_bf16 v[176:191], a[108:111], v[68:71], v[176:191]\n"
    "ds_read_b64_tr_b16 a[102:103], v89 offset:59392\n"
    "s_waitcnt lgkmcnt(5)\n"
    "v_fma_f32 v2, v2, v120, v124\n"
    "buffer_load_dwordx4 v31, s[40:43], s70 offen offset:3072 lds\n"
    "ds_read_b64_tr_b16 a[168:169], v94 offset:40960\n"
    "v_fma_f32 v3, v3, v121, v125\n"
    "v_cvt_pk_bf16_f32 v74, v2, v3\n"
    "v_mfma_f32_32x32x16_bf16 v[192:207], a[112:115], v[68:71], v[192:207]\n"
    "ds_read_b64_tr_b16 a[170:171], v94 offset:43008\n"
    "v_lshlrev_b32 v4, 16, v75\n"
    "ds_read_b64_tr_b16 a[104:105], v90 offset:57344\n"
    "ds_read_b64_tr_b16 a[106:107], v90 offset:59392\n"
    "v_and_b32 v5, 0xffff0000, v75\n"
    "v_fma_f32 v4, v4, v122, v126\n"
    "v_mfma_f32_32x32x16_bf16 v[208:223], a[116:119], v[68:71], v[208:223]\n"
    "ds_read_b64_tr_b16 a[172:173], v95 offset:40960\n"
    "v_fma_f32 v5, v5, v123, v127\n"
    "ds_read_b64_tr_b16 a[174:175], v95 offset:43008\n"
    "ds_read_b64_tr_b16 a[108:109], v91 offset:57344\n"
    "v_cvt_pk_bf16_f32 v75, v4, v5\n"
    "ds_read_b128 v[120:123], v85 offset:1440\n"
    "v_mfma_f32_32x32x16_bf16 v[224:239], a[120:123], v[68:71], v[224:239]\n"
    "ds_read_b64_tr_b16 a[110:111], v91 offset:59392\n"
    "ds_read_b128 v[124:127], v85 offset:1456\n"
    "ds_read_b64_tr_b16 a[176:177], v92 offset:41216\n"
    "ds_read_b64_tr_b16 a[178:179], v92 offset:43264\n"
    "v_lshlrev_b32 v80, 16, v76\n"
    "v_and_b32 v81, 0xffff0000, v76\n"
    "v_mfma_f32_32x32x16_bf16 v[240:255], a[124:127], v[68:71], v[240:255]\n"
    "s_mov_b32 m0, s61\n"
    "s_mov_b32 s70, 0x280000\n"
    "buffer_load_dwordx4 v28, s[40:43], s70 offen offset:0 lds\n"
    "s_waitcnt lgkmcnt(3)\n"
    "v_fma_f32 v80, v80, v112, v116\n"
    "ds_read_b64_tr_b16 a[112:113], v88 offset:57600\n"
    "ds_read_b64_tr_b16 a[114:115], v88 offset:59648\n"
    "v_fma_f32 v81, v81, v113, v117\n"
    "v_cvt_pk_bf16_f32 v76, v80, v81\n"
    "v_mfma_f32_32x32x16_bf16 v[128:143], a[128:131], v[72:75], v[128:143]\n"
    "ds_read_b64_tr_b16 a[180:181], v93 offset:41216\n"
    "v_lshlrev_b32 v82, 16, v77\n"
    "s_mov_b32 s73, 0x4000\n"
    "buffer_load_dwordx4 v[40:43], v1, s[36:39], s73 offen offset:2048\n"
    "ds_read_b64_tr_b16 a[182:183], v93 offset:43264\n"
    "v_and_b32 v83, 0xffff0000, v77\n"
    "v_fma_f32 v82, v82, v114, v118\n"
    "v_mfma_f32_32x32x16_bf16 v[144:159], a[132:135], v[72:75], v[144:159]\n"
    "ds_read_b64_tr_b16 a[116:117], v89 offset:57600\n"
    "v_fma_f32 v83, v83, v115, v119\n"
    "ds_read_b64_tr_b16 a[118:119], v89 offset:59648\n"
    "ds_read_b64_tr_b16 a[184:185], v94 offset:41216\n"
    "v_cvt_pk_bf16_f32 v77, v82, v83\n"
    "ds_read_b128 v[112:115], v85 offset:1536\n"
    "v_mfma_f32_32x32x16_bf16 v[160:175], a[136:139], v[72:75], v[160:175]\n"
    "ds_read_b64_tr_b16 a[186:187], v94 offset:43264\n"
    "ds_read_b128 v[116:119], v85 offset:1552\n"
    "buffer_load_dwordx4 v29, s[40:43], s70 offen offset:1024 lds\n"
    "ds_read_b64_tr_b16 a[120:121], v90 offset:57600\n"
    "v_lshlrev_b32 v2, 16, v78\n"
    "v_and_b32 v3, 0xffff0000, v78\n"
    "v_mfma_f32_32x32x16_bf16 v[176:191], a[140:143], v[72:75], v[176:191]\n"
    "ds_read_b64_tr_b16 a[122:123], v90 offset:59648\n"
    "s_waitcnt lgkmcnt(4)\n"
    "v_fma_f32 v2, v2, v120, v124\n"
    "ds_read_b64_tr_b16 a[188:189], v95 offset:41216\n"
    "ds_read_b64_tr_b16 a[190:191], v95 offset:43264\n"
    "v_fma_f32 v3, v3, v121, v125\n"
    "v_cvt_pk_bf16_f32 v78, v2, v3\n"
    "v_mfma_f32_32x32x16_bf16 v[192:207], a[144:147], v[72:75], v[192:207]\n"
    "ds_read_b64_tr_b16 a[124:125], v91 offset:57600\n"
    "v_lshlrev_b32 v4, 16, v79\n"
    "ds_read_b64_tr_b16 a[126:127], v91 offset:59648\n"
    "v_and_b32 v5, 0xffff0000, v79\n"
    "v_fma_f32 v4, v4, v122, v126\n"
    "v_fma_f32 v5, v5, v123, v127\n"
    "v_mfma_f32_32x32x16_bf16 v[208:223], a[148:151], v[72:75], v[208:223]\n"
    "v_cvt_pk_bf16_f32 v79, v4, v5\n"
    "ds_read_b128 v[120:123], v85 offset:1568\n"
    "ds_read_b128 v[124:127], v85 offset:1584\n"
    "s_waitcnt vmcnt(19)\n"
    "v_lshlrev_b32 v80, 16, v96\n"
    "v_and_b32 v81, 0xffff0000, v96\n"
    "s_waitcnt lgkmcnt(2)\n"
    "v_fma_f32 v80, v80, v112, v116\n"
    "v_mfma_f32_32x32x16_bf16 v[224:239], a[152:155], v[72:75], v[224:239]\n"
    "buffer_load_dwordx4 v30, s[40:43], s70 offen offset:2048 lds\n"
    "v_fma_f32 v81, v81, v113, v117\n"
    "v_cvt_pk_bf16_f32 v96, v80, v81\n"
    "v_lshlrev_b32 v82, 16, v97\n"
    "v_and_b32 v83, 0xffff0000, v97\n"
    "v_mfma_f32_32x32x16_bf16 v[240:255], a[156:159], v[72:75], v[240:255]\n"
    "v_fma_f32 v82, v82, v114, v118\n"
    "v_fma_f32 v83, v83, v115, v119\n"
    "s_mov_b32 s73, 0x4000\n"
    "buffer_load_dwordx4 v[44:47], v1, s[36:39], s73 offen offset:3072\n"
    "v_cvt_pk_bf16_f32 v97, v82, v83\n"
    "ds_read_b128 v[112:115], v85 offset:1664\n"
    "v_mfma_f32_32x32x16_bf16 v[128:143], a[160:163], v[76:79], v[128:143]\n"
    "ds_read_b128 v[116:119], v85 offset:1680\n"
    "v_lshlrev_b32 v2, 16, v98\n"
    "v_and_b32 v3, 0xffff0000, v98\n"
    "s_waitcnt lgkmcnt(2)\n"
    "v_fma_f32 v2, v2, v120, v124\n"
    "v_fma_f32 v3, v3, v121, v125\n"
    "v_cvt_pk_bf16_f32 v98, v2, v3\n"
    "v_mfma_f32_32x32x16_bf16 v[144:159], a[164:167], v[76:79], v[144:159]\n"
    "v_lshlrev_b32 v4, 16, v99\n"
    "v_and_b32 v5, 0xffff0000, v99\n"
    "buffer_load_dwordx4 v31, s[40:43], s70 offen offset:3072 lds\n"
    "v_fma_f32 v4, v4, v122, v126\n"
    "v_fma_f32 v5, v5, v123, v127\n"
    "v_mfma_f32_32x32x16_bf16 v[160:175], a[168:171], v[76:79], v[160:175]\n"
    "v_cvt_pk_bf16_f32 v99, v4, v5\n"
    "ds_read_b128 v[120:123], v85 offset:1696\n"
    "ds_read_b128 v[124:127], v85 offset:1712\n"
    "s_waitcnt vmcnt(19)\n"
    "v_lshlrev_b32 v80, 16, v100\n"
    "v_and_b32 v81, 0xffff0000, v100\n"
    "s_waitcnt lgkmcnt(2)\n"
    "v_fma_f32 v80, v80, v112, v116\n"
    "v_mfma_f32_32x32x16_bf16 v[176:191], a[172:175], v[76:79], v[176:191]\n"
    "v_fma_f32 v81, v81, v113, v117\n"
    "v_cvt_pk_bf16_f32 v100, v80, v81\n"
    "v_mfma_f32_32x32x16_bf16 v[192:207], a[176:179], v[76:79], v[192:207]\n"
    "s_waitcnt vmcnt(12) lgkmcnt(0)\n"
    "s_barrier\n"
    "ds_read_b64_tr_b16 a[128:129], v92 offset:49152\n"
    "ds_read_b64_tr_b16 a[130:131], v92 offset:51200\n"
    "v_mfma_f32_32x32x16_bf16 v[208:223], a[180:183], v[76:79], v[208:223]\n"
    "ds_read_b64_tr_b16 a[132:133], v93 offset:49152\n"
    "ds_read_b64_tr_b16 a[134:135], v93 offset:51200\n"
    "ds_read_b64_tr_b16 a[136:137], v94 offset:49152\n"
    "v_mfma_f32_32x32x16_bf16 v[224:239], a[184:187], v[76:79], v[224:239]\n"
    "ds_read_b64_tr_b16 a[138:139], v94 offset:51200\n"
    "ds_read_b64_tr_b16 a[140:141], v95 offset:49152\n"
    "ds_read_b64_tr_b16 a[142:143], v95 offset:51200\n"
    "v_mfma_f32_32x32x16_bf16 v[240:255], a[188:191], v[76:79], v[240:255]\n"
    "ds_read_b64_tr_b16 a[144:145], v92 offset:49408\n"
    "ds_read_b64_tr_b16 a[146:147], v92 offset:51456\n"
    "ds_read_b64_tr_b16 a[148:149], v93 offset:49408\n"
    "v_mfma_f32_32x32x16_bf16 v[128:143], a[64:67], v[96:99], v[128:143]\n"
    "ds_read_b64_tr_b16 a[150:151], v93 offset:51456\n"
    "v_lshlrev_b32 v82, 16, v101\n"
    "ds_read_b64_tr_b16 a[64:65], v88 offset:0\n"
    "ds_read_b64_tr_b16 a[66:67], v88 offset:2048\n"
    "v_and_b32 v83, 0xffff0000, v101\n"
    "v_fma_f32 v82, v82, v114, v118\n"
    "v_mfma_f32_32x32x16_bf16 v[144:159], a[68:71], v[96:99], v[144:159]\n"
    "s_mov_b32 m0, s65\n"
    "s_mov_b32 s70, 0x2c0000\n"
    "buffer_load_dwordx4 v28, s[40:43], s70 offen offset:0 lds\n"
    "v_fma_f32 v83, v83, v115, v119\n"
    "ds_read_b64_tr_b16 a[68:69], v89 offset:0\n"
    "ds_read_b64_tr_b16 a[70:71], v89 offset:2048\n"
    "v_cvt_pk_bf16_f32 v101, v82, v83\n"
    "ds_read_b128 v[112:115], v85 offset:1792\n"
    "v_mfma_f32_32x32x16_bf16 v[160:175], a[72:75], v[96:99], v[160:175]\n"
    "ds_read_b64_tr_b16 a[152:153], v94 offset:49408\n"
    "ds_read_b128 v[116:119], v85 offset:1808\n"
    "s_mov_b32 s73, 0x5000\n"
    "buffer_load_dwordx4 v[48:51], v1, s[36:39], s73 offen offset:0\n"
    "ds_read_b64_tr_b16 a[72:73], v90 offset:0\n"
    "v_lshlrev_b32 v2, 16, v102\n"
    "v_and_b32 v3, 0xffff0000, v102\n"
    "v_mfma_f32_32x32x16_bf16 v[176:191], a[76:79], v[96:99], v[176:191]\n"
    "ds_read_b64_tr_b16 a[74:75], v90 offset:2048\n"
    "v_fma_f32 v2, v2, v120, v124\n"
    "ds_read_b64_tr_b16 a[76:77], v91 offset:0\n"
    "ds_read_b64_tr_b16 a[78:79], v91 offset:2048\n"
    "v_fma_f32 v3, v3, v121, v125\n"
    "v_cvt_pk_bf16_f32 v102, v2, v3\n"
    "v_mfma_f32_32x32x16_bf16 v[192:207], a[80:83], v[96:99], v[192:207]\n"
    "ds_read_b64_tr_b16 a[154:155], v94 offset:51456\n"
    "v_lshlrev_b32 v4, 16, v103\n"
    "buffer_load_dwordx4 v29, s[40:43], s70 offen offset:1024 lds\n"
    "ds_read_b64_tr_b16 a[80:81], v88 offset:256\n"
    "v_and_b32 v5, 0xffff0000, v103\n"
    "v_fma_f32 v4, v4, v122, v126\n"
    "v_mfma_f32_32x32x16_bf16 v[208:223], a[84:87], v[96:99], v[208:223]\n"
    "ds_read_b64_tr_b16 a[82:83], v88 offset:2304\n"
    "v_fma_f32 v5, v5, v123, v127\n"
    "ds_read_b64_tr_b16 a[84:85], v89 offset:256\n"
    "ds_read_b64_tr_b16 a[86:87], v89 offset:2304\n"
    "v_cvt_pk_bf16_f32 v103, v4, v5\n"
    "ds_read_b128 v[120:123], v85 offset:1824\n"
    "v_mfma_f32_32x32x16_bf16 v[224:239], a[88:91], v[96:99], v[224:239]\n"
    "ds_read_b64_tr_b16 a[156:157], v95 offset:49408\n"
    "ds_read_b128 v[124:127], v85 offset:1840\n"
    "ds_read_b64_tr_b16 a[88:89], v90 offset:256\n"
    "ds_read_b64_tr_b16 a[90:91], v90 offset:2304\n"
    "v_lshlrev_b32 v80, 16, v104\n"
    "v_and_b32 v81, 0xffff0000, v104\n"
    "v_mfma_f32_32x32x16_bf16 v[240:255], a[92:95], v[96:99], v[240:255]\n"
    "ds_read_b64_tr_b16 a[158:159], v95 offset:51456\n"
    "s_waitcnt lgkmcnt(5)\n"
    "v_fma_f32 v80, v80, v112, v116\n"
    "ds_read_b64_tr_b16 a[92:93], v91 offset:256\n"
    "ds_read_b64_tr_b16 a[94:95], v91 offset:2304\n"
    "v_fma_f32 v81, v81, v113, v117\n"
    "v_cvt_pk_bf16_f32 v104, v80, v81\n"
    "v_mfma_f32_32x32x16_bf16 v[128:143], a[96:99], v[100:103], v[128:143]\n"
    "buffer_load_dwordx4 v30, s[40:43], s70 offen offset:2048 lds\n"
    "v_lshlrev_b32 v82, 16, v105\n"
    "ds_read_b64_tr_b16 a[160:161], v92 offset:57344\n"
    "ds_read_b64_tr_b16 a[162:163], v92 offset:59392\n"
    "v_and_b32 v83, 0xffff0000, v105\n"
    "v_fma_f32 v82, v82, v114, v118\n"
    "v_mfma_f32_32x32x16_bf16 v[144:159], a[100:103], v[100:103], v[144:159]\n"
    "ds_read_b64_tr_b16 a[96:97], v88 offset:8192\n"
    "v_fma_f32 v83, v83, v115, v119\n"
    "s_mov_b32 s73, 0x5000\n"
    "buffer_load_dwordx4 v[52:55], v1, s[36:39], s73 offen offset:1024\n"
    "ds_read_b64_tr_b16 a[98:99], v88 offset:10240\n"
    "v_cvt_pk_bf16_f32 v105, v82, v83\n"
    "ds_read_b128 v[112:115], v85 offset:1920\n"
    "v_mfma_f32_32x32x16_bf16 v[160:175], a[104:107], v[100:103], v[160:175]\n"
    "ds_read_b64_tr_b16 a[164:165], v93 offset:57344\n"
    "ds_read_b128 v[116:119], v85 offset:1936\n"
    "ds_read_b64_tr_b16 a[166:167], v93 offset:59392\n"
    "ds_read_b64_tr_b16 a[100:101], v89 offset:8192\n"
    "v_lshlrev_b32 v2, 16, v106\n"
    "v_and_b32 v3, 0xffff0000, v106\n"
    "v_mfma_f32_32x32x16_bf16 v[176:191], a[108:111], v[100:103], v[176:191]\n"
    "ds_read_b64_tr_b16 a[102:103], v89 offset:10240\n"
    "s_waitcnt lgkmcnt(5)\n"
    "v_fma_f32 v2, v2, v120, v124\n"
    "buffer_load_dwordx4 v31, s[40:43], s70 offen offset:3072 lds\n"
    "ds_read_b64_tr_b16 a[168:169], v94 offset:57344\n"
    "v_fma_f32 v3, v3, v121, v125\n"
    "v_cvt_pk_bf16_f32 v106, v2, v3\n"
    "v_mfma_f32_32x32x16_bf16 v[192:207], a[112:115], v[100:103], v[192:207]\n"
    "ds_read_b64_tr_b16 a[170:171], v94 offset:59392\n"
    "v_lshlrev_b32 v4, 16, v107\n"
    "ds_read_b64_tr_b16 a[104:105], v90 offset:8192\n"
    "ds_read_b64_tr_b16 a[106:107], v90 offset:10240\n"
    "v_and_b32 v5, 0xffff0000, v107\n"
    "v_fma_f32 v4, v4, v122, v126\n"
    "v_mfma_f32_32x32x16_bf16 v[208:223], a[116:119], v[100:103], v[208:223]\n"
    "ds_read_b64_tr_b16 a[172:173], v95 offset:57344\n"
    "v_fma_f32 v5, v5, v123, v127\n"
    "ds_read_b64_tr_b16 a[174:175], v95 offset:59392\n"
    "ds_read_b64_tr_b16 a[108:109], v91 offset:8192\n"
    "v_cvt_pk_bf16_f32 v107, v4, v5\n"
    "ds_read_b128 v[120:123], v85 offset:1952\n"
    "v_mfma_f32_32x32x16_bf16 v[224:239], a[120:123], v[100:103], v[224:239]\n"
    "ds_read_b64_tr_b16 a[110:111], v91 offset:10240\n"
    "ds_read_b128 v[124:127], v85 offset:1968\n"
    "ds_read_b64_tr_b16 a[176:177], v92 offset:57600\n"
    "ds_read_b64_tr_b16 a[178:179], v92 offset:59648\n"
    "v_lshlrev_b32 v80, 16, v108\n"
    "v_and_b32 v81, 0xffff0000, v108\n"
    "v_mfma_f32_32x32x16_bf16 v[240:255], a[124:127], v[100:103], v[240:255]\n"
    "s_mov_b32 m0, s62\n"
    "s_mov_b32 s70, 0x300000\n"
    "buffer_load_dwordx4 v28, s[40:43], s70 offen offset:0 lds\n"
    "s_waitcnt lgkmcnt(3)\n"
    "v_fma_f32 v80, v80, v112, v116\n"
    "ds_read_b64_tr_b16 a[112:113], v88 offset:8448\n"
    "ds_read_b64_tr_b16 a[114:115], v88 offset:10496\n"
    "v_fma_f32 v81, v81, v113, v117\n"
    "v_cvt_pk_bf16_f32 v108, v80, v81\n"
    "v_mfma_f32_32x32x16_bf16 v[128:143], a[128:131], v[104:107], v[128:143]\n"
    "ds_read_b64_tr_b16 a[180:181], v93 offset:57600\n"
    "v_lshlrev_b32 v82, 16, v109\n"
    "s_mov_b32 s73, 0x5000\n"
    "buffer_load_dwordx4 v[56:59], v1, s[36:39], s73 offen offset:2048\n"
    "ds_read_b64_tr_b16 a[182:183], v93 offset:59648\n"
    "v_and_b32 v83, 0xffff0000, v109\n"
    "v_fma_f32 v82, v82, v114, v118\n"
    "v_mfma_f32_32x32x16_bf16 v[144:159], a[132:135], v[104:107], v[144:159]\n"
    "ds_read_b64_tr_b16 a[116:117], v89 offset:8448\n"
    "v_fma_f32 v83, v83, v115, v119\n"
    "ds_read_b64_tr_b16 a[118:119], v89 offset:10496\n"
    "ds_read_b64_tr_b16 a[184:185], v94 offset:57600\n"
    "v_cvt_pk_bf16_f32 v109, v82, v83\n"
    "ds_read_b128 v[112:115], v85 offset:2048\n"
    "v_mfma_f32_32x32x16_bf16 v[160:175], a[136:139], v[104:107], v[160:175]\n"
    "ds_read_b64_tr_b16 a[186:187], v94 offset:59648\n"
    "ds_read_b128 v[116:119], v85 offset:2064\n"
    "buffer_load_dwordx4 v29, s[40:43], s70 offen offset:1024 lds\n"
    "ds_read_b64_tr_b16 a[120:121], v90 offset:8448\n"
    "v_lshlrev_b32 v2, 16, v110\n"
    "v_and_b32 v3, 0xffff0000, v110\n"
    "v_mfma_f32_32x32x16_bf16 v[176:191], a[140:143], v[104:107], v[176:191]\n"
    "ds_read_b64_tr_b16 a[122:123], v90 offset:10496\n"
    "s_waitcnt lgkmcnt(4)\n"
    "v_fma_f32 v2, v2, v120, v124\n"
    "ds_read_b64_tr_b16 a[188:189], v95 offset:57600\n"
    "ds_read_b64_tr_b16 a[190:191], v95 offset:59648\n"
    "v_fma_f32 v3, v3, v121, v125\n"
    "v_cvt_pk_bf16_f32 v110, v2, v3\n"
    "v_mfma_f32_32x32x16_bf16 v[192:207], a[144:147], v[104:107], v[192:207]\n"
    "ds_read_b64_tr_b16 a[124:125], v91 offset:8448\n"
    "v_lshlrev_b32 v4, 16, v111\n"
    "ds_read_b64_tr_b16 a[126:127], v91 offset:10496\n"
    "v_and_b32 v5, 0xffff0000, v111\n"
    "v_fma_f32 v4, v4, v122, v126\n"
    "v_fma_f32 v5, v5, v123, v127\n"
    "v_mfma_f32_32x32x16_bf16 v[208:223], a[148:151], v[104:107], v[208:223]\n"
    "v_cvt_pk_bf16_f32 v111, v4, v5\n"
    "ds_read_b128 v[120:123], v85 offset:2080\n"
    "ds_read_b128 v[124:127], v85 offset:2096\n"
    "s_waitcnt vmcnt(19)\n"
    "v_lshlrev_b32 v80, 16, v32\n"
    "v_and_b32 v81, 0xffff0000, v32\n"
    "s_waitcnt lgkmcnt(2)\n"
    "v_fma_f32 v80, v80, v112, v116\n"
    "v_mfma_f32_32x32x16_bf16 v[224:239], a[152:155], v[104:107], v[224:239]\n"
    "buffer_load_dwordx4 v30, s[40:43], s70 offen offset:2048 lds\n"
    "v_fma_f32 v81, v81, v113, v117\n"
    "v_cvt_pk_bf16_f32 v32, v80, v81\n"
    "v_lshlrev_b32 v82, 16, v33\n"
    "v_and_b32 v83, 0xffff0000, v33\n"
    "v_mfma_f32_32x32x16_bf16 v[240:255], a[156:159], v[104:107], v[240:255]\n"
    "v_fma_f32 v82, v82, v114, v118\n"
    "v_fma_f32 v83, v83, v115, v119\n"
    "s_mov_b32 s73, 0x5000\n"
    "buffer_load_dwordx4 v[60:63], v1, s[36:39], s73 offen offset:3072\n"
    "v_cvt_pk_bf16_f32 v33, v82, v83\n"
    "ds_read_b128 v[112:115], v85 offset:2176\n"
    "v_mfma_f32_32x32x16_bf16 v[128:143], a[160:163], v[108:111], v[128:143]\n"
    "ds_read_b128 v[116:119], v85 offset:2192\n"
    "v_lshlrev_b32 v2, 16, v34\n"
    "v_and_b32 v3, 0xffff0000, v34\n"
    "s_waitcnt lgkmcnt(2)\n"
    "v_fma_f32 v2, v2, v120, v124\n"
    "v_fma_f32 v3, v3, v121, v125\n"
    "v_cvt_pk_bf16_f32 v34, v2, v3\n"
    "v_mfma_f32_32x32x16_bf16 v[144:159], a[164:167], v[108:111], v[144:159]\n"
    "v_lshlrev_b32 v4, 16, v35\n"
    "v_and_b32 v5, 0xffff0000, v35\n"
    "buffer_load_dwordx4 v31, s[40:43], s70 offen offset:3072 lds\n"
    "v_fma_f32 v4, v4, v122, v126\n"
    "v_fma_f32 v5, v5, v123, v127\n"
    "v_mfma_f32_32x32x16_bf16 v[160:175], a[168:171], v[108:111], v[160:175]\n"
    "v_cvt_pk_bf16_f32 v35, v4, v5\n"
    "ds_read_b128 v[120:123], v85 offset:2208\n"
    "ds_read_b128 v[124:127], v85 offset:2224\n"
    "s_waitcnt vmcnt(19)\n"
    "v_lshlrev_b32 v80, 16, v36\n"
    "v_and_b32 v81, 0xffff0000, v36\n"
    "s_waitcnt lgkmcnt(2)\n"
    "v_fma_f32 v80, v80, v112, v116\n"
    "v_mfma_f32_32x32x16_bf16 v[176:191], a[172:175], v[108:111], v[176:191]\n"
    "v_fma_f32 v81, v81, v113, v117\n"
    "v_cvt_pk_bf16_f32 v36, v80, v81\n"
    "v_mfma_f32_32x32x16_bf16 v[192:207], a[176:179], v[108:111], v[192:207]\n"
    "s_waitcnt vmcnt(12) lgkmcnt(0)\n"
    "s_barrier\n"
    "ds_read_b64_tr_b16 a[128:129], v92 offset:0\n"
    "ds_read_b64_tr_b16 a[130:131], v92 offset:2048\n"
    "v_mfma_f32_32x32x16_bf16 v[208:223], a[180:183], v[108:111], v[208:223]\n"
    "ds_read_b64_tr_b16 a[132:133], v93 offset:0\n"
    "ds_read_b64_tr_b16 a[134:135], v93 offset:2048\n"
    "ds_read_b64_tr_b16 a[136:137], v94 offset:0\n"
    "v_mfma_f32_32x32x16_bf16 v[224:239], a[184:187], v[108:111], v[224:239]\n"
    "ds_read_b64_tr_b16 a[138:139], v94 offset:2048\n"
    "ds_read_b64_tr_b16 a[140:141], v95 offset:0\n"
    "ds_read_b64_tr_b16 a[142:143], v95 offset:2048\n"
    "v_mfma_f32_32x32x16_bf16 v[240:255], a[188:191], v[108:111], v[240:255]\n"
    "ds_read_b64_tr_b16 a[144:145], v92 offset:256\n"
    "ds_read_b64_tr_b16 a[146:147], v92 offset:2304\n"
    "ds_read_b64_tr_b16 a[148:149], v93 offset:256\n"
    "v_mfma_f32_32x32x16_bf16 v[128:143], a[64:67], v[32:35], v[128:143]\n"
    "ds_read_b64_tr_b16 a[150:151], v93 offset:2304\n"
    "v_lshlrev_b32 v82, 16, v37\n"
    "ds_read_b64_tr_b16 a[64:65], v88 offset:16384\n"
    "ds_read_b64_tr_b16 a[66:67], v88 offset:18432\n"
    "v_and_b32 v83, 0xffff0000, v37\n"
    "v_fma_f32 v82, v82, v114, v118\n"
    "v_mfma_f32_32x32x16_bf16 v[144:159], a[68:71], v[32:35], v[144:159]\n"
    "s_mov_b32 m0, s66\n"
    "s_mov_b32 s70, 0x340000\n"
    "buffer_load_dwordx4 v28, s[40:43], s70 offen offset:0 lds\n"
    "v_fma_f32 v83, v83, v115, v119\n"
    "ds_read_b64_tr_b16 a[68:69], v89 offset:16384\n"
    "ds_read_b64_tr_b16 a[70:71], v89 offset:18432\n"
    "v_cvt_pk_bf16_f32 v37, v82, v83\n"
    "ds_read_b128 v[112:115], v85 offset:2304\n"
    "v_mfma_f32_32x32x16_bf16 v[160:175], a[72:75], v[32:35], v[160:175]\n"
    "ds_read_b64_tr_b16 a[152:153], v94 offset:256\n"
    "ds_read_b128 v[116:119], v85 offset:2320\n"
    "s_mov_b32 s73, 0x6000\n"
    "buffer_load_dwordx4 v[64:67], v1, s[36:39], s73 offen offset:0\n"
    "ds_read_b64_tr_b16 a[72:73], v90 offset:16384\n"
    "v_lshlrev_b32 v2, 16, v38\n"
    "v_and_b32 v3, 0xffff0000, v38\n"
    "v_mfma_f32_32x32x16_bf16 v[176:191], a[76:79], v[32:35], v[176:191]\n"
    "ds_read_b64_tr_b16 a[74:75], v90 offset:18432\n"
    "v_fma_f32 v2, v2, v120, v124\n"
    "ds_read_b64_tr_b16 a[76:77], v91 offset:16384\n"
    "ds_read_b64_tr_b16 a[78:79], v91 offset:18432\n"
    "v_fma_f32 v3, v3, v121, v125\n"
    "v_cvt_pk_bf16_f32 v38, v2, v3\n"
    "v_mfma_f32_32x32x16_bf16 v[192:207], a[80:83], v[32:35], v[192:207]\n"
    "ds_read_b64_tr_b16 a[154:155], v94 offset:2304\n"
    "v_lshlrev_b32 v4, 16, v39\n"
    "buffer_load_dwordx4 v29, s[40:43], s70 offen offset:1024 lds\n"
    "ds_read_b64_tr_b16 a[80:81], v88 offset:16640\n"
    "v_and_b32 v5, 0xffff0000, v39\n"
    "v_fma_f32 v4, v4, v122, v126\n"
    "v_mfma_f32_32x32x16_bf16 v[208:223], a[84:87], v[32:35], v[208:223]\n"
    "ds_read_b64_tr_b16 a[82:83], v88 offset:18688\n"
    "v_fma_f32 v5, v5, v123, v127\n"
    "ds_read_b64_tr_b16 a[84:85], v89 offset:16640\n"
    "ds_read_b64_tr_b16 a[86:87], v89 offset:18688\n"
    "v_cvt_pk_bf16_f32 v39, v4, v5\n"
    "ds_read_b128 v[120:123], v85 offset:2336\n"
    "v_mfma_f32_32x32x16_bf16 v[224:239], a[88:91], v[32:35], v[224:239]\n"
    "ds_read_b64_tr_b16 a[156:157], v95 offset:256\n"
    "ds_read_b128 v[124:127], v85 offset:2352\n"
    "ds_read_b64_tr_b16 a[88:89], v90 offset:16640\n"
    "ds_read_b64_tr_b16 a[90:91], v90 offset:18688\n"
    "v_lshlrev_b32 v80, 16, v40\n"
    "v_and_b32 v81, 0xffff0000, v40\n"
    "v_mfma_f32_32x32x16_bf16 v[240:255], a[92:95], v[32:35], v[240:255]\n"
    "ds_read_b64_tr_b16 a[158:159], v95 offset:2304\n"
    "s_waitcnt lgkmcnt(5)\n"
    "v_fma_f32 v80, v80, v112, v116\n"
    "ds_read_b64_tr_b16 a[92:93], v91 offset:16640\n"
    "ds_read_b64_tr_b16 a[94:95], v91 offset:18688\n"
    "v_fma_f32 v81, v81, v113, v117\n"
    "v_cvt_pk_bf16_f32 v40, v80, v81\n"
    "v_mfma_f32_32x32x16_bf16 v[128:143], a[96:99], v[36:39], v[128:143]\n"
    "buffer_load_dwordx4 v30, s[40:43], s70 offen offset:2048 lds\n"
    "v_lshlrev_b32 v82, 16, v41\n"
    "ds_read_b64_tr_b16 a[160:161], v92 offset:8192\n"
    "ds_read_b64_tr_b16 a[162:163], v92 offset:10240\n"
    "v_and_b32 v83, 0xffff0000, v41\n"
    "v_fma_f32 v82, v82, v114, v118\n"
    "v_mfma_f32_32x32x16_bf16 v[144:159], a[100:103], v[36:39], v[144:159]\n"
    "ds_read_b64_tr_b16 a[96:97], v88 offset:24576\n"
    "v_fma_f32 v83, v83, v115, v119\n"
    "s_mov_b32 s73, 0x6000\n"
    "buffer_load_dwordx4 v[68:71], v1, s[36:39], s73 offen offset:1024\n"
    "ds_read_b64_tr_b16 a[98:99], v88 offset:26624\n"
    "v_cvt_pk_bf16_f32 v41, v82, v83\n"
    "ds_read_b128 v[112:115], v85 offset:2432\n"
    "v_mfma_f32_32x32x16_bf16 v[160:175], a[104:107], v[36:39], v[160:175]\n"
    "ds_read_b64_tr_b16 a[164:165], v93 offset:8192\n"
    "ds_read_b128 v[116:119], v85 offset:2448\n"
    "ds_read_b64_tr_b16 a[166:167], v93 offset:10240\n"
    "ds_read_b64_tr_b16 a[100:101], v89 offset:24576\n"
    "v_lshlrev_b32 v2, 16, v42\n"
    "v_and_b32 v3, 0xffff0000, v42\n"
    "v_mfma_f32_32x32x16_bf16 v[176:191], a[108:111], v[36:39], v[176:191]\n"
    "ds_read_b64_tr_b16 a[102:103], v89 offset:26624\n"
    "s_waitcnt lgkmcnt(5)\n"
    "v_fma_f32 v2, v2, v120, v124\n"
    "buffer_load_dwordx4 v31, s[40:43], s70 offen offset:3072 lds\n"
    "ds_read_b64_tr_b16 a[168:169], v94 offset:8192\n"
    "v_fma_f32 v3, v3, v121, v125\n"
    "v_cvt_pk_bf16_f32 v42, v2, v3\n"
    "v_mfma_f32_32x32x16_bf16 v[192:207], a[112:115], v[36:39], v[192:207]\n"
    "ds_read_b64_tr_b16 a[170:171], v94 offset:10240\n"
    "v_lshlrev_b32 v4, 16, v43\n"
    "ds_read_b64_tr_b16 a[104:105], v90 offset:24576\n"
    "ds_read_b64_tr_b16 a[106:107], v90 offset:26624\n"
    "v_and_b32 v5, 0xffff0000, v43\n"
    "v_fma_f32 v4, v4, v122, v126\n"
    "v_mfma_f32_32x32x16_bf16 v[208:223], a[116:119], v[36:39], v[208:223]\n"
    "ds_read_b64_tr_b16 a[172:173], v95 offset:8192\n"
    "v_fma_f32 v5, v5, v123, v127\n"
    "ds_read_b64_tr_b16 a[174:175], v95 offset:10240\n"
    "ds_read_b64_tr_b16 a[108:109], v91 offset:24576\n"
    "v_cvt_pk_bf16_f32 v43, v4, v5\n"
    "ds_read_b128 v[120:123], v85 offset:2464\n"
    "v_mfma_f32_32x32x16_bf16 v[224:239], a[120:123], v[36:39], v[224:239]\n"
    "ds_read_b64_tr_b16 a[110:111], v91 offset:26624\n"
    "ds_read_b128 v[124:127], v85 offset:2480\n"
    "ds_read_b64_tr_b16 a[176:177], v92 offset:8448\n"
    "ds_read_b64_tr_b16 a[178:179], v92 offset:10496\n"
    "v_lshlrev_b32 v80, 16, v44\n"
    "v_and_b32 v81, 0xffff0000, v44\n"
    "v_mfma_f32_32x32x16_bf16 v[240:255], a[124:127], v[36:39], v[240:255]\n"
    "s_mov_b32 m0, s63\n"
    "s_mov_b32 s68, 0\n"
    "buffer_load_dwordx4 v20, s[44:47], s68 offen offset:0 lds\n"
    "s_waitcnt lgkmcnt(3)\n"
    "v_fma_f32 v80, v80, v112, v116\n"
    "ds_read_b64_tr_b16 a[112:113], v88 offset:24832\n"
    "ds_read_b64_tr_b16 a[114:115], v88 offset:26880\n"
    "v_fma_f32 v81, v81, v113, v117\n"
    "v_cvt_pk_bf16_f32 v44, v80, v81\n"
    "v_mfma_f32_32x32x16_bf16 v[128:143], a[128:131], v[40:43], v[128:143]\n"
    "ds_read_b64_tr_b16 a[180:181], v93 offset:8448\n"
    "v_lshlrev_b32 v82, 16, v45\n"
    "s_mov_b32 s73, 0x6000\n"
    "buffer_load_dwordx4 v[72:75], v1, s[36:39], s73 offen offset:2048\n"
    "ds_read_b64_tr_b16 a[182:183], v93 offset:10496\n"
    "v_and_b32 v83, 0xffff0000, v45\n"
    "v_fma_f32 v82, v82, v114, v118\n"
    "v_mfma_f32_32x32x16_bf16 v[144:159], a[132:135], v[40:43], v[144:159]\n"
    "ds_read_b64_tr_b16 a[116:117], v89 offset:24832\n"
    "v_fma_f32 v83, v83, v115, v119\n"
    "ds_read_b64_tr_b16 a[118:119], v89 offset:26880\n"
    "ds_read_b64_tr_b16 a[184:185], v94 offset:8448\n"
    "v_cvt_pk_bf16_f32 v45, v82, v83\n"
    "ds_read_b128 v[112:115], v85 offset:2560\n"
    "v_mfma_f32_32x32x16_bf16 v[160:175], a[136:139], v[40:43], v[160:175]\n"
    "ds_read_b64_tr_b16 a[186:187], v94 offset:10496\n"
    "ds_read_b128 v[116:119], v85 offset:2576\n"
    "buffer_load_dwordx4 v21, s[44:47], s68 offen offset:1024 lds\n"
    "ds_read_b64_tr_b16 a[120:121], v90 offset:24832\n"
    "v_lshlrev_b32 v2, 16, v46\n"
    "v_and_b32 v3, 0xffff0000, v46\n"
    "v_mfma_f32_32x32x16_bf16 v[176:191], a[140:143], v[40:43], v[176:191]\n"
    "ds_read_b64_tr_b16 a[122:123], v90 offset:26880\n"
    "s_waitcnt lgkmcnt(4)\n"
    "v_fma_f32 v2, v2, v120, v124\n"
    "ds_read_b64_tr_b16 a[188:189], v95 offset:8448\n"
    "ds_read_b64_tr_b16 a[190:191], v95 offset:10496\n"
    "v_fma_f32 v3, v3, v121, v125\n"
    "v_cvt_pk_bf16_f32 v46, v2, v3\n"
    "v_mfma_f32_32x32x16_bf16 v[192:207], a[144:147], v[40:43], v[192:207]\n"
    "ds_read_b64_tr_b16 a[124:125], v91 offset:24832\n"
    "v_lshlrev_b32 v4, 16, v47\n"
    "ds_read_b64_tr_b16 a[126:127], v91 offset:26880\n"
    "v_and_b32 v5, 0xffff0000, v47\n"
    "v_fma_f32 v4, v4, v122, v126\n"
    "v_fma_f32 v5, v5, v123, v127\n"
    "v_mfma_f32_32x32x16_bf16 v[208:223], a[148:151], v[40:43], v[208:223]\n"
    "v_cvt_pk_bf16_f32 v47, v4, v5\n"
    "ds_read_b128 v[120:123], v85 offset:2592\n"
    "ds_read_b128 v[124:127], v85 offset:2608\n"
    "s_waitcnt vmcnt(19)\n"
    "v_lshlrev_b32 v80, 16, v48\n"
    "v_and_b32 v81, 0xffff0000, v48\n"
    "s_waitcnt lgkmcnt(2)\n"
    "v_fma_f32 v80, v80, v112, v116\n"
    "v_mfma_f32_32x32x16_bf16 v[224:239], a[152:155], v[40:43], v[224:239]\n"
    "buffer_load_dwordx4 v22, s[44:47], s68 offen offset:2048 lds\n"
    "v_fma_f32 v81, v81, v113, v117\n"
    "v_cvt_pk_bf16_f32 v48, v80, v81\n"
    "v_lshlrev_b32 v82, 16, v49\n"
    "v_and_b32 v83, 0xffff0000, v49\n"
    "v_mfma_f32_32x32x16_bf16 v[240:255], a[156:159], v[40:43], v[240:255]\n"
    "v_fma_f32 v82, v82, v114, v118\n"
    "v_fma_f32 v83, v83, v115, v119\n"
    "s_mov_b32 s73, 0x6000\n"
    "buffer_load_dwordx4 v[76:79], v1, s[36:39], s73 offen offset:3072\n"
    "v_cvt_pk_bf16_f32 v49, v82, v83\n"
    "ds_read_b128 v[112:115], v85 offset:2688\n"
    "v_mfma_f32_32x32x16_bf16 v[128:143], a[160:163], v[44:47], v[128:143]\n"
    "ds_read_b128 v[116:119], v85 offset:2704\n"
    "v_lshlrev_b32 v2, 16, v50\n"
    "v_and_b32 v3, 0xffff0000, v50\n"
    "s_waitcnt lgkmcnt(2)\n"
    "v_fma_f32 v2, v2, v120, v124\n"
    "v_fma_f32 v3, v3, v121, v125\n"
    "v_cvt_pk_bf16_f32 v50, v2, v3\n"
    "v_mfma_f32_32x32x16_bf16 v[144:159], a[164:167], v[44:47], v[144:159]\n"
    "v_lshlrev_b32 v4, 16, v51\n"
    "v_and_b32 v5, 0xffff0000, v51\n"
    "buffer_load_dwordx4 v23, s[44:47], s68 offen offset:3072 lds\n"
    "v_fma_f32 v4, v4, v122, v126\n"
    "v_fma_f32 v5, v5, v123, v127\n"
    "v_mfma_f32_32x32x16_bf16 v[160:175], a[168:171], v[44:47], v[160:175]\n"
    "v_cvt_pk_bf16_f32 v51, v4, v5\n"
    "ds_read_b128 v[120:123], v85 offset:2720\n"
    "ds_read_b128 v[124:127], v85 offset:2736\n"
    "s_waitcnt vmcnt(19)\n"
    "v_lshlrev_b32 v80, 16, v52\n"
    "v_and_b32 v81, 0xffff0000, v52\n"
    "s_waitcnt lgkmcnt(2)\n"
    "v_fma_f32 v80, v80, v112, v116\n"
    "v_mfma_f32_32x32x16_bf16 v[176:191], a[172:175], v[44:47], v[176:191]\n"
    "v_fma_f32 v81, v81, v113, v117\n"
    "v_cvt_pk_bf16_f32 v52, v80, v81\n"
    "v_mfma_f32_32x32x16_bf16 v[192:207], a[176:179], v[44:47], v[192:207]\n"
    "s_waitcnt vmcnt(12) lgkmcnt(0)\n"
    "s_barrier\n"
    "ds_read_b64_tr_b16 a[128:129], v92 offset:16384\n"
    "ds_read_b64_tr_b16 a[130:131], v92 offset:18432\n"
    "v_mfma_f32_32x32x16_bf16 v[208:223], a[180:183], v[44:47], v[208:223]\n"
    "ds_read_b64_tr_b16 a[132:133], v93 offset:16384\n"
    "ds_read_b64_tr_b16 a[134:135], v93 offset:18432\n"
    "ds_read_b64_tr_b16 a[136:137], v94 offset:16384\n"
    "v_mfma_f32_32x32x16_bf16 v[224:239], a[184:187], v[44:47], v[224:239]\n"
    "ds_read_b64_tr_b16 a[138:139], v94 offset:18432\n"
    "ds_read_b64_tr_b16 a[140:141], v95 offset:16384\n"
    "ds_read_b64_tr_b16 a[142:143], v95 offset:18432\n"
    "v_mfma_f32_32x32x16_bf16 v[240:255], a[188:191], v[44:47], v[240:255]\n"
    "ds_read_b64_tr_b16 a[144:145], v92 offset:16640\n"
    "ds_read_b64_tr_b16 a[146:147], v92 offset:18688\n"
    "ds_read_b64_tr_b16 a[148:149], v93 offset:16640\n"
    "v_mfma_f32_32x32x16_bf16 v[128:143], a[64:67], v[48:51], v[128:143]\n"
    "ds_read_b64_tr_b16 a[150:151], v93 offset:18688\n"
    "v_lshlrev_b32 v82, 16, v53\n"
    "ds_read_b64_tr_b16 a[64:65], v88 offset:32768\n"
    "ds_read_b64_tr_b16 a[66:67], v88 offset:34816\n"
    "v_and_b32 v83, 0xffff0000, v53\n"
    "v_fma_f32 v82, v82, v114, v118\n"
    "v_mfma_f32_32x32x16_bf16 v[144:159], a[68:71], v[48:51], v[144:159]\n"
    "s_mov_b32 m0, s60\n"
    "s_mov_b32 s68, 0x4000\n"
    "buffer_load_dwordx4 v20, s[44:47], s68 offen offset:0 lds\n"
    "v_fma_f32 v83, v83, v115, v119\n"
    "ds_read_b64_tr_b16 a[68:69], v89 offset:32768\n"
    "ds_read_b64_tr_b16 a[70:71], v89 offset:34816\n"
    "v_cvt_pk_bf16_f32 v53, v82, v83\n"
    "ds_read_b128 v[112:115], v85 offset:2816\n"
    "v_mfma_f32_32x32x16_bf16 v[160:175], a[72:75], v[48:51], v[160:175]\n"
    "ds_read_b64_tr_b16 a[152:153], v94 offset:16640\n"
    "ds_read_b128 v[116:119], v85 offset:2832\n"
    "ds_read_b64_tr_b16 a[72:73], v90 offset:32768\n"
    "ds_read_b64_tr_b16 a[74:75], v90 offset:34816\n"
    "v_lshlrev_b32 v2, 16, v54\n"
    "v_and_b32 v3, 0xffff0000, v54\n"
    "v_mfma_f32_32x32x16_bf16 v[176:191], a[76:79], v[48:51], v[176:191]\n"
    "ds_read_b64_tr_b16 a[154:155], v94 offset:18688\n"
    "v_fma_f32 v2, v2, v120, v124\n"
    "ds_read_b64_tr_b16 a[76:77], v91 offset:32768\n"
    "ds_read_b64_tr_b16 a[78:79], v91 offset:34816\n"
    "v_fma_f32 v3, v3, v121, v125\n"
    "v_cvt_pk_bf16_f32 v54, v2, v3\n"
    "v_mfma_f32_32x32x16_bf16 v[192:207], a[80:83], v[48:51], v[192:207]\n"
    "ds_read_b64_tr_b16 a[156:157], v95 offset:16640\n"
    "v_lshlrev_b32 v4, 16, v55\n"
    "buffer_load_dwordx4 v21, s[44:47], s68 offen offset:1024 lds\n"
    "ds_read_b64_tr_b16 a[80:81], v88 offset:33024\n"
    "v_and_b32 v5, 0xffff0000, v55\n"
    "v_fma_f32 v4, v4, v122, v126\n"
    "v_mfma_f32_32x32x16_bf16 v[208:223], a[84:87], v[48:51], v[208:223]\n"
    "ds_read_b64_tr_b16 a[82:83], v88 offset:35072\n"
    "v_fma_f32 v5, v5, v123, v127\n"
    "ds_read_b64_tr_b16 a[84:85], v89 offset:33024\n"
    "ds_read_b64_tr_b16 a[86:87], v89 offset:35072\n"
    "v_cvt_pk_bf16_f32 v55, v4, v5\n"
    "ds_read_b128 v[120:123], v85 offset:2848\n"
    "v_mfma_f32_32x32x16_bf16 v[224:239], a[88:91], v[48:51], v[224:239]\n"
    "ds_read_b64_tr_b16 a[158:159], v95 offset:18688\n"
    "ds_read_b128 v[124:127], v85 offset:2864\n"
    "ds_read_b64_tr_b16 a[88:89], v90 offset:33024\n"
    "ds_read_b64_tr_b16 a[90:91], v90 offset:35072\n"
    "v_lshlrev_b32 v80, 16, v56\n"
    "v_and_b32 v81, 0xffff0000, v56\n"
    "v_mfma_f32_32x32x16_bf16 v[240:255], a[92:95], v[48:51], v[240:255]\n"
    "ds_read_b64_tr_b16 a[160:161], v92 offset:24576\n"
    "s_waitcnt lgkmcnt(5)\n"
    "v_fma_f32 v80, v80, v112, v116\n"
    "ds_read_b64_tr_b16 a[92:93], v91 offset:33024\n"
    "ds_read_b64_tr_b16 a[94:95], v91 offset:35072\n"
    "v_fma_f32 v81, v81, v113, v117\n"
    "v_cvt_pk_bf16_f32 v56, v80, v81\n"
    "v_mfma_f32_32x32x16_bf16 v[128:143], a[96:99], v[52:55], v[128:143]\n"
    "buffer_load_dwordx4 v22, s[44:47], s68 offen offset:2048 lds\n"
    "v_lshlrev_b32 v82, 16, v57\n"
    "ds_read_b64_tr_b16 a[162:163], v92 offset:26624\n"
    "ds_read_b64_tr_b16 a[96:97], v88 offset:40960\n"
    "v_and_b32 v83, 0xffff0000, v57\n"
    "v_fma_f32 v82, v82, v114, v118\n"
    "v_mfma_f32_32x32x16_bf16 v[144:159], a[100:103], v[52:55], v[144:159]\n"
    "ds_read_b64_tr_b16 a[98:99], v88 offset:43008\n"
    "v_fma_f32 v83, v83, v115, v119\n"
    "ds_read_b64_tr_b16 a[164:165], v93 offset:24576\n"
    "ds_read_b64_tr_b16 a[166:167], v93 offset:26624\n"
    "v_cvt_pk_bf16_f32 v57, v82, v83\n"
    "ds_read_b128 v[112:115], v85 offset:2944\n"
    "v_mfma_f32_32x32x16_bf16 v[160:175], a[104:107], v[52:55], v[160:175]\n"
    "ds_read_b64_tr_b16 a[100:101], v89 offset:40960\n"
    "ds_read_b128 v[116:119], v85 offset:2960\n"
    "ds_read_b64_tr_b16 a[102:103], v89 offset:43008\n"
    "ds_read_b64_tr_b16 a[168:169], v94 offset:24576\n"
    "v_lshlrev_b32 v2, 16, v58\n"
    "v_and_b32 v3, 0xffff0000, v58\n"
    "v_mfma_f32_32x32x16_bf16 v[176:191], a[108:111], v[52:55], v[176:191]\n"
    "ds_read_b64_tr_b16 a[170:171], v94 offset:26624\n"
    "s_waitcnt lgkmcnt(5)\n"
    "v_fma_f32 v2, v2, v120, v124\n"
    "buffer_load_dwordx4 v23, s[44:47], s68 offen offset:3072 lds\n"
    "ds_read_b64_tr_b16 a[104:105], v90 offset:40960\n"
    "v_fma_f32 v3, v3, v121, v125\n"
    "v_cvt_pk_bf16_f32 v58, v2, v3\n"
    "v_mfma_f32_32x32x16_bf16 v[192:207], a[112:115], v[52:55], v[192:207]\n"
    "ds_read_b64_tr_b16 a[106:107], v90 offset:43008\n"
    "v_lshlrev_b32 v4, 16, v59\n"
    "ds_read_b64_tr_b16 a[172:173], v95 offset:24576\n"
    "ds_read_b64_tr_b16 a[174:175], v95 offset:26624\n"
    "v_and_b32 v5, 0xffff0000, v59\n"
    "v_fma_f32 v4, v4, v122, v126\n"
    "v_mfma_f32_32x32x16_bf16 v[208:223], a[116:119], v[52:55], v[208:223]\n"
    "ds_read_b64_tr_b16 a[108:109], v91 offset:40960\n"
    "v_fma_f32 v5, v5, v123, v127\n"
    "ds_read_b64_tr_b16 a[110:111], v91 offset:43008\n"
    "ds_read_b64_tr_b16 a[176:177], v92 offset:24832\n"
    "v_cvt_pk_bf16_f32 v59, v4, v5\n"
    "ds_read_b128 v[120:123], v85 offset:2976\n"
    "v_mfma_f32_32x32x16_bf16 v[224:239], a[120:123], v[52:55], v[224:239]\n"
    "ds_read_b64_tr_b16 a[178:179], v92 offset:26880\n"
    "ds_read_b128 v[124:127], v85 offset:2992\n"
    "ds_read_b64_tr_b16 a[112:113], v88 offset:41216\n"
    "ds_read_b64_tr_b16 a[114:115], v88 offset:43264\n"
    "v_lshlrev_b32 v80, 16, v60\n"
    "v_and_b32 v81, 0xffff0000, v60\n"
    "v_mfma_f32_32x32x16_bf16 v[240:255], a[124:127], v[52:55], v[240:255]\n"
    "s_mov_b32 m0, s64\n"
    "s_mov_b32 s69, 0\n"
    "buffer_load_dwordx4 v24, s[48:51], s69 offen offset:0 lds\n"
    "s_waitcnt lgkmcnt(3)\n"
    "v_fma_f32 v80, v80, v112, v116\n"
    "ds_read_b64_tr_b16 a[180:181], v93 offset:24832\n"
    "ds_read_b64_tr_b16 a[182:183], v93 offset:26880\n"
    "v_fma_f32 v81, v81, v113, v117\n"
    "v_cvt_pk_bf16_f32 v60, v80, v81\n"
    "v_mfma_f32_32x32x16_bf16 v[128:143], a[128:131], v[56:59], v[128:143]\n"
    "ds_read_b64_tr_b16 a[116:117], v89 offset:41216\n"
    "v_lshlrev_b32 v82, 16, v61\n"
    "ds_read_b64_tr_b16 a[118:119], v89 offset:43264\n"
    "ds_read_b64_tr_b16 a[184:185], v94 offset:24832\n"
    "v_and_b32 v83, 0xffff0000, v61\n"
    "v_fma_f32 v82, v82, v114, v118\n"
    "v_mfma_f32_32x32x16_bf16 v[144:159], a[132:135], v[56:59], v[144:159]\n"
    "ds_read_b64_tr_b16 a[186:187], v94 offset:26880\n"
    "v_fma_f32 v83, v83, v115, v119\n"
    "ds_read_b64_tr_b16 a[120:121], v90 offset:41216\n"
    "ds_read_b64_tr_b16 a[122:123], v90 offset:43264\n"
    "v_cvt_pk_bf16_f32 v61, v82, v83\n"
    "ds_read_b128 v[112:115], v85 offset:3072\n"
    "v_mfma_f32_32x32x16_bf16 v[160:175], a[136:139], v[56:59], v[160:175]\n"
    "ds_read_b64_tr_b16 a[188:189], v95 offset:24832\n"
    "ds_read_b128 v[116:119], v85 offset:3088\n"
    "buffer_load_dwordx4 v25, s[48:51], s69 offen offset:1024 lds\n"
    "ds_read_b64_tr_b16 a[190:191], v95 offset:26880\n"
    "v_lshlrev_b32 v2, 16, v62\n"
    "v_and_b32 v3, 0xffff0000, v62\n"
    "v_mfma_f32_32x32x16_bf16 v[176:191], a[140:143], v[56:59], v[176:191]\n"
    "ds_read_b64_tr_b16 a[124:125], v91 offset:41216\n"
    "s_waitcnt lgkmcnt(4)\n"
    "v_fma_f32 v2, v2, v120, v124\n"
    "ds_read_b64_tr_b16 a[126:127], v91 offset:43264\n"
    "v_fma_f32 v3, v3, v121, v125\n"
    "v_cvt_pk_bf16_f32 v62, v2, v3\n"
    "v_lshlrev_b32 v4, 16, v63\n"
    "v_mfma_f32_32x32x16_bf16 v[192:207], a[144:147], v[56:59], v[192:207]\n"
    "v_and_b32 v5, 0xffff0000, v63\n"
    "v_fma_f32 v4, v4, v122, v126\n"
    "v_fma_f32 v5, v5, v123, v127\n"
    "v_cvt_pk_bf16_f32 v63, v4, v5\n"
    "ds_read_b128 v[120:123], v85 offset:3104\n"
    "ds_read_b128 v[124:127], v85 offset:3120\n"
    "v_mfma_f32_32x32x16_bf16 v[208:223], a[148:151], v[56:59], v[208:223]\n"
    "s_waitcnt vmcnt(16)\n"
    "v_lshlrev_b32 v80, 16, v64\n"
    "v_and_b32 v81, 0xffff0000, v64\n"
    "s_waitcnt lgkmcnt(2)\n"
    "v_fma_f32 v80, v80, v112, v116\n"
    "v_fma_f32 v81, v81, v113, v117\n"
    "v_cvt_pk_bf16_f32 v64, v80, v81\n"
    "v_lshlrev_b32 v82, 16, v65\n"
    "v_mfma_f32_32x32x16_bf16 v[224:239], a[152:155], v[56:59], v[224:239]\n"
    "buffer_load_dwordx4 v26, s[48:51], s69 offen offset:2048 lds\n"
    "v_and_b32 v83, 0xffff0000, v65\n"
    "v_fma_f32 v82, v82, v114, v118\n"
    "v_fma_f32 v83, v83, v115, v119\n"
    "v_cvt_pk_bf16_f32 v65, v82, v83\n"
    "v_mfma_f32_32x32x16_bf16 v[240:255], a[156:159], v[56:59], v[240:255]\n"
    "ds_read_b128 v[112:115], v85 offset:3200\n"
    "ds_read_b128 v[116:119], v85 offset:3216\n"
    "v_lshlrev_b32 v2, 16, v66\n"
    "v_and_b32 v3, 0xffff0000, v66\n"
    "s_waitcnt lgkmcnt(2)\n"
    "v_fma_f32 v2, v2, v120, v124\n"
    "v_fma_f32 v3, v3, v121, v125\n"
    "v_mfma_f32_32x32x16_bf16 v[128:143], a[160:163], v[60:63], v[128:143]\n"
    "v_cvt_pk_bf16_f32 v66, v2, v3\n"
    "v_lshlrev_b32 v4, 16, v67\n"
    "v_and_b32 v5, 0xffff0000, v67\n"
    "v_fma_f32 v4, v4, v122, v126\n"
    "v_fma_f32 v5, v5, v123, v127\n"
    "v_cvt_pk_bf16_f32 v67, v4, v5\n"
    "v_mfma_f32_32x32x16_bf16 v[144:159], a[164:167], v[60:63], v[144:159]\n"
    "ds_read_b128 v[120:123], v85 offset:3232\n"
    "ds_read_b128 v[124:127], v85 offset:3248\n"
    "buffer_load_dwordx4 v27, s[48:51], s69 offen offset:3072 lds\n"
    "s_waitcnt vmcnt(15)\n"
    "v_lshlrev_b32 v80, 16, v68\n"
    "v_and_b32 v81, 0xffff0000, v68\n"
    "v_mfma_f32_32x32x16_bf16 v[160:175], a[168:171], v[60:63], v[160:175]\n"
    "s_waitcnt lgkmcnt(2)\n"
    "v_fma_f32 v80, v80, v112, v116\n"
    "v_fma_f32 v81, v81, v113, v117\n"
    "v_cvt_pk_bf16_f32 v68, v80, v81\n"
    "v_mfma_f32_32x32x16_bf16 v[176:191], a[172:175], v[60:63], v[176:191]\n"
    "v_mfma_f32_32x32x16_bf16 v[192:207], a[176:179], v[60:63], v[192:207]\n"
    "s_waitcnt vmcnt(8) lgkmcnt(0)\n"
    "s_barrier\n"
    "ds_read_b64_tr_b16 a[128:129], v92 offset:32768\n"
    "ds_read_b64_tr_b16 a[130:131], v92 offset:34816\n"
    "v_mfma_f32_32x32x16_bf16 v[208:223], a[180:183], v[60:63], v[208:223]\n"
    "ds_read_b64_tr_b16 a[132:133], v93 offset:32768\n"
    "ds_read_b64_tr_b16 a[134:135], v93 offset:34816\n"
    "ds_read_b64_tr_b16 a[136:137], v94 offset:32768\n"
    "v_mfma_f32_32x32x16_bf16 v[224:239], a[184:187], v[60:63], v[224:239]\n"
    "ds_read_b64_tr_b16 a[138:139], v94 offset:34816\n"
    "ds_read_b64_tr_b16 a[140:141], v95 offset:32768\n"
    "ds_read_b64_tr_b16 a[142:143], v95 offset:34816\n"
    "v_mfma_f32_32x32x16_bf16 v[240:255], a[188:191], v[60:63], v[240:255]\n"
    "ds_read_b64_tr_b16 a[144:145], v92 offset:33024\n"
    "ds_read_b64_tr_b16 a[146:147], v92 offset:35072\n"
    "ds_read_b64_tr_b16 a[148:149], v93 offset:33024\n"
    "v_mfma_f32_32x32x16_bf16 v[128:143], a[64:67], v[64:67], v[128:143]\n"
    "ds_read_b64_tr_b16 a[150:151], v93 offset:35072\n"
    "v_lshlrev_b32 v82, 16, v69\n"
    "ds_read_b128 a[64:67], v8 offset:49152\n"
    "ds_read_b64_tr_b16 a[152:153], v94 offset:33024\n"
    "v_and_b32 v83, 0xffff0000, v69\n"
    "v_fma_f32 v82, v82, v114, v118\n"
    "v_mfma_f32_32x32x16_bf16 v[144:159], a[68:71], v[64:67], v[144:159]\n"
    "s_mov_b32 m0, s61\n"
    "s_mov_b32 s68, 0x8000\n"
    "buffer_load_dwordx4 v20, s[44:47], s68 offen offset:0 lds\n"
    "v_fma_f32 v83, v83, v115, v119\n"
    "ds_read_b128 a[68:71], v8 offset:57344\n"
    "ds_read_b64_tr_b16 a[154:155], v94 offset:35072\n"
    "v_cvt_pk_bf16_f32 v69, v82, v83\n"
    "ds_read_b128 v[112:115], v85 offset:3328\n"
    "v_mfma_f32_32x32x16_bf16 v[160:175], a[72:75], v[64:67], v[160:175]\n"
    "ds_read_b64_tr_b16 a[156:157], v95 offset:33024\n"
    "ds_read_b128 v[116:119], v85 offset:3344\n"
    "ds_read_b128 a[72:75], v9 offset:49152\n"
    "ds_read_b64_tr_b16 a[158:159], v95 offset:35072\n"
    "v_lshlrev_b32 v2, 16, v70\n"
    "v_and_b32 v3, 0xffff0000, v70\n"
    "v_mfma_f32_32x32x16_bf16 v[176:191], a[76:79], v[64:67], v[176:191]\n"
    "ds_read_b64_tr_b16 a[160:161], v92 offset:40960\n"
    "v_fma_f32 v2, v2, v120, v124\n"
    "ds_read_b128 a[76:79], v9 offset:57344\n"
    "ds_read_b64_tr_b16 a[162:163], v92 offset:43008\n"
    "v_fma_f32 v3, v3, v121, v125\n"
    "v_cvt_pk_bf16_f32 v70, v2, v3\n"
    "v_mfma_f32_32x32x16_bf16 v[192:207], a[80:83], v[64:67], v[192:207]\n"
    "ds_read_b64_tr_b16 a[164:165], v93 offset:40960\n"
    "v_lshlrev_b32 v4, 16, v71\n"
    "buffer_load_dwordx4 v21, s[44:47], s68 offen offset:1024 lds\n"
    "ds_read_b128 a[80:83], v10 offset:49152\n"
    "v_and_b32 v5, 0xffff0000, v71\n"
    "v_fma_f32 v4, v4, v122, v126\n"
    "v_mfma_f32_32x32x16_bf16 v[208:223], a[84:87], v[64:67], v[208:223]\n"
    "ds_read_b64_tr_b16 a[166:167], v93 offset:43008\n"
    "v_fma_f32 v5, v5, v123, v127\n"
    "ds_read_b128 a[84:87], v10 offset:57344\n"
    "ds_read_b64_tr_b16 a[168:169], v94 offset:40960\n"
    "v_cvt_pk_bf16_f32 v71, v4, v5\n"
    "ds_read_b128 v[120:123], v85 offset:3360\n"
    "v_mfma_f32_32x32x16_bf16 v[224:239], a[88:91], v[64:67], v[224:239]\n"
    "ds_read_b64_tr_b16 a[170:171], v94 offset:43008\n"
    "ds_read_b128 v[124:127], v85 offset:3376\n"
    "ds_read_b128 a[88:91], v11 offset:49152\n"
    "ds_read_b64_tr_b16 a[172:173], v95 offset:40960\n"
    "v_lshlrev_b32 v80, 16, v72\n"
    "v_and_b32 v81, 0xffff0000, v72\n"
    "v_mfma_f32_32x32x16_bf16 v[240:255], a[92:95], v[64:67], v[240:255]\n"
    "ds_read_b64_tr_b16 a[174:175], v95 offset:43008\n"
    "s_waitcnt lgkmcnt(5)\n"
    "v_fma_f32 v80, v80, v112, v116\n"
    "ds_read_b128 a[92:95], v11 offset:57344\n"
    "ds_read_b64_tr_b16 a[176:177], v92 offset:41216\n"
    "v_fma_f32 v81, v81, v113, v117\n"
    "v_cvt_pk_bf16_f32 v72, v80, v81\n"
    "v_mfma_f32_32x32x16_bf16 v[128:143], a[96:99], v[68:71], v[128:143]\n"
    "buffer_load_dwordx4 v22, s[44:47], s68 offen offset:2048 lds\n"
    "v_lshlrev_b32 v82, 16, v73\n"
    "ds_read_b128 a[96:99], v8 offset:49408\n"
    "ds_read_b64_tr_b16 a[178:179], v92 offset:43264\n"
    "v_and_b32 v83, 0xffff0000, v73\n"
    "v_fma_f32 v82, v82, v114, v118\n"
    "v_mfma_f32_32x32x16_bf16 v[144:159], a[100:103], v[68:71], v[144:159]\n"
    "ds_read_b64_tr_b16 a[180:181], v93 offset:41216\n"
    "v_fma_f32 v83, v83, v115, v119\n"
    "ds_read_b128 a[100:103], v8 offset:57600\n"
    "ds_read_b64_tr_b16 a[182:183], v93 offset:43264\n"
    "v_cvt_pk_bf16_f32 v73, v82, v83\n"
    "ds_read_b128 v[112:115], v85 offset:3456\n"
    "v_mfma_f32_32x32x16_bf16 v[160:175], a[104:107], v[68:71], v[160:175]\n"
    "ds_read_b64_tr_b16 a[184:185], v94 offset:41216\n"
    "ds_read_b128 v[116:119], v85 offset:3472\n"
    "ds_read_b128 a[104:107], v9 offset:49408\n"
    "ds_read_b64_tr_b16 a[186:187], v94 offset:43264\n"
    "v_lshlrev_b32 v2, 16, v74\n"
    "v_and_b32 v3, 0xffff0000, v74\n"
    "v_mfma_f32_32x32x16_bf16 v[176:191], a[108:111], v[68:71], v[176:191]\n"
    "ds_read_b64_tr_b16 a[188:189], v95 offset:41216\n"
    "s_waitcnt lgkmcnt(5)\n"
    "v_fma_f32 v2, v2, v120, v124\n"
    "buffer_load_dwordx4 v23, s[44:47], s68 offen offset:3072 lds\n"
    "ds_read_b128 a[108:111], v9 offset:57600\n"
    "v_fma_f32 v3, v3, v121, v125\n"
    "v_cvt_pk_bf16_f32 v74, v2, v3\n"
    "v_mfma_f32_32x32x16_bf16 v[192:207], a[112:115], v[68:71], v[192:207]\n"
    "ds_read_b64_tr_b16 a[190:191], v95 offset:43264\n"
    "v_lshlrev_b32 v4, 16, v75\n"
    "ds_read_b128 a[112:115], v10 offset:49408\n"
    "v_and_b32 v5, 0xffff0000, v75\n"
    "v_fma_f32 v4, v4, v122, v126\n"
    "v_fma_f32 v5, v5, v123, v127\n"
    "v_mfma_f32_32x32x16_bf16 v[208:223], a[116:119], v[68:71], v[208:223]\n"
    "v_cvt_pk_bf16_f32 v75, v4, v5\n"
    "ds_read_b128 v[120:123], v85 offset:3488\n"
    "ds_read_b128 a[116:119], v10 offset:57600\n"
    "ds_read_b128 v[124:127], v85 offset:3504\n"
    "v_lshlrev_b32 v80, 16, v76\n"
    "v_and_b32 v81, 0xffff0000, v76\n"
    "v_mfma_f32_32x32x16_bf16 v[224:239], a[120:123], v[68:71], v[224:239]\n"
    "s_waitcnt lgkmcnt(3)\n"
    "v_fma_f32 v80, v80, v112, v116\n"
    "v_fma_f32 v81, v81, v113, v117\n"
    "ds_read_b128 a[120:123], v11 offset:49408\n"
    "v_cvt_pk_bf16_f32 v76, v80, v81\n"
    "v_lshlrev_b32 v82, 16, v77\n"
    "v_and_b32 v83, 0xffff0000, v77\n"
    "v_mfma_f32_32x32x16_bf16 v[240:255], a[124:127], v[68:71], v[240:255]\n"
    "s_mov_b32 m0, s65\n"
    "s_mov_b32 s69, 0x4000\n"
    "buffer_load_dwordx4 v24, s[48:51], s69 offen offset:0 lds\n"
    "ds_read_b128 a[124:127], v11 offset:57600\n"
    "v_fma_f32 v82, v82, v114, v118\n"
    "v_fma_f32 v83, v83, v115, v119\n"
    "v_cvt_pk_bf16_f32 v77, v82, v83\n"
    "v_mfma_f32_32x32x16_bf16 v[128:143], a[128:131], v[72:75], v[128:143]\n"
    "v_lshlrev_b32 v2, 16, v78\n"
    "v_and_b32 v3, 0xffff0000, v78\n"
    "s_waitcnt lgkmcnt(1)\n"
    "v_fma_f32 v2, v2, v120, v124\n"
    "v_fma_f32 v3, v3, v121, v125\n"
    "v_cvt_pk_bf16_f32 v78, v2, v3\n"
    "v_lshlrev_b32 v4, 16, v79\n"
    "v_mfma_f32_32x32x16_bf16 v[144:159], a[132:135], v[72:75], v[144:159]\n"
    "v_and_b32 v5, 0xffff0000, v79\n"
    "v_fma_f32 v4, v4, v122, v126\n"
    "v_fma_f32 v5, v5, v123, v127\n"
    "v_cvt_pk_bf16_f32 v79, v4, v5\n"
    "v_mfma_f32_32x32x16_bf16 v[160:175], a[136:139], v[72:75], v[160:175]\n"
    "buffer_load_dwordx4 v25, s[48:51], s69 offen offset:1024 lds\n"
    "v_mfma_f32_32x32x16_bf16 v[176:191], a[140:143], v[72:75], v[176:191]\n"
    "v_mfma_f32_32x32x16_bf16 v[192:207], a[144:147], v[72:75], v[192:207]\n"
    "v_mfma_f32_32x32x16_bf16 v[208:223], a[148:151], v[72:75], v[208:223]\n"
    "v_mfma_f32_32x32x16_bf16 v[224:239], a[152:155], v[72:75], v[224:239]\n"
    "buffer_load_dwordx4 v26, s[48:51], s69 offen offset:2048 lds\n"
    "v_mfma_f32_32x32x16_bf16 v[240:255], a[156:159], v[72:75], v[240:255]\n"
    "v_mfma_f32_32x32x16_bf16 v[128:143], a[160:163], v[76:79], v[128:143]\n"
    "v_mfma_f32_32x32x16_bf16 v[144:159], a[164:167], v[76:79], v[144:159]\n"
    "buffer_load_dwordx4 v27, s[48:51], s69 offen offset:3072 lds\n"
    "v_mfma_f32_32x32x16_bf16 v[160:175], a[168:171], v[76:79], v[160:175]\n"
    "v_mfma_f32_32x32x16_bf16 v[176:191], a[172:175], v[76:79], v[176:191]\n"
    "v_mfma_f32_32x32x16_bf16 v[192:207], a[176:179], v[76:79], v[192:207]\n"
    "s_waitcnt vmcnt(12) lgkmcnt(0)\n"
    "s_barrier\n"
    "v_mfma_f32_32x32x16_bf16 v[208:223], a[180:183], v[76:79], v[208:223]\n"
    "v_mfma_f32_32x32x16_bf16 v[224:239], a[184:187], v[76:79], v[224:239]\n"
    "v_mfma_f32_32x32x16_bf16 v[240:255], a[188:191], v[76:79], v[240:255]\n"
    "s_nop 7\n"
    "v_cvt_pk_bf16_f32 v128, v128, v129\n"
    "v_cvt_pk_bf16_f32 v129, v130, v131\n"
    "v_cvt_pk_bf16_f32 v130, v132, v133\n"
    "v_cvt_pk_bf16_f32 v131, v134, v135\n"
    "v_cvt_pk_bf16_f32 v132, v136, v137\n"
    "v_cvt_pk_bf16_f32 v133, v138, v139\n"
    "v_cvt_pk_bf16_f32 v134, v140, v141\n"
    "v_cvt_pk_bf16_f32 v135, v142, v143\n"
    "v_cvt_pk_bf16_f32 v144, v144, v145\n"
    "v_cvt_pk_bf16_f32 v145, v146, v147\n"
    "v_cvt_pk_bf16_f32 v146, v148, v149\n"
    "v_cvt_pk_bf16_f32 v147, v150, v151\n"
    "v_cvt_pk_bf16_f32 v148, v152, v153\n"
    "v_cvt_pk_bf16_f32 v149, v154, v155\n"
    "v_cvt_pk_bf16_f32 v150, v156, v157\n"
    "v_cvt_pk_bf16_f32 v151, v158, v159\n"
    "v_permlane16_swap_b32 v128, v132\n"
    "v_permlane16_swap_b32 v129, v133\n"
    "v_permlane16_swap_b32 v130, v134\n"
    "v_permlane16_swap_b32 v131, v135\n"
    "v_cvt_pk_bf16_f32 v160, v160, v161\n"
    "v_cvt_pk_bf16_f32 v161, v162, v163\n"
    "v_cvt_pk_bf16_f32 v162, v164, v165\n"
    "v_cvt_pk_bf16_f32 v163, v166, v167\n"
    "v_cvt_pk_bf16_f32 v164, v168, v169\n"
    "v_cvt_pk_bf16_f32 v165, v170, v171\n"
    "v_cvt_pk_bf16_f32 v166, v172, v173\n"
    "v_cvt_pk_bf16_f32 v167, v174, v175\n"
    "v_permlane16_swap_b32 v144, v148\n"
    "v_permlane16_swap_b32 v145, v149\n"
    "v_permlane16_swap_b32 v146, v150\n"
    "v_permlane16_swap_b32 v147, v151\n"
    "v_accvgpr_write_b32 a0, v128\n"
    "v_accvgpr_write_b32 a1, v129\n"
    "v_accvgpr_write_b32 a2, v130\n"
    "v_accvgpr_write_b32 a3, v131\n"
    "v_accvgpr_write_b32 a4, v132\n"
    "v_accvgpr_write_b32 a5, v133\n"
    "v_accvgpr_write_b32 a6, v134\n"
    "v_accvgpr_write_b32 a7, v135\n"
    "v_cvt_pk_bf16_f32 v176, v176, v177\n"
    "v_cvt_pk_bf16_f32 v177, v178, v179\n"
    "v_cvt_pk_bf16_f32 v178, v180, v181\n"
    "v_cvt_pk_bf16_f32 v179, v182, v183\n"
    "v_cvt_pk_bf16_f32 v180, v184, v185\n"
    "v_cvt_pk_bf16_f32 v181, v186, v187\n"
    "v_cvt_pk_bf16_f32 v182, v188, v189\n"
    "v_cvt_pk_bf16_f32 v183, v190, v191\n"
    "v_permlane16_swap_b32 v160, v164\n"
    "v_permlane16_swap_b32 v161, v165\n"
    "v_permlane16_swap_b32 v162, v166\n"
    "v_permlane16_swap_b32 v163, v167\n"
    "v_accvgpr_write_b32 a8, v144\n"
    "v_accvgpr_write_b32 a9, v145\n"
    "v_accvgpr_write_b32 a10, v146\n"
    "v_accvgpr_write_b32 a11, v147\n"
    "v_accvgpr_write_b32 a12, v148\n"
    "v_accvgpr_write_b32 a13, v149\n"
    "v_accvgpr_write_b32 a14, v150\n"
    "v_accvgpr_write_b32 a15, v151\n"
    "v_cvt_pk_bf16_f32 v192, v192, v193\n"
    "v_cvt_pk_bf16_f32 v193, v194, v195\n"
    "v_cvt_pk_bf16_f32 v194, v196, v197\n"
    "v_cvt_pk_bf16_f32 v195, v198, v199\n"
    "v_cvt_pk_bf16_f32 v196, v200, v201\n"
    "v_cvt_pk_bf16_f32 v197, v202, v203\n"
    "v_cvt_pk_bf16_f32 v198, v204, v205\n"
    "v_cvt_pk_bf16_f32 v199, v206, v207\n"
    "v_permlane16_swap_b32 v176, v180\n"
    "v_permlane16_swap_b32 v177, v181\n"
    "v_permlane16_swap_b32 v178, v182\n"
    "v_permlane16_swap_b32 v179, v183\n"
    "v_accvgpr_write_b32 a16, v160\n"
    "v_accvgpr_write_b32 a17, v161\n"
    "v_accvgpr_write_b32 a18, v162\n"
    "v_accvgpr_write_b32 a19, v163\n"
    "v_accvgpr_write_b32 a20, v164\n"
    "v_accvgpr_write_b32 a21, v165\n"
    "v_accvgpr_write_b32 a22, v166\n"
    "v_accvgpr_write_b32 a23, v167\n"
    "v_cvt_pk_bf16_f32 v208, v208, v209\n"
    "v_cvt_pk_bf16_f32 v209, v210, v211\n"
    "v_cvt_pk_bf16_f32 v210, v212, v213\n"
    "v_cvt_pk_bf16_f32 v211, v214, v215\n"
    "v_cvt_pk_bf16_f32 v212, v216, v217\n"
    "v_cvt_pk_bf16_f32 v213, v218, v219\n"
    "v_cvt_pk_bf16_f32 v214, v220, v221\n"
    "v_cvt_pk_bf16_f32 v215, v222, v223\n"
    "v_permlane16_swap_b32 v192, v196\n"
    "v_permlane16_swap_b32 v193, v197\n"
    "v_permlane16_swap_b32 v194, v198\n"
    "v_permlane16_swap_b32 v195, v199\n"
    "v_accvgpr_write_b32 a24, v176\n"
    "v_accvgpr_write_b32 a25, v177\n"
    "v_accvgpr_write_b32 a26, v178\n"
    "v_accvgpr_write_b32 a27, v179\n"
    "v_accvgpr_write_b32 a28, v180\n"
    "v_accvgpr_write_b32 a29, v181\n"
    "v_accvgpr_write_b32 a30, v182\n"
    "v_accvgpr_write_b32 a31, v183\n"
    "v_cvt_pk_bf16_f32 v224, v224, v225\n"
    "v_cvt_pk_bf16_f32 v225, v226, v227\n"
    "v_cvt_pk_bf16_f32 v226, v228, v229\n"
    "v_cvt_pk_bf16_f32 v227, v230, v231\n"
    "v_cvt_pk_bf16_f32 v228, v232, v233\n"
    "v_cvt_pk_bf16_f32 v229, v234, v235\n"
    "v_cvt_pk_bf16_f32 v230, v236, v237\n"
    "v_cvt_pk_bf16_f32 v231, v238, v239\n"
    "v_permlane16_swap_b32 v208, v212\n"
    "v_permlane16_swap_b32 v209, v213\n"
    "v_permlane16_swap_b32 v210, v214\n"
    "v_permlane16_swap_b32 v211, v215\n"
    "v_accvgpr_write_b32 a32, v192\n"
    "v_accvgpr_write_b32 a33, v193\n"
    "v_accvgpr_write_b32 a34, v194\n"
    "v_accvgpr_write_b32 a35, v195\n"
    "v_accvgpr_write_b32 a36, v196\n"
    "v_accvgpr_write_b32 a37, v197\n"
    "v_accvgpr_write_b32 a38, v198\n"
    "v_accvgpr_write_b32 a39, v199\n"
    "v_cvt_pk_bf16_f32 v240, v240, v241\n"
    "v_cvt_pk_bf16_f32 v241, v242, v243\n"
    "v_cvt_pk_bf16_f32 v242, v244, v245\n"
    "v_cvt_pk_bf16_f32 v243, v246, v247\n"
    "v_cvt_pk_bf16_f32 v244, v248, v249\n"
    "v_cvt_pk_bf16_f32 v245, v250, v251\n"
    "v_cvt_pk_bf16_f32 v246, v252, v253\n"
    "v_cvt_pk_bf16_f32 v247, v254, v255\n"
    "v_permlane16_swap_b32 v224, v228\n"
    "v_permlane16_swap_b32 v225, v229\n"
    "v_permlane16_swap_b32 v226, v230\n"
    "v_permlane16_swap_b32 v227, v231\n"
    "v_accvgpr_write_b32 a40, v208\n"
    "v_accvgpr_write_b32 a41, v209\n"
    "v_accvgpr_write_b32 a42, v210\n"
    "v_accvgpr_write_b32 a43, v211\n"
    "v_accvgpr_write_b32 a44, v212\n"
    "v_accvgpr_write_b32 a45, v213\n"
    "v_accvgpr_write_b32 a46, v214\n"
    "v_accvgpr_write_b32 a47, v215\n"
    "s_nop 1\n"
    "v_permlane16_swap_b32 v240, v244\n"
    "v_permlane16_swap_b32 v241, v245\n"
    "v_permlane16_swap_b32 v242, v246\n"
    "v_permlane16_swap_b32 v243, v247\n"
    "v_accvgpr_write_b32 a48, v224\n"
    "v_accvgpr_write_b32 a49, v225\n"
    "v_accvgpr_write_b32 a50, v226\n"
    "v_accvgpr_write_b32 a51, v227\n"
    "v_accvgpr_write_b32 a52, v228\n"
    "v_accvgpr_write_b32 a53, v229\n"
    "v_accvgpr_write_b32 a54, v230\n"
    "v_accvgpr_write_b32 a55, v231\n"
    "v_accvgpr_write_b32 a56, v240\n"
    "v_accvgpr_write_b32 a57, v241\n"
    "v_accvgpr_write_b32 a58, v242\n"
    "v_accvgpr_write_b32 a59, v243\n"
    "v_accvgpr_write_b32 a60, v244\n"
    "v_accvgpr_write_b32 a61, v245\n"
    "v_accvgpr_write_b32 a62, v246\n"
    "v_accvgpr_write_b32 a63, v247\n"
    "s_nop 1\n"
    "v_mfma_f32_16x16x32_bf16 v[32:35], a[64:67], a[0:3], 0\n"
    "v_mfma_f32_16x16x32_bf16 v[40:43], a[64:67], a[4:7], 0\n"
    "v_mfma_f32_16x16x32_bf16 v[36:39], a[68:71], a[0:3], 0\n"
    "s_mov_b32 m0, s62\n"
    "s_mov_b32 s68, 0xc000\n"
    "buffer_load_dwordx4 v20, s[44:47], s68 offen offset:0 lds\n"
    "s_mov_b64 s[56:57], %[yp]\n"
    "s_mov_b32 s58, 0x7000\n"
    "s_mov_b32 s59, 0x20000\n"
    "v_mfma_f32_16x16x32_bf16 v[44:47], a[68:71], a[4:7], 0\n"
    "ds_read_b128 a[64:67], v8 offset:0\n"
    "v_bfe_u32 v95, v0, 2, 2\n"
    "v_mfma_f32_16x16x32_bf16 v[32:35], a[72:75], a[8:11], v[32:35]\n"
    "ds_read_b128 a[68:71], v8 offset:8192\n"
    "v_lshrrev_b32 v92, 5, v0\n"
    "v_mfma_f32_16x16x32_bf16 v[40:43], a[72:75], a[12:15], v[40:43]\n"
    "v_lshl_add_u32 v92, v92, 3, v95\n"
    "v_mfma_f32_16x16x32_bf16 v[36:39], a[76:79], a[8:11], v[36:39]\n"
    "buffer_load_dwordx4 v21, s[44:47], s68 offen offset:1024 lds\n"
    "v_mfma_f32_16x16x32_bf16 v[44:47], a[76:79], a[12:15], v[44:47]\n"
    "ds_read_b128 a[72:75], v9 offset:0\n"
    "v_lshl_add_u32 v93, v92, 7, v86\n"
    "v_mfma_f32_16x16x32_bf16 v[32:35], a[80:83], a[16:19], v[32:35]\n"
    "ds_read_b128 a[76:79], v9 offset:8192\n"
    "v_add_u32 v93, %[lb], v93\n"
    "v_mfma_f32_16x16x32_bf16 v[40:43], a[80:83], a[20:23], v[40:43]\n"
    "v_lshrrev_b32 v92, 1, v95\n"
    "v_mfma_f32_16x16x32_bf16 v[36:39], a[84:87], a[16:19], v[36:39]\n"
    "buffer_load_dwordx4 v22, s[44:47], s68 offen offset:2048 lds\n"
    "v_mfma_f32_16x16x32_bf16 v[44:47], a[84:87], a[20:23], v[44:47]\n"
    "ds_read_b128 a[80:83], v10 offset:0\n"
    "v_xor_b32 v94, 0, v92\n"
    "v_mfma_f32_16x16x32_bf16 v[32:35], a[88:91], a[24:27], v[32:35]\n"
    "ds_read_b128 a[84:87], v10 offset:8192\n"
    "v_lshl_add_u32 v88, v94, 6, v93\n"
    "v_mfma_f32_16x16x32_bf16 v[40:43], a[88:91], a[28:31], v[40:43]\n"
    "v_add_u32 v90, 0x10000, v88\n"
    "v_mfma_f32_16x16x32_bf16 v[36:39], a[92:95], a[24:27], v[36:39]\n"
    "buffer_load_dwordx4 v23, s[44:47], s68 offen offset:3072 lds\n"
    "v_mfma_f32_16x16x32_bf16 v[44:47], a[92:95], a[28:31], v[44:47]\n"
    "ds_read_b128 a[88:91], v11 offset:0\n"
    "v_xor_b32 v94, 1, v92\n"
    "v_mfma_f32_16x16x32_bf16 v[32:35], a[96:99], a[32:35], v[32:35]\n"
    "ds_read_b128 a[92:95], v11 offset:8192\n"
    "v_lshl_add_u32 v89, v94, 6, v93\n"
    "v_mfma_f32_16x16x32_bf16 v[40:43], a[96:99], a[36:39], v[40:43]\n"
    "v_add_u32 v91, 0x10000, v89\n"
    "v_mfma_f32_16x16x32_bf16 v[36:39], a[100:103], a[32:35], v[36:39]\n"
    "s_mov_b32 m0, s66\n"
    "s_mov_b32 s69, 0x8000\n"
    "buffer_load_dwordx4 v24, s[48:51], s69 offen offset:0 lds\n"
    "v_mfma_f32_16x16x32_bf16 v[44:47], a[100:103], a[36:39], v[44:47]\n"
    "ds_read_b128 a[96:99], v8 offset:256\n"
    "v_lshrrev_b32 v92, 3, v0\n"
    "s_lshl_b32 s72, %[w], 5\n"
    "v_mfma_f32_16x16x32_bf16 v[32:35], a[104:107], a[40:43], v[32:35]\n"
    "ds_read_b128 a[100:103], v8 offset:8448\n"
    "v_add_u32 v92, s72, v92\n"
    "v_mfma_f32_16x16x32_bf16 v[40:43], a[104:107], a[44:47], v[40:43]\n"
    "v_mul_u32_u24 v92, 0x380, v92\n"
    "v_mfma_f32_16x16x32_bf16 v[36:39], a[108:111], a[40:43], v[36:39]\n"
    "buffer_load_dwordx4 v25, s[48:51], s69 offen offset:1024 lds\n"
    "v_mfma_f32_16x16x32_bf16 v[44:47], a[108:111], a[44:47], v[44:47]\n"
    "ds_read_b128 a[104:107], v9 offset:256\n"
    "v_bfe_u32 v93, v0, 4, 1\n"
    "v_mfma_f32_16x16x32_bf16 v[32:35], a[112:115], a[48:51], v[32:35]\n"
    "ds_read_b128 a[108:111], v9 offset:8448\n"
    "v_lshlrev_b32 v93, 2, v93\n"
    "v_mfma_f32_16x16x32_bf16 v[40:43], a[112:115], a[52:55], v[40:43]\n"
    "v_and_b32 v94, 7, v0\n"
    "v_mfma_f32_16x16x32_bf16 v[36:39], a[116:119], a[48:51], v[36:39]\n"
    "ds_read_b128 a[112:115], v10 offset:256\n"
    "v_xor_b32 v93, v93, v94\n"
    "v_mfma_f32_16x16x32_bf16 v[44:47], a[116:119], a[52:55], v[44:47]\n"
    "buffer_load_dwordx4 v26, s[48:51], s69 offen offset:2048 lds\n"
    "v_mfma_f32_16x16x32_bf16 v[32:35], a[120:123], a[56:59], v[32:35]\n"
    "ds_read_b128 a[116:119], v10 offset:8448\n"
    "v_lshl_add_u32 v28, v93, 4, v92\n"
    "v_mfma_f32_16x16x32_bf16 v[40:43], a[120:123], a[60:63], v[40:43]\n"
    "v_add_u32 v29, 0x1800, v28\n"
    "v_mfma_f32_16x16x32_bf16 v[36:39], a[124:127], a[56:59], v[36:39]\n"
    "ds_read_b128 a[120:123], v11 offset:256\n"
    "v_add_u32 v30, 0x3000, v28\n"
    "v_add_u32 v31, 0x4800, v28\n"
    "v_mfma_f32_16x16x32_bf16 v[44:47], a[124:127], a[60:63], v[44:47]\n"
    "buffer_load_dwordx4 v27, s[48:51], s69 offen offset:3072 lds\n"
    "ds_read_b128 a[124:127], v11 offset:8448\n"
    "s_mov_b32 m0, s63\n"
    "s_mov_b32 s68, 0x10000\n"
    "buffer_load_dwordx4 v20, s[44:47], s68 offen offset:0 lds\n"
    "s_nop 7\n"
    "v_max3_f32 v82, v32, v33, v34\n"
    "v_max3_f32 v83, v40, v41, v42\n"
    "v_max3_f32 v82, v82, v35, v36\n"
    "v_max3_f32 v83, v83, v43, v44\n"
    "v_max3_f32 v82, v82, v37, v38\n"
    "v_max3_f32 v83, v83, v45, v46\n"
    "v_max_f32 v82, v82, v39\n"
    "v_max_f32 v83, v83, v47\n"
    "s_nop 1\n"
    "v_mov_b32 v92, v82\n"
    "v_mov_b32 v93, v83\n"
    "s_nop 1\n"
    "v_permlane16_swap_b32 v82, v92\n"
    "v_permlane16_swap_b32 v83, v93\n"
    "s_nop 1\n"
    "v_max_f32 v82, v82, v92\n"
    "v_max_f32 v83, v83, v93\n"
    "v_mov_b32 v92, v82\n"
    "v_mov_b32 v93, v83\n"
    "s_nop 1\n"
    "v_permlane32_swap_b32 v82, v92\n"
    "v_permlane32_swap_b32 v83, v93\n"
    "s_nop 1\n"
    "v_max_f32 v92, v82, v92\n"
    "v_max_f32 v93, v83, v93\n"
    "v_add_f32 v92, 0x42c80000, v92\n"
    "v_ceil_f32 v92, v92\n"
    "v_add_f32 v93, 0x42c80000, v93\n"
    "v_ceil_f32 v93, v93\n"
    "v_sub_f32 v32, v32, v92\n"
    "v_sub_f32 v33, v33, v92\n"
    "v_sub_f32 v34, v34, v92\n"
    "v_sub_f32 v35, v35, v92\n"
    "v_sub_f32 v36, v36, v92\n"
    "v_sub_f32 v37, v37, v92\n"
    "v_sub_f32 v38, v38, v92\n"
    "v_sub_f32 v39, v39, v92\n"
    "v_sub_f32 v40, v40, v93\n"
    "v_sub_f32 v41, v41, v93\n"
    "v_sub_f32 v42, v42, v93\n"
    "v_sub_f32 v43, v43, v93\n"
    "v_sub_f32 v44, v44, v93\n"
    "v_sub_f32 v45, v45, v93\n"
    "v_sub_f32 v46, v46, v93\n"
    "v_sub_f32 v47, v47, v93\n"
    "v_sub_f32 v64, 0, v92\n"
    "v_sub_f32 v65, 0, v92\n"
    "v_sub_f32 v66, 0, v92\n"
    "v_sub_f32 v67, 0, v92\n"
    "v_sub_f32 v68, 0, v93\n"
    "v_sub_f32 v69, 0, v93\n"
    "v_sub_f32 v70, 0, v93\n"
    "v_sub_f32 v71, 0, v93\n"
    "v_mov_b32 v80, 0\n"
    "v_mov_b32 v81, 0\n"
    "buffer_load_dwordx4 v21, s[44:47], s68 offen offset:1024 lds\n"
    "buffer_load_dwordx4 v22, s[44:47], s68 offen offset:2048 lds\n"
    "buffer_load_dwordx4 v23, s[44:47], s68 offen offset:3072 lds\n"
    "s_waitcnt vmcnt(16) lgkmcnt(0)\n"
    "s_barrier\n"
    "ds_read_b64_tr_b16 a[128:129], v12 offset:0\n"
    "ds_read_b64_tr_b16 a[130:131], v12 offset:8192\n"
    "ds_read_b64_tr_b16 a[132:133], v13 offset:0\n"
    "ds_read_b64_tr_b16 a[134:135], v13 offset:8192\n"
    "ds_read_b64_tr_b16 a[136:137], v14 offset:0\n"
    "ds_read_b64_tr_b16 a[138:139], v14 offset:8192\n"
    "ds_read_b64_tr_b16 a[140:141], v15 offset:0\n"
    "v_mfma_f32_16x16x32_bf16 v[48:51], a[64:67], a[0:3], v[64:67]\n"
    "ds_read_b64_tr_b16 a[142:143], v15 offset:8192\n"
    "v_exp_f32 v32, v32\n"
    "v_mfma_f32_16x16x32_bf16 v[56:59], a[64:67], a[4:7], v[68:71]\n"
    "ds_read_b64_tr_b16 a[144:145], v16 offset:0\n"
    "v_exp_f32 v33, v33\n"
    "v_mfma_f32_16x16x32_bf16 v[52:55], a[68:71], a[0:3], v[64:67]\n"
    "s_mov_b32 m0, s67\n"
    "s_mov_b32 s69, 0xc000\n"
    "buffer_load_dwordx4 v24, s[48:51], s69 offen offset:0 lds\n"
    "v_mfma_f32_16x16x32_bf16 v[60:63], a[68:71], a[4:7], v[68:71]\n"
    "ds_read_b64_tr_b16 a[146:147], v16 offset:8192\n"
    "v_exp_f32 v34, v34\n"
    "v_mfma_f32_16x16x32_bf16 v[48:51], a[72:75], a[8:11], v[48:51]\n"
    "ds_read_b64_tr_b16 a[148:149], v17 offset:0\n"
    "v_exp_f32 v35, v35\n"
    "v_mfma_f32_16x16x32_bf16 v[56:59], a[72:75], a[12:15], v[56:59]\n"
    "s_mov_b32 s72, %[pf]\n"
    "buffer_load_dwordx4 v[96:99], v1, s[52:55], s72 offen offset:0\n"
    "v_mfma_f32_16x16x32_bf16 v[52:55], a[76:79], a[8:11], v[52:55]\n"
    "ds_read_b64_tr_b16 a[150:151], v17 offset:8192\n"
    "v_exp_f32 v36, v36\n"
    "v_mfma_f32_16x16x32_bf16 v[60:63], a[76:79], a[12:15], v[60:63]\n"
    "ds_read_b64_tr_b16 a[152:153], v18 offset:0\n"
    "v_exp_f32 v37, v37\n"
    "v_mfma_f32_16x16x32_bf16 v[48:51], a[80:83], a[16:19], v[48:51]\n"
    "ds_read_b64_tr_b16 a[154:155], v18 offset:8192\n"
    "v_exp_f32 v38, v38\n"
    "v_mfma_f32_16x16x32_bf16 v[56:59], a[80:83], a[20:23], v[56:59]\n"
    "buffer_load_dwordx4 v25, s[48:51], s69 offen offset:1024 lds\n"
    "v_mfma_f32_16x16x32_bf16 v[52:55], a[84:87], a[16:19], v[52:55]\n"
    "ds_read_b64_tr_b16 a[156:157], v19 offset:0\n"
    "v_exp_f32 v39, v39\n"
    "v_mfma_f32_16x16x32_bf16 v[60:63], a[84:87], a[20:23], v[60:63]\n"
    "ds_read_b64_tr_b16 a[158:159], v19 offset:8192\n"
    "v_cvt_pk_bf16_f32 v72, v32, v33\n"
    "v_mfma_f32_16x16x32_bf16 v[48:51], a[88:91], a[24:27], v[48:51]\n"
    "ds_read_b64_tr_b16 a[160:161], v12 offset:256\n"
    "v_cvt_pk_bf16_f32 v73, v34, v35\n"
    "v_mfma_f32_16x16x32_bf16 v[56:59], a[88:91], a[28:31], v[56:59]\n"
    "ds_read_b64_tr_b16 a[162:163], v12 offset:8448\n"
    "v_cvt_pk_bf16_f32 v74, v36, v37\n"
    "v_mfma_f32_16x16x32_bf16 v[52:55], a[92:95], a[24:27], v[52:55]\n"
    "ds_read_b64_tr_b16 a[164:165], v13 offset:256\n"
    "v_cvt_pk_bf16_f32 v75, v38, v39\n"
    "v_mfma_f32_16x16x32_bf16 v[60:63], a[92:95], a[28:31], v[60:63]\n"
    "ds_read_b64_tr_b16 a[166:167], v13 offset:8448\n"
    "v_add_f32 v80, v80, v32\n"
    "v_mfma_f32_16x16x32_bf16 v[48:51], a[96:99], a[32:35], v[48:51]\n"
    "buffer_load_dwordx4 v26, s[48:51], s69 offen offset:2048 lds\n"
    "v_mfma_f32_16x16x32_bf16 v[56:59], a[96:99], a[36:39], v[56:59]\n"
    "ds_read_b64_tr_b16 a[168:169], v14 offset:256\n"
    "v_add_f32 v80, v80, v33\n"
    "v_mfma_f32_16x16x32_bf16 v[52:55], a[100:103], a[32:35], v[52:55]\n"
    "ds_read_b64_tr_b16 a[170:171], v14 offset:8448\n"
    "v_add_f32 v80, v80, v34\n"
    "v_mfma_f32_16x16x32_bf16 v[60:63], a[100:103], a[36:39], v[60:63]\n"
    "buffer_load_dwordx4 v[100:103], v1, s[52:55], s72 offen offset:1024\n"
    "v_mfma_f32_16x16x32_bf16 v[48:51], a[104:107], a[40:43], v[48:51]\n"
    "ds_read_b64_tr_b16 a[172:173], v15 offset:256\n"
    "v_add_f32 v80, v80, v35\n"
    "v_mfma_f32_16x16x32_bf16 v[56:59], a[104:107], a[44:47], v[56:59]\n"
    "ds_read_b64_tr_b16 a[174:175], v15 offset:8448\n"
    "v_exp_f32 v40, v40\n"
    "v_mfma_f32_16x16x32_bf16 v[52:55], a[108:111], a[40:43], v[52:55]\n"
    "ds_read_b64_tr_b16 a[176:177], v16 offset:256\n"
    "v_exp_f32 v41, v41\n"
    "v_mfma_f32_16x16x32_bf16 v[60:63], a[108:111], a[44:47], v[60:63]\n"
    "buffer_load_dwordx4 v27, s[48:51], s69 offen offset:3072 lds\n"
    "v_mfma_f32_16x16x32_bf16 v[48:51], a[112:115], a[48:51], v[48:51]\n"
    "ds_read_b64_tr_b16 a[178:179], v16 offset:8448\n"
    "v_add_f32 v80, v80, v36\n"
    "v_mfma_f32_16x16x32_bf16 v[56:59], a[112:115], a[52:55], v[56:59]\n"
    "ds_read_b64_tr_b16 a[180:181], v17 offset:256\n"
    "v_exp_f32 v42, v42\n"
    "v_mfma_f32_16x16x32_bf16 v[52:55], a[116:119], a[48:51], v[52:55]\n"
    "ds_read_b64_tr_b16 a[182:183], v17 offset:8448\n"
    "v_exp_f32 v43, v43\n"
    "v_mfma_f32_16x16x32_bf16 v[60:63], a[116:119], a[52:55], v[60:63]\n"
    "ds_read_b64_tr_b16 a[184:185], v18 offset:256\n"
    "v_add_f32 v80, v80, v37\n"
    "v_mfma_f32_16x16x32_bf16 v[48:51], a[120:123], a[56:59], v[48:51]\n"
    "ds_read_b64_tr_b16 a[186:187], v18 offset:8448\n"
    "v_exp_f32 v44, v44\n"
    "v_mfma_f32_16x16x32_bf16 v[56:59], a[120:123], a[60:63], v[56:59]\n"
    "ds_read_b64_tr_b16 a[188:189], v19 offset:256\n"
    "v_exp_f32 v45, v45\n"
    "v_mfma_f32_16x16x32_bf16 v[52:55], a[124:127], a[56:59], v[52:55]\n"
    "s_mov_b32 m0, s60\n"
    "s_mov_b32 s68, 0x14000\n"
    "buffer_load_dwordx4 v20, s[44:47], s68 offen offset:0 lds\n"
    "v_mfma_f32_16x16x32_bf16 v[60:63], a[124:127], a[60:63], v[60:63]\n"
    "ds_read_b64_tr_b16 a[190:191], v19 offset:8448\n"
    "v_add_f32 v80, v80, v38\n"
    "s_waitcnt lgkmcnt(2)\n"
    "v_mfma_f32_16x16x32_bf16 v[128:131], a[128:131], v[72:75], 0\n"
    "ds_read_b128 a[64:67], v8 offset:16384\n"
    "v_exp_f32 v46, v46\n"
    "v_mfma_f32_16x16x32_bf16 v[136:139], a[132:135], v[72:75], 0\n"
    "buffer_load_dwordx4 v[104:107], v1, s[52:55], s72 offen offset:2048\n"
    "v_mfma_f32_16x16x32_bf16 v[144:147], a[136:139], v[72:75], 0\n"
    "ds_read_b128 a[68:71], v8 offset:24576\n"
    "v_exp_f32 v47, v47\n"
    "v_mfma_f32_16x16x32_bf16 v[152:155], a[140:143], v[72:75], 0\n"
    "ds_read_b128 a[72:75], v9 offset:16384\n"
    "v_add_f32 v80, v80, v39\n"
    "v_mfma_f32_16x16x32_bf16 v[160:163], a[144:147], v[72:75], 0\n"
    "ds_read_b128 a[76:79], v9 offset:24576\n"
    "v_cvt_pk_bf16_f32 v76, v40, v41\n"
    "v_mfma_f32_16x16x32_bf16 v[168:171], a[148:151], v[72:75], 0\n"
    "buffer_load_dwordx4 v21, s[44:47], s68 offen offset:1024 lds\n"
    "v_mfma_f32_16x16x32_bf16 v[176:179], a[152:155], v[72:75], 0\n"
    "ds_read_b128 a[80:83], v10 offset:16384\n"
    "v_cvt_pk_bf16_f32 v77, v42, v43\n"
    "v_mfma_f32_16x16x32_bf16 v[184:187], a[156:159], v[72:75], 0\n"
    "ds_read_b128 a[84:87], v10 offset:24576\n"
    "v_cvt_pk_bf16_f32 v78, v44, v45\n"
    "v_mfma_f32_16x16x32_bf16 v[192:195], a[160:163], v[72:75], 0\n"
    "ds_read_b128 a[88:91], v11 offset:16384\n"
    "v_cvt_pk_bf16_f32 v79, v46, v47\n"
    "v_mfma_f32_16x16x32_bf16 v[200:203], a[164:167], v[72:75], 0\n"
    "ds_read_b128 a[92:95], v11 offset:24576\n"
    "v_max3_f32 v82, v48, v49, v50\n"
    "v_mfma_f32_16x16x32_bf16 v[208:211], a[168:171], v[72:75], 0\n"
    "ds_read_b128 a[96:99], v8 offset:16640\n"
    "v_add_f32 v81, v81, v40\n"
    "v_mfma_f32_16x16x32_bf16 v[216:219], a[172:175], v[72:75], 0\n"
    "ds_read_b128 a[100:103], v8 offset:24832\n"
    "v_max3_f32 v83, v56, v57, v58\n"
    "v_mfma_f32_16x16x32_bf16 v[224:227], a[176:179], v[72:75], 0\n"
    "buffer_load_dwordx4 v22, s[44:47], s68 offen offset:2048 lds\n"
    "v_mfma_f32_16x16x32_bf16 v[232:235], a[180:183], v[72:75], 0\n"
    "ds_read_b128 a[104:107], v9 offset:16640\n"
    "v_add_f32 v81, v81, v41\n"
    "v_mfma_f32_16x16x32_bf16 v[240:243], a[184:187], v[72:75], 0\n"
    "ds_read_b128 a[108:111], v9 offset:24832\n"
    "v_max3_f32 v82, v82, v51, v52\n"
    "s_waitcnt lgkmcnt(2)\n"
    "v_mfma_f32_16x16x32_bf16 v[248:251], a[188:191], v[72:75], 0\n"
    "ds_read_b128 a[112:115], v10 offset:16640\n"
    "v_add_f32 v81, v81, v42\n"
    "v_mfma_f32_16x16x32_bf16 v[132:135], a[128:131], v[76:79], 0\n"
    "ds_read_b128 a[116:119], v10 offset:24832\n"
    "v_max3_f32 v83, v83, v59, v60\n"
    "v_mfma_f32_16x16x32_bf16 v[140:143], a[132:135], v[76:79], 0\n"
    "ds_read_b128 a[120:123], v11 offset:16640\n"
    "v_add_f32 v81, v81, v43\n"
    "v_mfma_f32_16x16x32_bf16 v[148:151], a[136:139], v[76:79], 0\n"
    "buffer_load_dwordx4 v[108:111], v1, s[52:55], s72 offen offset:3072\n"
    "v_mfma_f32_16x16x32_bf16 v[156:159], a[140:143], v[76:79], 0\n"
    "ds_read_b128 a[124:127], v11 offset:24832\n"
    "v_max3_f32 v82, v82, v53, v54\n"
    "v_mfma_f32_16x16x32_bf16 v[164:167], a[144:147], v[76:79], 0\n"
    "buffer_load_dwordx4 v23, s[44:47], s68 offen offset:3072 lds\n"
    "v_mfma_f32_16x16x32_bf16 v[172:175], a[148:151], v[76:79], 0\n"
    "v_add_f32 v81, v81, v44\n"
    "v_mfma_f32_16x16x32_bf16 v[180:183], a[152:155], v[76:79], 0\n"
    "v_max3_f32 v83, v83, v61, v62\n"
    "v_mfma_f32_16x16x32_bf16 v[188:191], a[156:159], v[76:79], 0\n"
    "v_add_f32 v81, v81, v45\n"
    "v_mfma_f32_16x16x32_bf16 v[196:199], a[160:163], v[76:79], 0\n"
    "v_max3_f32 v82, v82, v55, v63\n"
    "v_mfma_f32_16x16x32_bf16 v[204:207], a[164:167], v[76:79], 0\n"
    "s_waitcnt vmcnt(20) lgkmcnt(0)\n"
    "s_barrier\n"
    "ds_read_b64_tr_b16 a[128:129], v12 offset:16384\n"
    "v_mfma_f32_16x16x32_bf16 v[212:215], a[168:171], v[76:79], 0\n"
    "ds_read_b64_tr_b16 a[130:131], v12 offset:24576\n"
    "v_add_f32 v81, v81, v46\n"
    "v_mfma_f32_16x16x32_bf16 v[220:223], a[172:175], v[76:79], 0\n"
    "ds_read_b64_tr_b16 a[132:133], v13 offset:16384\n"
    "v_max_f32 v82, v82, v83\n"
    "v_mfma_f32_16x16x32_bf16 v[228:231], a[176:179], v[76:79], 0\n"
    "ds_read_b64_tr_b16 a[134:135], v13 offset:24576\n"
    "v_add_f32 v81, v81, v47\n"
    "v_mfma_f32_16x16x32_bf16 v[236:239], a[180:183], v[76:79], 0\n"
    "ds_read_b64_tr_b16 a[136:137], v14 offset:16384\n"
    "v_cmp_lt_f32 vcc, s71, v82\n"
    "v_mfma_f32_16x16x32_bf16 v[244:247], a[184:187], v[76:79], 0\n"
    "ds_read_b64_tr_b16 a[138:139], v14 offset:24576\n"
    "v_mfma_f32_16x16x32_bf16 v[252:255], a[188:191], v[76:79], 0\n"
    "ds_read_b64_tr_b16 a[140:141], v15 offset:16384\n"
    "s_cbranch_vccnz LR8_%=\n"
    "LB8_%=:\n"
    "v_mfma_f32_16x16x32_bf16 v[32:35], a[64:67], a[0:3], v[64:67]\n"
    "ds_read_b64_tr_b16 a[142:143], v15 offset:24576\n"
    "v_exp_f32 v48, v48\n"
    "v_mfma_f32_16x16x32_bf16 v[40:43], a[64:67], a[4:7], v[68:71]\n"
    "ds_read_b64_tr_b16 a[144:145], v16 offset:16384\n"
    "v_exp_f32 v49, v49\n"
    "v_mfma_f32_16x16x32_bf16 v[36:39], a[68:71], a[0:3], v[64:67]\n"
    "s_mov_b32 m0, s64\n"
    "s_add_u32 s69, s69, 0x4000\n"
    "buffer_load_dwordx4 v24, s[48:51], s69 offen offset:0 lds\n"
    "v_mfma_f32_16x16x32_bf16 v[44:47], a[68:71], a[4:7], v[68:71]\n"
    "ds_read_b64_tr_b16 a[146:147], v16 offset:24576\n"
    "v_exp_f32 v50, v50\n"
    "v_mfma_f32_16x16x32_bf16 v[32:35], a[72:75], a[8:11], v[32:35]\n"
    "ds_read_b64_tr_b16 a[148:149], v17 offset:16384\n"
    "v_exp_f32 v51, v51\n"
    "v_mfma_f32_16x16x32_bf16 v[40:43], a[72:75], a[12:15], v[40:43]\n"
    "ds_read_b64_tr_b16 a[150:151], v17 offset:24576\n"
    "v_exp_f32 v52, v52\n"
    "v_mfma_f32_16x16x32_bf16 v[36:39], a[76:79], a[8:11], v[36:39]\n"
    "ds_read_b64_tr_b16 a[152:153], v18 offset:16384\n"
    "v_exp_f32 v53, v53\n"
    "v_mfma_f32_16x16x32_bf16 v[44:47], a[76:79], a[12:15], v[44:47]\n"
    "ds_read_b64_tr_b16 a[154:155], v18 offset:24576\n"
    "v_exp_f32 v54, v54\n"
    "v_mfma_f32_16x16x32_bf16 v[32:35], a[80:83], a[16:19], v[32:35]\n"
    "ds_read_b64_tr_b16 a[156:157], v19 offset:16384\n"
    "v_exp_f32 v55, v55\n"
    "v_mfma_f32_16x16x32_bf16 v[40:43], a[80:83], a[20:23], v[40:43]\n"
    "buffer_load_dwordx4 v25, s[48:51], s69 offen offset:1024 lds\n"
    "v_mfma_f32_16x16x32_bf16 v[36:39], a[84:87], a[16:19], v[36:39]\n"
    "ds_read_b64_tr_b16 a[158:159], v19 offset:24576\n"
    "v_cvt_pk_bf16_f32 v72, v48, v49\n"
    "v_mfma_f32_16x16x32_bf16 v[44:47], a[84:87], a[20:23], v[44:47]\n"
    "ds_read_b64_tr_b16 a[160:161], v12 offset:16640\n"
    "v_cvt_pk_bf16_f32 v73, v50, v51\n"
    "v_mfma_f32_16x16x32_bf16 v[32:35], a[88:91], a[24:27], v[32:35]\n"
    "ds_read_b64_tr_b16 a[162:163], v12 offset:24832\n"
    "v_cvt_pk_bf16_f32 v74, v52, v53\n"
    "v_mfma_f32_16x16x32_bf16 v[40:43], a[88:91], a[28:31], v[40:43]\n"
    "ds_read_b64_tr_b16 a[164:165], v13 offset:16640\n"
    "v_cvt_pk_bf16_f32 v75, v54, v55\n"
    "v_mfma_f32_16x16x32_bf16 v[36:39], a[92:95], a[24:27], v[36:39]\n"
    "ds_read_b64_tr_b16 a[166:167], v13 offset:24832\n"
    "v_add_f32 v80, v80, v48\n"
    "v_mfma_f32_16x16x32_bf16 v[44:47], a[92:95], a[28:31], v[44:47]\n"
    "ds_read_b64_tr_b16 a[168:169], v14 offset:16640\n"
    "v_add_f32 v80, v80, v49\n"
    "v_mfma_f32_16x16x32_bf16 v[32:35], a[96:99], a[32:35], v[32:35]\n"
    "buffer_load_dwordx4 v26, s[48:51], s69 offen offset:2048 lds\n"
    "v_mfma_f32_16x16x32_bf16 v[40:43], a[96:99], a[36:39], v[40:43]\n"
    "ds_read_b64_tr_b16 a[170:171], v14 offset:24832\n"
    "v_add_f32 v80, v80, v50\n"
    "v_mfma_f32_16x16x32_bf16 v[36:39], a[100:103], a[32:35], v[36:39]\n"
    "ds_read_b64_tr_b16 a[172:173], v15 offset:16640\n"
    "v_add_f32 v80, v80, v51\n"
    "v_mfma_f32_16x16x32_bf16 v[44:47], a[100:103], a[36:39], v[44:47]\n"
    "ds_read_b64_tr_b16 a[174:175], v15 offset:24832\n"
    "v_exp_f32 v56, v56\n"
    "v_mfma_f32_16x16x32_bf16 v[32:35], a[104:107], a[40:43], v[32:35]\n"
    "ds_read_b64_tr_b16 a[176:177], v16 offset:16640\n"
    "v_exp_f32 v57, v57\n"
    "v_mfma_f32_16x16x32_bf16 v[40:43], a[104:107], a[44:47], v[40:43]\n"
    "ds_read_b64_tr_b16 a[178:179], v16 offset:24832\n"
    "v_add_f32 v80, v80, v52\n"
    "v_mfma_f32_16x16x32_bf16 v[36:39], a[108:111], a[40:43], v[36:39]\n"
    "ds_read_b64_tr_b16 a[180:181], v17 offset:16640\n"
    "v_exp_f32 v58, v58\n"
    "v_mfma_f32_16x16x32_bf16 v[44:47], a[108:111], a[44:47], v[44:47]\n"
    "buffer_load_dwordx4 v27, s[48:51], s69 offen offset:3072 lds\n"
    "v_mfma_f32_16x16x32_bf16 v[32:35], a[112:115], a[48:51], v[32:35]\n"
    "ds_read_b64_tr_b16 a[182:183], v17 offset:24832\n"
    "v_exp_f32 v59, v59\n"
    "v_mfma_f32_16x16x32_bf16 v[40:43], a[112:115], a[52:55], v[40:43]\n"
    "ds_read_b64_tr_b16 a[184:185], v18 offset:16640\n"
    "v_add_f32 v80, v80, v53\n"
    "v_mfma_f32_16x16x32_bf16 v[36:39], a[116:119], a[48:51], v[36:39]\n"
    "ds_read_b64_tr_b16 a[186:187], v18 offset:24832\n"
    "v_exp_f32 v60, v60\n"
    "v_mfma_f32_16x16x32_bf16 v[44:47], a[116:119], a[52:55], v[44:47]\n"
    "ds_read_b64_tr_b16 a[188:189], v19 offset:16640\n"
    "v_exp_f32 v61, v61\n"
    "v_mfma_f32_16x16x32_bf16 v[32:35], a[120:123], a[56:59], v[32:35]\n"
    "ds_read_b64_tr_b16 a[190:191], v19 offset:24832\n"
    "v_add_f32 v80, v80, v54\n"
    "v_mfma_f32_16x16x32_bf16 v[40:43], a[120:123], a[60:63], v[40:43]\n"
    "ds_read_b128 a[64:67], v8 offset:32768\n"
    "v_exp_f32 v62, v62\n"
    "v_mfma_f32_16x16x32_bf16 v[36:39], a[124:127], a[56:59], v[36:39]\n"
    "s_mov_b32 m0, s61\n"
    "s_add_u32 s68, s68, 0x4000\n"
    "buffer_load_dwordx4 v20, s[44:47], s68 offen offset:0 lds\n"
    "v_mfma_f32_16x16x32_bf16 v[44:47], a[124:127], a[60:63], v[44:47]\n"
    "ds_read_b128 a[68:71], v8 offset:40960\n"
    "v_exp_f32 v63, v63\n"
    "s_waitcnt lgkmcnt(2)\n"
    "v_mfma_f32_16x16x32_bf16 v[128:131], a[128:131], v[72:75], v[128:131]\n"
    "ds_read_b128 a[72:75], v9 offset:32768\n"
    "v_add_f32 v80, v80, v55\n"
    "v_mfma_f32_16x16x32_bf16 v[136:139], a[132:135], v[72:75], v[136:139]\n"
    "ds_read_b128 a[76:79], v9 offset:40960\n"
    "v_cvt_pk_bf16_f32 v76, v56, v57\n"
    "v_mfma_f32_16x16x32_bf16 v[144:147], a[136:139], v[72:75], v[144:147]\n"
    "ds_read_b128 a[80:83], v10 offset:32768\n"
    "v_cvt_pk_bf16_f32 v77, v58, v59\n"
    "v_mfma_f32_16x16x32_bf16 v[152:155], a[140:143], v[72:75], v[152:155]\n"
    "ds_read_b128 a[84:87], v10 offset:40960\n"
    "v_cvt_pk_bf16_f32 v78, v60, v61\n"
    "v_mfma_f32_16x16x32_bf16 v[160:163], a[144:147], v[72:75], v[160:163]\n"
    "ds_read_b128 a[88:91], v11 offset:32768\n"
    "v_cvt_pk_bf16_f32 v79, v62, v63\n"
    "v_mfma_f32_16x16x32_bf16 v[168:171], a[148:151], v[72:75], v[168:171]\n"
    "buffer_load_dwordx4 v21, s[44:47], s68 offen offset:1024 lds\n"
    "v_mfma_f32_16x16x32_bf16 v[176:179], a[152:155], v[72:75], v[176:179]\n"
    "ds_read_b128 a[92:95], v11 offset:40960\n"
    "v_max3_f32 v82, v32, v33, v34\n"
    "v_mfma_f32_16x16x32_bf16 v[184:187], a[156:159], v[72:75], v[184:187]\n"
    "ds_read_b128 a[96:99], v8 offset:33024\n"
    "v_add_f32 v81, v81, v56\n"
    "v_mfma_f32_16x16x32_bf16 v[192:195], a[160:163], v[72:75], v[192:195]\n"
    "ds_read_b128 a[100:103], v8 offset:41216\n"
    "v_max3_f32 v83, v40, v41, v42\n"
    "v_mfma_f32_16x16x32_bf16 v[200:203], a[164:167], v[72:75], v[200:203]\n"
    "ds_read_b128 a[104:107], v9 offset:33024\n"
    "v_add_f32 v81, v81, v57\n"
    "v_mfma_f32_16x16x32_bf16 v[208:211], a[168:171], v[72:75], v[208:211]\n"
    "ds_read_b128 a[108:111], v9 offset:41216\n"
    "v_max3_f32 v82, v82, v35, v36\n"
    "v_mfma_f32_16x16x32_bf16 v[216:219], a[172:175], v[72:75], v[216:219]\n"
    "ds_read_b128 a[112:115], v10 offset:33024\n"
    "v_add_f32 v81, v81, v58\n"
    "v_mfma_f32_16x16x32_bf16 v[224:227], a[176:179], v[72:75], v[224:227]\n"
    "buffer_load_dwordx4 v22, s[44:47], s68 offen offset:2048 lds\n"
    "v_mfma_f32_16x16x32_bf16 v[232:235], a[180:183], v[72:75], v[232:235]\n"
    "ds_read_b128 a[116:119], v10 offset:41216\n"
    "v_max3_f32 v83, v83, v43, v44\n"
    "v_mfma_f32_16x16x32_bf16 v[240:243], a[184:187], v[72:75], v[240:243]\n"
    "ds_read_b128 a[120:123], v11 offset:33024\n"
    "v_add_f32 v81, v81, v59\n"
    "v_mfma_f32_16x16x32_bf16 v[248:251], a[188:191], v[72:75], v[248:251]\n"
    "ds_read_b128 a[124:127], v11 offset:41216\n"
    "v_max3_f32 v82, v82, v37, v38\n"
    "v_mfma_f32_16x16x32_bf16 v[132:135], a[128:131], v[76:79], v[132:135]\n"
    "v_add_f32 v81, v81, v60\n"
    "v_mfma_f32_16x16x32_bf16 v[140:143], a[132:135], v[76:79], v[140:143]\n"
    "v_max3_f32 v83, v83, v45, v46\n"
    "v_mfma_f32_16x16x32_bf16 v[148:151], a[136:139], v[76:79], v[148:151]\n"
    "v_add_f32 v81, v81, v61\n"
    "v_mfma_f32_16x16x32_bf16 v[156:159], a[140:143], v[76:79], v[156:159]\n"
    "buffer_load_dwordx4 v23, s[44:47], s68 offen offset:3072 lds\n"
    "v_mfma_f32_16x16x32_bf16 v[164:167], a[144:147], v[76:79], v[164:167]\n"
    "v_max3_f32 v82, v82, v39, v47\n"
    "v_mfma_f32_16x16x32_bf16 v[172:175], a[148:151], v[76:79], v[172:175]\n"
    "v_add_f32 v81, v81, v62\n"
    "v_mfma_f32_16x16x32_bf16 v[180:183], a[152:155], v[76:79], v[180:183]\n"
    "v_max_f32 v82, v82, v83\n"
    "v_mfma_f32_16x16x32_bf16 v[188:191], a[156:159], v[76:79], v[188:191]\n"
    "v_add_f32 v81, v81, v63\n"
    "v_mfma_f32_16x16x32_bf16 v[196:199], a[160:163], v[76:79], v[196:199]\n"
    "v_cmp_lt_f32 vcc, s71, v82\n"
    "v_mfma_f32_16x16x32_bf16 v[204:207], a[164:167], v[76:79], v[204:207]\n"
    "s_waitcnt vmcnt(20) lgkmcnt(0)\n"
    "s_barrier\n"
    "LOOP_%=:\n"
    "ds_read_b64_tr_b16 a[128:129], v12 offset:32768\n"
    "v_mfma_f32_16x16x32_bf16 v[212:215], a[168:171], v[76:79], v[212:215]\n"
    "ds_read_b64_tr_b16 a[130:131], v12 offset:40960\n"
    "v_mfma_f32_16x16x32_bf16 v[220:223], a[172:175], v[76:79], v[220:223]\n"
    "ds_read_b64_tr_b16 a[132:133], v13 offset:32768\n"
    "v_mfma_f32_16x16x32_bf16 v[228:231], a[176:179], v[76:79], v[228:231]\n"
    "ds_read_b64_tr_b16 a[134:135], v13 offset:40960\n"
    "v_mfma_f32_16x16x32_bf16 v[236:239], a[180:183], v[76:79], v[236:239]\n"
    "ds_read_b64_tr_b16 a[136:137], v14 offset:32768\n"
    "v_mfma_f32_16x16x32_bf16 v[244:247], a[184:187], v[76:79], v[244:247]\n"
    "ds_read_b64_tr_b16 a[138:139], v14 offset:40960\n"
    "v_mfma_f32_16x16x32_bf16 v[252:255], a[188:191], v[76:79], v[252:255]\n"
    "ds_read_b64_tr_b16 a[140:141], v15 offset:32768\n"
    "s_cbranch_vccnz LR9_%=\n"
    "LB9_%=:\n"
    "v_mfma_f32_16x16x32_bf16 v[48:51], a[64:67], a[0:3], v[64:67]\n"
    "ds_read_b64_tr_b16 a[142:143], v15 offset:40960\n"
    "v_exp_f32 v32, v32\n"
    "v_mfma_f32_16x16x32_bf16 v[56:59], a[64:67], a[4:7], v[68:71]\n"
    "ds_read_b64_tr_b16 a[144:145], v16 offset:32768\n"
    "v_exp_f32 v33, v33\n"
    "v_mfma_f32_16x16x32_bf16 v[52:55], a[68:71], a[0:3], v[64:67]\n"
    "s_mov_b32 m0, s65\n"
    "s_add_u32 s69, s69, 0x4000\n"
    "buffer_load_dwordx4 v24, s[48:51], s69 offen offset:0 lds\n"
    "v_mfma_f32_16x16x32_bf16 v[60:63], a[68:71], a[4:7], v[68:71]\n"
    "ds_read_b64_tr_b16 a[146:147], v16 offset:40960\n"
    "v_exp_f32 v34, v34\n"
    "v_mfma_f32_16x16x32_bf16 v[48:51], a[72:75], a[8:11], v[48:51]\n"
    "ds_read_b64_tr_b16 a[148:149], v17 offset:32768\n"
    "v_exp_f32 v35, v35\n"
    "v_mfma_f32_16x16x32_bf16 v[56:59], a[72:75], a[12:15], v[56:59]\n"
    "ds_read_b64_tr_b16 a[150:151], v17 offset:40960\n"
    "v_exp_f32 v36, v36\n"
    "v_mfma_f32_16x16x32_bf16 v[52:55], a[76:79], a[8:11], v[52:55]\n"
    "ds_read_b64_tr_b16 a[152:153], v18 offset:32768\n"
    "v_exp_f32 v37, v37\n"
    "v_mfma_f32_16x16x32_bf16 v[60:63], a[76:79], a[12:15], v[60:63]\n"
    "ds_read_b64_tr_b16 a[154:155], v18 offset:40960\n"
    "v_exp_f32 v38, v38\n"
    "v_mfma_f32_16x16x32_bf16 v[48:51], a[80:83], a[16:19], v[48:51]\n"
    "ds_read_b64_tr_b16 a[156:157], v19 offset:32768\n"
    "v_exp_f32 v39, v39\n"
    "v_mfma_f32_16x16x32_bf16 v[56:59], a[80:83], a[20:23], v[56:59]\n"
    "buffer_load_dwordx4 v25, s[48:51], s69 offen offset:1024 lds\n"
    "v_mfma_f32_16x16x32_bf16 v[52:55], a[84:87], a[16:19], v[52:55]\n"
    "ds_read_b64_tr_b16 a[158:159], v19 offset:40960\n"
    "v_cvt_pk_bf16_f32 v72, v32, v33\n"
    "v_mfma_f32_16x16x32_bf16 v[60:63], a[84:87], a[20:23], v[60:63]\n"
    "ds_read_b64_tr_b16 a[160:161], v12 offset:33024\n"
    "v_cvt_pk_bf16_f32 v73, v34, v35\n"
    "v_mfma_f32_16x16x32_bf16 v[48:51], a[88:91], a[24:27], v[48:51]\n"
    "ds_read_b64_tr_b16 a[162:163], v12 offset:41216\n"
    "v_cvt_pk_bf16_f32 v74, v36, v37\n"
    "v_mfma_f32_16x16x32_bf16 v[56:59], a[88:91], a[28:31], v[56:59]\n"
    "ds_read_b64_tr_b16 a[164:165], v13 offset:33024\n"
    "v_cvt_pk_bf16_f32 v75, v38, v39\n"
    "v_mfma_f32_16x16x32_bf16 v[52:55], a[92:95], a[24:27], v[52:55]\n"
    "ds_read_b64_tr_b16 a[166:167], v13 offset:41216\n"
    "v_add_f32 v80, v80, v32\n"
    "v_mfma_f32_16x16x32_bf16 v[60:63], a[92:95], a[28:31], v[60:63]\n"
    "ds_read_b64_tr_b16 a[168:169], v14 offset:33024\n"
    "v_add_f32 v80, v80, v33\n"
    "v_mfma_f32_16x16x32_bf16 v[48:51], a[96:99], a[32:35], v[48:51]\n"
    "buffer_load_dwordx4 v26, s[48:51], s69 offen offset:2048 lds\n"
    "v_mfma_f32_16x16x32_bf16 v[56:59], a[96:99], a[36:39], v[56:59]\n"
    "ds_read_b64_tr_b16 a[170:171], v14 offset:41216\n"
    "v_add_f32 v80, v80, v34\n"
    "v_mfma_f32_16x16x32_bf16 v[52:55], a[100:103], a[32:35], v[52:55]\n"
    "ds_read_b64_tr_b16 a[172:173], v15 offset:33024\n"
    "v_add_f32 v80, v80, v35\n"
    "v_mfma_f32_16x16x32_bf16 v[60:63], a[100:103], a[36:39], v[60:63]\n"
    "ds_read_b64_tr_b16 a[174:175], v15 offset:41216\n"
    "v_exp_f32 v40, v40\n"
    "v_mfma_f32_16x16x32_bf16 v[48:51], a[104:107], a[40:43], v[48:51]\n"
    "ds_read_b64_tr_b16 a[176:177], v16 offset:33024\n"
    "v_exp_f32 v41, v41\n"
    "v_mfma_f32_16x16x32_bf16 v[56:59], a[104:107], a[44:47], v[56:59]\n"
    "ds_read_b64_tr_b16 a[178:179], v16 offset:41216\n"
    "v_add_f32 v80, v80, v36\n"
    "v_mfma_f32_16x16x32_bf16 v[52:55], a[108:111], a[40:43], v[52:55]\n"
    "ds_read_b64_tr_b16 a[180:181], v17 offset:33024\n"
    "v_exp_f32 v42, v42\n"
    "v_mfma_f32_16x16x32_bf16 v[60:63], a[108:111], a[44:47], v[60:63]\n"
    "buffer_load_dwordx4 v27, s[48:51], s69 offen offset:3072 lds\n"
    "v_mfma_f32_16x16x32_bf16 v[48:51], a[112:115], a[48:51], v[48:51]\n"
    "ds_read_b64_tr_b16 a[182:183], v17 offset:41216\n"
    "v_exp_f32 v43, v43\n"
    "v_mfma_f32_16x16x32_bf16 v[56:59], a[112:115], a[52:55], v[56:59]\n"
    "ds_read_b64_tr_b16 a[184:185], v18 offset:33024\n"
    "v_add_f32 v80, v80, v37\n"
    "v_mfma_f32_16x16x32_bf16 v[52:55], a[116:119], a[48:51], v[52:55]\n"
    "ds_read_b64_tr_b16 a[186:187], v18 offset:41216\n"
    "v_exp_f32 v44, v44\n"
    "v_mfma_f32_16x16x32_bf16 v[60:63], a[116:119], a[52:55], v[60:63]\n"
    "ds_read_b64_tr_b16 a[188:189], v19 offset:33024\n"
    "v_exp_f32 v45, v45\n"
    "v_mfma_f32_16x16x32_bf16 v[48:51], a[120:123], a[56:59], v[48:51]\n"
    "ds_read_b64_tr_b16 a[190:191], v19 offset:41216\n"
    "v_add_f32 v80, v80, v38\n"
    "v_mfma_f32_16x16x32_bf16 v[56:59], a[120:123], a[60:63], v[56:59]\n"
    "ds_read_b128 a[64:67], v8 offset:49152\n"
    "v_exp_f32 v46, v46\n"
    "v_mfma_f32_16x16x32_bf16 v[52:55], a[124:127], a[56:59], v[52:55]\n"
    "s_mov_b32 m0, s62\n"
    "s_add_u32 s68, s68, 0x4000\n"
    "buffer_load_dwordx4 v20, s[44:47], s68 offen offset:0 lds\n"
    "v_mfma_f32_16x16x32_bf16 v[60:63], a[124:127], a[60:63], v[60:63]\n"
    "ds_read_b128 a[68:71], v8 offset:57344\n"
    "v_exp_f32 v47, v47\n"
    "s_waitcnt lgkmcnt(2)\n"
    "v_mfma_f32_16x16x32_bf16 v[128:131], a[128:131], v[72:75], v[128:131]\n"
    "ds_read_b128 a[72:75], v9 offset:49152\n"
    "v_add_f32 v80, v80, v39\n"
    "v_mfma_f32_16x16x32_bf16 v[136:139], a[132:135], v[72:75], v[136:139]\n"
    "ds_read_b128 a[76:79], v9 offset:57344\n"
    "v_cvt_pk_bf16_f32 v76, v40, v41\n"
    "v_mfma_f32_16x16x32_bf16 v[144:147], a[136:139], v[72:75], v[144:147]\n"
    "ds_read_b128 a[80:83], v10 offset:49152\n"
    "v_cvt_pk_bf16_f32 v77, v42, v43\n"
    "v_mfma_f32_16x16x32_bf16 v[152:155], a[140:143], v[72:75], v[152:155]\n"
    "ds_read_b128 a[84:87], v10 offset:57344\n"
    "v_cvt_pk_bf16_f32 v78, v44, v45\n"
    "v_mfma_f32_16x16x32_bf16 v[160:163], a[144:147], v[72:75], v[160:163]\n"
    "ds_read_b128 a[88:91], v11 offset:49152\n"
    "v_cvt_pk_bf16_f32 v79, v46, v47\n"
    "v_mfma_f32_16x16x32_bf16 v[168:171], a[148:151], v[72:75], v[168:171]\n"
    "buffer_load_dwordx4 v21, s[44:47], s68 offen offset:1024 lds\n"
    "v_mfma_f32_16x16x32_bf16 v[176:179], a[152:155], v[72:75], v[176:179]\n"
    "ds_read_b128 a[92:95], v11 offset:57344\n"
    "v_max3_f32 v82, v48, v49, v50\n"
    "v_mfma_f32_16x16x32_bf16 v[184:187], a[156:159], v[72:75], v[184:187]\n"
    "ds_read_b128 a[96:99], v8 offset:49408\n"
    "v_add_f32 v81, v81, v40\n"
    "v_mfma_f32_16x16x32_bf16 v[192:195], a[160:163], v[72:75], v[192:195]\n"
    "ds_read_b128 a[100:103], v8 offset:57600\n"
    "v_max3_f32 v83, v56, v57, v58\n"
    "v_mfma_f32_16x16x32_bf16 v[200:203], a[164:167], v[72:75], v[200:203]\n"
    "ds_read_b128 a[104:107], v9 offset:49408\n"
    "v_add_f32 v81, v81, v41\n"
    "v_mfma_f32_16x16x32_bf16 v[208:211], a[168:171], v[72:75], v[208:211]\n"
    "ds_read_b128 a[108:111], v9 offset:57600\n"
    "v_max3_f32 v82, v82, v51, v52\n"
    "v_mfma_f32_16x16x32_bf16 v[216:219], a[172:175], v[72:75], v[216:219]\n"
    "ds_read_b128 a[112:115], v10 offset:49408\n"
    "v_add_f32 v81, v81, v42\n"
    "v_mfma_f32_16x16x32_bf16 v[224:227], a[176:179], v[72:75], v[224:227]\n"
    "buffer_load_dwordx4 v22, s[44:47], s68 offen offset:2048 lds\n"
    "v_mfma_f32_16x16x32_bf16 v[232:235], a[180:183], v[72:75], v[232:235]\n"
    "ds_read_b128 a[116:119], v10 offset:57600\n"
    "v_max3_f32 v83, v83, v59, v60\n"
    "v_mfma_f32_16x16x32_bf16 v[240:243], a[184:187], v[72:75], v[240:243]\n"
    "ds_read_b128 a[120:123], v11 offset:49408\n"
    "v_add_f32 v81, v81, v43\n"
    "v_mfma_f32_16x16x32_bf16 v[248:251], a[188:191], v[72:75], v[248:251]\n"
    "ds_read_b128 a[124:127], v11 offset:57600\n"
    "v_max3_f32 v82, v82, v53, v54\n"
    "v_mfma_f32_16x16x32_bf16 v[132:135], a[128:131], v[76:79], v[132:135]\n"
    "v_add_f32 v81, v81, v44\n"
    "v_mfma_f32_16x16x32_bf16 v[140:143], a[132:135], v[76:79], v[140:143]\n"
    "v_max3_f32 v83, v83, v61, v62\n"
    "v_mfma_f32_16x16x32_bf16 v[148:151], a[136:139], v[76:79], v[148:151]\n"
    "v_add_f32 v81, v81, v45\n"
    "v_mfma_f32_16x16x32_bf16 v[156:159], a[140:143], v[76:79], v[156:159]\n"
    "buffer_load_dwordx4 v23, s[44:47], s68 offen offset:3072 lds\n"
    "v_mfma_f32_16x16x32_bf16 v[164:167], a[144:147], v[76:79], v[164:167]\n"
    "v_max3_f32 v82, v82, v55, v63\n"
    "v_mfma_f32_16x16x32_bf16 v[172:175], a[148:151], v[76:79], v[172:175]\n"
    "v_add_f32 v81, v81, v46\n"
    "v_mfma_f32_16x16x32_bf16 v[180:183], a[152:155], v[76:79], v[180:183]\n"
    "v_max_f32 v82, v82, v83\n"
    "v_mfma_f32_16x16x32_bf16 v[188:191], a[156:159], v[76:79], v[188:191]\n"
    "v_add_f32 v81, v81, v47\n"
    "v_mfma_f32_16x16x32_bf16 v[196:199], a[160:163], v[76:79], v[196:199]\n"
    "v_cmp_lt_f32 vcc, s71, v82\n"
    "v_mfma_f32_16x16x32_bf16 v[204:207], a[164:167], v[76:79], v[204:207]\n"
    "s_waitcnt vmcnt(16) lgkmcnt(0)\n"
    "s_barrier\n"
    "ds_read_b64_tr_b16 a[128:129], v12 offset:49152\n"
    "v_mfma_f32_16x16x32_bf16 v[212:215], a[168:171], v[76:79], v[212:215]\n"
    "ds_read_b64_tr_b16 a[130:131], v12 offset:57344\n"
    "v_mfma_f32_16x16x32_bf16 v[220:223], a[172:175], v[76:79], v[220:223]\n"
    "ds_read_b64_tr_b16 a[132:133], v13 offset:49152\n"
    "v_mfma_f32_16x16x32_bf16 v[228:231], a[176:179], v[76:79], v[228:231]\n"
    "ds_read_b64_tr_b16 a[134:135], v13 offset:57344\n"
    "v_mfma_f32_16x16x32_bf16 v[236:239], a[180:183], v[76:79], v[236:239]\n"
    "ds_read_b64_tr_b16 a[136:137], v14 offset:49152\n"
    "v_mfma_f32_16x16x32_bf16 v[244:247], a[184:187], v[76:79], v[244:247]\n"
    "ds_read_b64_tr_b16 a[138:139], v14 offset:57344\n"
    "v_mfma_f32_16x16x32_bf16 v[252:255], a[188:191], v[76:79], v[252:255]\n"
    "ds_read_b64_tr_b16 a[140:141], v15 offset:49152\n"
    "s_cbranch_vccnz LR10_%=\n"
    "LB10_%=:\n"
    "v_mfma_f32_16x16x32_bf16 v[32:35], a[64:67], a[0:3], v[64:67]\n"
    "ds_read_b64_tr_b16 a[142:143], v15 offset:57344\n"
    "v_exp_f32 v48, v48\n"
    "v_mfma_f32_16x16x32_bf16 v[40:43], a[64:67], a[4:7], v[68:71]\n"
    "ds_read_b64_tr_b16 a[144:145], v16 offset:49152\n"
    "v_exp_f32 v49, v49\n"
    "v_mfma_f32_16x16x32_bf16 v[36:39], a[68:71], a[0:3], v[64:67]\n"
    "s_mov_b32 m0, s66\n"
    "s_add_u32 s69, s69, 0x4000\n"
    "buffer_load_dwordx4 v24, s[48:51], s69 offen offset:0 lds\n"
    "v_mfma_f32_16x16x32_bf16 v[44:47], a[68:71], a[4:7], v[68:71]\n"
    "ds_read_b64_tr_b16 a[146:147], v16 offset:57344\n"
    "v_exp_f32 v50, v50\n"
    "v_mfma_f32_16x16x32_bf16 v[32:35], a[72:75], a[8:11], v[32:35]\n"
    "ds_read_b64_tr_b16 a[148:149], v17 offset:49152\n"
    "v_exp_f32 v51, v51\n"
    "v_mfma_f32_16x16x32_bf16 v[40:43], a[72:75], a[12:15], v[40:43]\n"
    "ds_read_b64_tr_b16 a[150:151], v17 offset:57344\n"
    "v_exp_f32 v52, v52\n"
    "v_mfma_f32_16x16x32_bf16 v[36:39], a[76:79], a[8:11], v[36:39]\n"
    "ds_read_b64_tr_b16 a[152:153], v18 offset:49152\n"
    "v_exp_f32 v53, v53\n"
    "v_mfma_f32_16x16x32_bf16 v[44:47], a[76:79], a[12:15], v[44:47]\n"
    "ds_read_b64_tr_b16 a[154:155], v18 offset:57344\n"
    "v_exp_f32 v54, v54\n"
    "v_mfma_f32_16x16x32_bf16 v[32:35], a[80:83], a[16:19], v[32:35]\n"
    "ds_read_b64_tr_b16 a[156:157], v19 offset:49152\n"
    "v_exp_f32 v55, v55\n"
    "v_mfma_f32_16x16x32_bf16 v[40:43], a[80:83], a[20:23], v[40:43]\n"
    "buffer_load_dwordx4 v25, s[48:51], s69 offen offset:1024 lds\n"
    "v_mfma_f32_16x16x32_bf16 v[36:39], a[84:87], a[16:19], v[36:39]\n"
    "ds_read_b64_tr_b16 a[158:159], v19 offset:57344\n"
    "v_cvt_pk_bf16_f32 v72, v48, v49\n"
    "v_mfma_f32_16x16x32_bf16 v[44:47], a[84:87], a[20:23], v[44:47]\n"
    "ds_read_b64_tr_b16 a[160:161], v12 offset:49408\n"
    "v_cvt_pk_bf16_f32 v73, v50, v51\n"
    "v_mfma_f32_16x16x32_bf16 v[32:35], a[88:91], a[24:27], v[32:35]\n"
    "ds_read_b64_tr_b16 a[162:163], v12 offset:57600\n"
    "v_cvt_pk_bf16_f32 v74, v52, v53\n"
    "v_mfma_f32_16x16x32_bf16 v[40:43], a[88:91], a[28:31], v[40:43]\n"
    "ds_read_b64_tr_b16 a[164:165], v13 offset:49408\n"
    "v_cvt_pk_bf16_f32 v75, v54, v55\n"
    "v_mfma_f32_16x16x32_bf16 v[36:39], a[92:95], a[24:27], v[36:39]\n"
    "ds_read_b64_tr_b16 a[166:167], v13 offset:57600\n"
    "v_add_f32 v80, v80, v48\n"
    "v_mfma_f32_16x16x32_bf16 v[44:47], a[92:95], a[28:31], v[44:47]\n"
    "ds_read_b64_tr_b16 a[168:169], v14 offset:49408\n"
    "v_add_f32 v80, v80, v49\n"
    "v_mfma_f32_16x16x32_bf16 v[32:35], a[96:99], a[32:35], v[32:35]\n"
    "buffer_load_dwordx4 v26, s[48:51], s69 offen offset:2048 lds\n"
    "v_mfma_f32_16x16x32_bf16 v[40:43], a[96:99], a[36:39], v[40:43]\n"
    "ds_read_b64_tr_b16 a[170:171], v14 offset:57600\n"
    "v_add_f32 v80, v80, v50\n"
    "v_mfma_f32_16x16x32_bf16 v[36:39], a[100:103], a[32:35], v[36:39]\n"
    "ds_read_b64_tr_b16 a[172:173], v15 offset:49408\n"
    "v_add_f32 v80, v80, v51\n"
    "v_mfma_f32_16x16x32_bf16 v[44:47], a[100:103], a[36:39], v[44:47]\n"
    "ds_read_b64_tr_b16 a[174:175], v15 offset:57600\n"
    "v_exp_f32 v56, v56\n"
    "v_mfma_f32_16x16x32_bf16 v[32:35], a[104:107], a[40:43], v[32:35]\n"
    "ds_read_b64_tr_b16 a[176:177], v16 offset:49408\n"
    "v_exp_f32 v57, v57\n"
    "v_mfma_f32_16x16x32_bf16 v[40:43], a[104:107], a[44:47], v[40:43]\n"
    "ds_read_b64_tr_b16 a[178:179], v16 offset:57600\n"
    "v_add_f32 v80, v80, v52\n"
    "v_mfma_f32_16x16x32_bf16 v[36:39], a[108:111], a[40:43], v[36:39]\n"
    "ds_read_b64_tr_b16 a[180:181], v17 offset:49408\n"
    "v_exp_f32 v58, v58\n"
    "v_mfma_f32_16x16x32_bf16 v[44:47], a[108:111], a[44:47], v[44:47]\n"
    "buffer_load_dwordx4 v27, s[48:51], s69 offen offset:3072 lds\n"
    "v_mfma_f32_16x16x32_bf16 v[32:35], a[112:115], a[48:51], v[32:35]\n"
    "ds_read_b64_tr_b16 a[182:183], v17 offset:57600\n"
    "v_exp_f32 v59, v59\n"
    "v_mfma_f32_16x16x32_bf16 v[40:43], a[112:115], a[52:55], v[40:43]\n"
    "ds_read_b64_tr_b16 a[184:185], v18 offset:49408\n"
    "v_add_f32 v80, v80, v53\n"
    "v_mfma_f32_16x16x32_bf16 v[36:39], a[116:119], a[48:51], v[36:39]\n"
    "ds_read_b64_tr_b16 a[186:187], v18 offset:57600\n"
    "v_exp_f32 v60, v60\n"
    "v_mfma_f32_16x16x32_bf16 v[44:47], a[116:119], a[52:55], v[44:47]\n"
    "ds_read_b64_tr_b16 a[188:189], v19 offset:49408\n"
    "v_exp_f32 v61, v61\n"
    "v_mfma_f32_16x16x32_bf16 v[32:35], a[120:123], a[56:59], v[32:35]\n"
    "ds_read_b64_tr_b16 a[190:191], v19 offset:57600\n"
    "v_add_f32 v80, v80, v54\n"
    "v_mfma_f32_16x16x32_bf16 v[40:43], a[120:123], a[60:63], v[40:43]\n"
    "ds_read_b128 a[64:67], v8 offset:0\n"
    "v_exp_f32 v62, v62\n"
    "v_mfma_f32_16x16x32_bf16 v[36:39], a[124:127], a[56:59], v[36:39]\n"
    "s_mov_b32 m0, s63\n"
    "s_add_u32 s68, s68, 0x4000\n"
    "buffer_load_dwordx4 v20, s[44:47], s68 offen offset:0 lds\n"
    "v_mfma_f32_16x16x32_bf16 v[44:47], a[124:127], a[60:63], v[44:47]\n"
    "ds_read_b128 a[68:71], v8 offset:8192\n"
    "v_exp_f32 v63, v63\n"
    "s_waitcnt lgkmcnt(2)\n"
    "v_mfma_f32_16x16x32_bf16 v[128:131], a[128:131], v[72:75], v[128:131]\n"
    "ds_read_b128 a[72:75], v9 offset:0\n"
    "v_add_f32 v80, v80, v55\n"
    "v_mfma_f32_16x16x32_bf16 v[136:139], a[132:135], v[72:75], v[136:139]\n"
    "ds_read_b128 a[76:79], v9 offset:8192\n"
    "v_cvt_pk_bf16_f32 v76, v56, v57\n"
    "v_mfma_f32_16x16x32_bf16 v[144:147], a[136:139], v[72:75], v[144:147]\n"
    "ds_read_b128 a[80:83], v10 offset:0\n"
    "v_cvt_pk_bf16_f32 v77, v58, v59\n"
    "v_mfma_f32_16x16x32_bf16 v[152:155], a[140:143], v[72:75], v[152:155]\n"
    "ds_read_b128 a[84:87], v10 offset:8192\n"
    "v_cvt_pk_bf16_f32 v78, v60, v61\n"
    "v_mfma_f32_16x16x32_bf16 v[160:163], a[144:147], v[72:75], v[160:163]\n"
    "ds_read_b128 a[88:91], v11 offset:0\n"
    "v_cvt_pk_bf16_f32 v79, v62, v63\n"
    "v_mfma_f32_16x16x32_bf16 v[168:171], a[148:151], v[72:75], v[168:171]\n"
    "buffer_load_dwordx4 v21, s[44:47], s68 offen offset:1024 lds\n"
    "v_mfma_f32_16x16x32_bf16 v[176:179], a[152:155], v[72:75], v[176:179]\n"
    "ds_read_b128 a[92:95], v11 offset:8192\n"
    "v_max3_f32 v82, v32, v33, v34\n"
    "v_mfma_f32_16x16x32_bf16 v[184:187], a[156:159], v[72:75], v[184:187]\n"
    "ds_read_b128 a[96:99], v8 offset:256\n"
    "v_add_f32 v81, v81, v56\n"
    "v_mfma_f32_16x16x32_bf16 v[192:195], a[160:163], v[72:75], v[192:195]\n"
    "ds_read_b128 a[100:103], v8 offset:8448\n"
    "v_max3_f32 v83, v40, v41, v42\n"
    "v_mfma_f32_16x16x32_bf16 v[200:203], a[164:167], v[72:75], v[200:203]\n"
    "ds_read_b128 a[104:107], v9 offset:256\n"
    "v_add_f32 v81, v81, v57\n"
    "v_mfma_f32_16x16x32_bf16 v[208:211], a[168:171], v[72:75], v[208:211]\n"
    "ds_read_b128 a[108:111], v9 offset:8448\n"
    "v_max3_f32 v82, v82, v35, v36\n"
    "v_mfma_f32_16x16x32_bf16 v[216:219], a[172:175], v[72:75], v[216:219]\n"
    "ds_read_b128 a[112:115], v10 offset:256\n"
    "v_add_f32 v81, v81, v58\n"
    "v_mfma_f32_16x16x32_bf16 v[224:227], a[176:179], v[72:75], v[224:227]\n"
    "buffer_load_dwordx4 v22, s[44:47], s68 offen offset:2048 lds\n"
    "v_mfma_f32_16x16x32_bf16 v[232:235], a[180:183], v[72:75], v[232:235]\n"
    "ds_read_b128 a[116:119], v10 offset:8448\n"
    "v_max3_f32 v83, v83, v43, v44\n"
    "v_mfma_f32_16x16x32_bf16 v[240:243], a[184:187], v[72:75], v[240:243]\n"
    "ds_read_b128 a[120:123], v11 offset:256\n"
    "v_add_f32 v81, v81, v59\n"
    "v_mfma_f32_16x16x32_bf16 v[248:251], a[188:191], v[72:75], v[248:251]\n"
    "ds_read_b128 a[124:127], v11 offset:8448\n"
    "v_max3_f32 v82, v82, v37, v38\n"
    "v_mfma_f32_16x16x32_bf16 v[132:135], a[128:131], v[76:79], v[132:135]\n"
    "v_add_f32 v81, v81, v60\n"
    "v_mfma_f32_16x16x32_bf16 v[140:143], a[132:135], v[76:79], v[140:143]\n"
    "v_max3_f32 v83, v83, v45, v46\n"
    "v_mfma_f32_16x16x32_bf16 v[148:151], a[136:139], v[76:79], v[148:151]\n"
    "v_add_f32 v81, v81, v61\n"
    "v_mfma_f32_16x16x32_bf16 v[156:159], a[140:143], v[76:79], v[156:159]\n"
    "buffer_load_dwordx4 v23, s[44:47], s68 offen offset:3072 lds\n"
    "v_mfma_f32_16x16x32_bf16 v[164:167], a[144:147], v[76:79], v[164:167]\n"
    "v_max3_f32 v82, v82, v39, v47\n"
    "v_mfma_f32_16x16x32_bf16 v[172:175], a[148:151], v[76:79], v[172:175]\n"
    "v_add_f32 v81, v81, v62\n"
    "v_mfma_f32_16x16x32_bf16 v[180:183], a[152:155], v[76:79], v[180:183]\n"
    "v_max_f32 v82, v82, v83\n"
    "v_mfma_f32_16x16x32_bf16 v[188:191], a[156:159], v[76:79], v[188:191]\n"
    "v_add_f32 v81, v81, v63\n"
    "v_mfma_f32_16x16x32_bf16 v[196:199], a[160:163], v[76:79], v[196:199]\n"
    "v_cmp_lt_f32 vcc, s71, v82\n"
    "v_mfma_f32_16x16x32_bf16 v[204:207], a[164:167], v[76:79], v[204:207]\n"
    "s_waitcnt vmcnt(16) lgkmcnt(0)\n"
    "s_barrier\n"
    "ds_read_b64_tr_b16 a[128:129], v12 offset:0\n"
    "v_mfma_f32_16x16x32_bf16 v[212:215], a[168:171], v[76:79], v[212:215]\n"
    "ds_read_b64_tr_b16 a[130:131], v12 offset:8192\n"
    "v_mfma_f32_16x16x32_bf16 v[220:223], a[172:175], v[76:79], v[220:223]\n"
    "ds_read_b64_tr_b16 a[132:133], v13 offset:0\n"
    "v_mfma_f32_16x16x32_bf16 v[228:231], a[176:179], v[76:79], v[228:231]\n"
    "ds_read_b64_tr_b16 a[134:135], v13 offset:8192\n"
    "v_mfma_f32_16x16x32_bf16 v[236:239], a[180:183], v[76:79], v[236:239]\n"
    "ds_read_b64_tr_b16 a[136:137], v14 offset:0\n"
    "v_mfma_f32_16x16x32_bf16 v[244:247], a[184:187], v[76:79], v[244:247]\n"
    "ds_read_b64_tr_b16 a[138:139], v14 offset:8192\n"
    "v_mfma_f32_16x16x32_bf16 v[252:255], a[188:191], v[76:79], v[252:255]\n"
    "ds_read_b64_tr_b16 a[140:141], v15 offset:0\n"
    "s_cbranch_vccnz LR11_%=\n"
    "LB11_%=:\n"
    "v_mfma_f32_16x16x32_bf16 v[48:51], a[64:67], a[0:3], v[64:67]\n"
    "ds_read_b64_tr_b16 a[142:143], v15 offset:8192\n"
    "v_exp_f32 v32, v32\n"
    "v_mfma_f32_16x16x32_bf16 v[56:59], a[64:67], a[4:7], v[68:71]\n"
    "ds_read_b64_tr_b16 a[144:145], v16 offset:0\n"
    "v_exp_f32 v33, v33\n"
    "v_mfma_f32_16x16x32_bf16 v[52:55], a[68:71], a[0:3], v[64:67]\n"
    "s_mov_b32 m0, s67\n"
    "s_add_u32 s69, s69, 0x4000\n"
    "buffer_load_dwordx4 v24, s[48:51], s69 offen offset:0 lds\n"
    "v_mfma_f32_16x16x32_bf16 v[60:63], a[68:71], a[4:7], v[68:71]\n"
    "ds_read_b64_tr_b16 a[146:147], v16 offset:8192\n"
    "v_exp_f32 v34, v34\n"
    "v_mfma_f32_16x16x32_bf16 v[48:51], a[72:75], a[8:11], v[48:51]\n"
    "ds_read_b64_tr_b16 a[148:149], v17 offset:0\n"
    "v_exp_f32 v35, v35\n"
    "v_mfma_f32_16x16x32_bf16 v[56:59], a[72:75], a[12:15], v[56:59]\n"
    "ds_read_b64_tr_b16 a[150:151], v17 offset:8192\n"
    "v_exp_f32 v36, v36\n"
    "v_mfma_f32_16x16x32_bf16 v[52:55], a[76:79], a[8:11], v[52:55]\n"
    "ds_read_b64_tr_b16 a[152:153], v18 offset:0\n"
    "v_exp_f32 v37, v37\n"
    "v_mfma_f32_16x16x32_bf16 v[60:63], a[76:79], a[12:15], v[60:63]\n"
    "ds_read_b64_tr_b16 a[154:155], v18 offset:8192\n"
    "v_exp_f32 v38, v38\n"
    "v_mfma_f32_16x16x32_bf16 v[48:51], a[80:83], a[16:19], v[48:51]\n"
    "ds_read_b64_tr_b16 a[156:157], v19 offset:0\n"
    "v_exp_f32 v39, v39\n"
    "v_mfma_f32_16x16x32_bf16 v[56:59], a[80:83], a[20:23], v[56:59]\n"
    "buffer_load_dwordx4 v25, s[48:51], s69 offen offset:1024 lds\n"
    "v_mfma_f32_16x16x32_bf16 v[52:55], a[84:87], a[16:19], v[52:55]\n"
    "ds_read_b64_tr_b16 a[158:159], v19 offset:8192\n"
    "v_cvt_pk_bf16_f32 v72, v32, v33\n"
    "v_mfma_f32_16x16x32_bf16 v[60:63], a[84:87], a[20:23], v[60:63]\n"
    "ds_read_b64_tr_b16 a[160:161], v12 offset:256\n"
    "v_cvt_pk_bf16_f32 v73, v34, v35\n"
    "v_mfma_f32_16x16x32_bf16 v[48:51], a[88:91], a[24:27], v[48:51]\n"
    "ds_read_b64_tr_b16 a[162:163], v12 offset:8448\n"
    "v_cvt_pk_bf16_f32 v74, v36, v37\n"
    "v_mfma_f32_16x16x32_bf16 v[56:59], a[88:91], a[28:31], v[56:59]\n"
    "ds_read_b64_tr_b16 a[164:165], v13 offset:256\n"
    "v_cvt_pk_bf16_f32 v75, v38, v39\n"
    "v_mfma_f32_16x16x32_bf16 v[52:55], a[92:95], a[24:27], v[52:55]\n"
    "ds_read_b64_tr_b16 a[166:167], v13 offset:8448\n"
    "v_add_f32 v80, v80, v32\n"
    "v_mfma_f32_16x16x32_bf16 v[60:63], a[92:95], a[28:31], v[60:63]\n"
    "ds_read_b64_tr_b16 a[168:169], v14 offset:256\n"
    "v_add_f32 v80, v80, v33\n"
    "v_mfma_f32_16x16x32_bf16 v[48:51], a[96:99], a[32:35], v[48:51]\n"
    "buffer_load_dwordx4 v26, s[48:51], s69 offen offset:2048 lds\n"
    "v_mfma_f32_16x16x32_bf16 v[56:59], a[96:99], a[36:39], v[56:59]\n"
    "ds_read_b64_tr_b16 a[170:171], v14 offset:8448\n"
    "v_add_f32 v80, v80, v34\n"
    "v_mfma_f32_16x16x32_bf16 v[52:55], a[100:103], a[32:35], v[52:55]\n"
    "ds_read_b64_tr_b16 a[172:173], v15 offset:256\n"
    "v_add_f32 v80, v80, v35\n"
    "v_mfma_f32_16x16x32_bf16 v[60:63], a[100:103], a[36:39], v[60:63]\n"
    "ds_read_b64_tr_b16 a[174:175], v15 offset:8448\n"
    "v_exp_f32 v40, v40\n"
    "v_mfma_f32_16x16x32_bf16 v[48:51], a[104:107], a[40:43], v[48:51]\n"
    "ds_read_b64_tr_b16 a[176:177], v16 offset:256\n"
    "v_exp_f32 v41, v41\n"
    "v_mfma_f32_16x16x32_bf16 v[56:59], a[104:107], a[44:47], v[56:59]\n"
    "ds_read_b64_tr_b16 a[178:179], v16 offset:8448\n"
    "v_add_f32 v80, v80, v36\n"
    "v_mfma_f32_16x16x32_bf16 v[52:55], a[108:111], a[40:43], v[52:55]\n"
    "ds_read_b64_tr_b16 a[180:181], v17 offset:256\n"
    "v_exp_f32 v42, v42\n"
    "v_mfma_f32_16x16x32_bf16 v[60:63], a[108:111], a[44:47], v[60:63]\n"
    "buffer_load_dwordx4 v27, s[48:51], s69 offen offset:3072 lds\n"
    "v_mfma_f32_16x16x32_bf16 v[48:51], a[112:115], a[48:51], v[48:51]\n"
    "ds_read_b64_tr_b16 a[182:183], v17 offset:8448\n"
    "v_exp_f32 v43, v43\n"
    "v_mfma_f32_16x16x32_bf16 v[56:59], a[112:115], a[52:55], v[56:59]\n"
    "ds_read_b64_tr_b16 a[184:185], v18 offset:256\n"
    "v_add_f32 v80, v80, v37\n"
    "v_mfma_f32_16x16x32_bf16 v[52:55], a[116:119], a[48:51], v[52:55]\n"
    "ds_read_b64_tr_b16 a[186:187], v18 offset:8448\n"
    "v_exp_f32 v44, v44\n"
    "v_mfma_f32_16x16x32_bf16 v[60:63], a[116:119], a[52:55], v[60:63]\n"
    "ds_read_b64_tr_b16 a[188:189], v19 offset:256\n"
    "v_exp_f32 v45, v45\n"
    "v_mfma_f32_16x16x32_bf16 v[48:51], a[120:123], a[56:59], v[48:51]\n"
    "ds_read_b64_tr_b16 a[190:191], v19 offset:8448\n"
    "v_add_f32 v80, v80, v38\n"
    "v_mfma_f32_16x16x32_bf16 v[56:59], a[120:123], a[60:63], v[56:59]\n"
    "ds_read_b128 a[64:67], v8 offset:16384\n"
    "v_exp_f32 v46, v46\n"
    "v_mfma_f32_16x16x32_bf16 v[52:55], a[124:127], a[56:59], v[52:55]\n"
    "s_mov_b32 m0, s60\n"
    "s_add_u32 s68, s68, 0x4000\n"
    "buffer_load_dwordx4 v20, s[44:47], s68 offen offset:0 lds\n"
    "v_mfma_f32_16x16x32_bf16 v[60:63], a[124:127], a[60:63], v[60:63]\n"
    "ds_read_b128 a[68:71], v8 offset:24576\n"
    "v_exp_f32 v47, v47\n"
    "s_waitcnt lgkmcnt(2)\n"
    "v_mfma_f32_16x16x32_bf16 v[128:131], a[128:131], v[72:75], v[128:131]\n"
    "ds_read_b128 a[72:75], v9 offset:16384\n"
    "v_add_f32 v80, v80, v39\n"
    "v_mfma_f32_16x16x32_bf16 v[136:139], a[132:135], v[72:75], v[136:139]\n"
    "ds_read_b128 a[76:79], v9 offset:24576\n"
    "v_cvt_pk_bf16_f32 v76, v40, v41\n"
    "v_mfma_f32_16x16x32_bf16 v[144:147], a[136:139], v[72:75], v[144:147]\n"
    "ds_read_b128 a[80:83], v10 offset:16384\n"
    "v_cvt_pk_bf16_f32 v77, v42, v43\n"
    "v_mfma_f32_16x16x32_bf16 v[152:155], a[140:143], v[72:75], v[152:155]\n"
    "ds_read_b128 a[84:87], v10 offset:24576\n"
    "v_cvt_pk_bf16_f32 v78, v44, v45\n"
    "v_mfma_f32_16x16x32_bf16 v[160:163], a[144:147], v[72:75], v[160:163]\n"
    "ds_read_b128 a[88:91], v11 offset:16384\n"
    "v_cvt_pk_bf16_f32 v79, v46, v47\n"
    "v_mfma_f32_16x16x32_bf16 v[168:171], a[148:151], v[72:75], v[168:171]\n"
    "buffer_load_dwordx4 v21, s[44:47], s68 offen offset:1024 lds\n"
    "v_mfma_f32_16x16x32_bf16 v[176:179], a[152:155], v[72:75], v[176:179]\n"
    "ds_read_b128 a[92:95], v11 offset:24576\n"
    "v_max3_f32 v82, v48, v49, v50\n"
    "v_mfma_f32_16x16x32_bf16 v[184:187], a[156:159], v[72:75], v[184:187]\n"
    "ds_read_b128 a[96:99], v8 offset:16640\n"
    "v_add_f32 v81, v81, v40\n"
    "v_mfma_f32_16x16x32_bf16 v[192:195], a[160:163], v[72:75], v[192:195]\n"
    "ds_read_b128 a[100:103], v8 offset:24832\n"
    "v_max3_f32 v83, v56, v57, v58\n"
    "v_mfma_f32_16x16x32_bf16 v[200:203], a[164:167], v[72:75], v[200:203]\n"
    "ds_read_b128 a[104:107], v9 offset:16640\n"
    "v_add_f32 v81, v81, v41\n"
    "v_mfma_f32_16x16x32_bf16 v[208:211], a[168:171], v[72:75], v[208:211]\n"
    "ds_read_b128 a[108:111], v9 offset:24832\n"
    "v_max3_f32 v82, v82, v51, v52\n"
    "v_mfma_f32_16x16x32_bf16 v[216:219], a[172:175], v[72:75], v[216:219]\n"
    "ds_read_b128 a[112:115], v10 offset:16640\n"
    "v_add_f32 v81, v81, v42\n"
    "v_mfma_f32_16x16x32_bf16 v[224:227], a[176:179], v[72:75], v[224:227]\n"
    "buffer_load_dwordx4 v22, s[44:47], s68 offen offset:2048 lds\n"
    "v_mfma_f32_16x16x32_bf16 v[232:235], a[180:183], v[72:75], v[232:235]\n"
    "ds_read_b128 a[116:119], v10 offset:24832\n"
    "v_max3_f32 v83, v83, v59, v60\n"
    "v_mfma_f32_16x16x32_bf16 v[240:243], a[184:187], v[72:75], v[240:243]\n"
    "ds_read_b128 a[120:123], v11 offset:16640\n"
    "v_add_f32 v81, v81, v43\n"
    "v_mfma_f32_16x16x32_bf16 v[248:251], a[188:191], v[72:75], v[248:251]\n"
    "ds_read_b128 a[124:127], v11 offset:24832\n"
    "v_max3_f32 v82, v82, v53, v54\n"
    "v_mfma_f32_16x16x32_bf16 v[132:135], a[128:131], v[76:79], v[132:135]\n"
    "v_add_f32 v81, v81, v44\n"
    "v_mfma_f32_16x16x32_bf16 v[140:143], a[132:135], v[76:79], v[140:143]\n"
    "v_max3_f32 v83, v83, v61, v62\n"
    "v_mfma_f32_16x16x32_bf16 v[148:151], a[136:139], v[76:79], v[148:151]\n"
    "v_add_f32 v81, v81, v45\n"
    "v_mfma_f32_16x16x32_bf16 v[156:159], a[140:143], v[76:79], v[156:159]\n"
    "buffer_load_dwordx4 v23, s[44:47], s68 offen offset:3072 lds\n"
    "v_mfma_f32_16x16x32_bf16 v[164:167], a[144:147], v[76:79], v[164:167]\n"
    "v_max3_f32 v82, v82, v55, v63\n"
    "v_mfma_f32_16x16x32_bf16 v[172:175], a[148:151], v[76:79], v[172:175]\n"
    "v_add_f32 v81, v81, v46\n"
    "v_mfma_f32_16x16x32_bf16 v[180:183], a[152:155], v[76:79], v[180:183]\n"
    "v_max_f32 v82, v82, v83\n"
    "v_mfma_f32_16x16x32_bf16 v[188:191], a[156:159], v[76:79], v[188:191]\n"
    "v_add_f32 v81, v81, v47\n"
    "v_mfma_f32_16x16x32_bf16 v[196:199], a[160:163], v[76:79], v[196:199]\n"
    "v_cmp_lt_f32 vcc, s71, v82\n"
    "v_mfma_f32_16x16x32_bf16 v[204:207], a[164:167], v[76:79], v[204:207]\n"
    "s_waitcnt vmcnt(16) lgkmcnt(0)\n"
    "s_barrier\n"
    "ds_read_b64_tr_b16 a[128:129], v12 offset:16384\n"
    "v_mfma_f32_16x16x32_bf16 v[212:215], a[168:171], v[76:79], v[212:215]\n"
    "ds_read_b64_tr_b16 a[130:131], v12 offset:24576\n"
    "v_mfma_f32_16x16x32_bf16 v[220:223], a[172:175], v[76:79], v[220:223]\n"
    "ds_read_b64_tr_b16 a[132:133], v13 offset:16384\n"
    "v_mfma_f32_16x16x32_bf16 v[228:231], a[176:179], v[76:79], v[228:231]\n"
    "ds_read_b64_tr_b16 a[134:135], v13 offset:24576\n"
    "v_mfma_f32_16x16x32_bf16 v[236:239], a[180:183], v[76:79], v[236:239]\n"
    "ds_read_b64_tr_b16 a[136:137], v14 offset:16384\n"
    "v_mfma_f32_16x16x32_bf16 v[244:247], a[184:187], v[76:79], v[244:247]\n"
    "ds_read_b64_tr_b16 a[138:139], v14 offset:24576\n"
    "v_mfma_f32_16x16x32_bf16 v[252:255], a[188:191], v[76:79], v[252:255]\n"
    "ds_read_b64_tr_b16 a[140:141], v15 offset:16384\n"
    "s_cbranch_vccnz LR12_%=\n"
    "LB12_%=:\n"
    "v_mfma_f32_16x16x32_bf16 v[32:35], a[64:67], a[0:3], v[64:67]\n"
    "ds_read_b64_tr_b16 a[142:143], v15 offset:24576\n"
    "v_exp_f32 v48, v48\n"
    "v_mfma_f32_16x16x32_bf16 v[40:43], a[64:67], a[4:7], v[68:71]\n"
    "ds_read_b64_tr_b16 a[144:145], v16 offset:16384\n"
    "v_exp_f32 v49, v49\n"
    "v_mfma_f32_16x16x32_bf16 v[36:39], a[68:71], a[0:3], v[64:67]\n"
    "s_mov_b32 m0, s64\n"
    "s_add_u32 s69, s69, 0x4000\n"
    "buffer_load_dwordx4 v24, s[48:51], s69 offen offset:0 lds\n"
    "v_mfma_f32_16x16x32_bf16 v[44:47], a[68:71], a[4:7], v[68:71]\n"
    "ds_read_b64_tr_b16 a[146:147], v16 offset:24576\n"
    "v_exp_f32 v50, v50\n"
    "v_mfma_f32_16x16x32_bf16 v[32:35], a[72:75], a[8:11], v[32:35]\n"
    "ds_read_b64_tr_b16 a[148:149], v17 offset:16384\n"
    "v_exp_f32 v51, v51\n"
    "v_mfma_f32_16x16x32_bf16 v[40:43], a[72:75], a[12:15], v[40:43]\n"
    "ds_read_b64_tr_b16 a[150:151], v17 offset:24576\n"
    "v_exp_f32 v52, v52\n"
    "v_mfma_f32_16x16x32_bf16 v[36:39], a[76:79], a[8:11], v[36:39]\n"
    "ds_read_b64_tr_b16 a[152:153], v18 offset:16384\n"
    "v_exp_f32 v53, v53\n"
    "v_mfma_f32_16x16x32_bf16 v[44:47], a[76:79], a[12:15], v[44:47]\n"
    "ds_read_b64_tr_b16 a[154:155], v18 offset:24576\n"
    "v_exp_f32 v54, v54\n"
    "v_mfma_f32_16x16x32_bf16 v[32:35], a[80:83], a[16:19], v[32:35]\n"
    "ds_read_b64_tr_b16 a[156:157], v19 offset:16384\n"
    "v_exp_f32 v55, v55\n"
    "v_mfma_f32_16x16x32_bf16 v[40:43], a[80:83], a[20:23], v[40:43]\n"
    "buffer_load_dwordx4 v25, s[48:51], s69 offen offset:1024 lds\n"
    "v_mfma_f32_16x16x32_bf16 v[36:39], a[84:87], a[16:19], v[36:39]\n"
    "ds_read_b64_tr_b16 a[158:159], v19 offset:24576\n"
    "v_cvt_pk_bf16_f32 v72, v48, v49\n"
    "v_mfma_f32_16x16x32_bf16 v[44:47], a[84:87], a[20:23], v[44:47]\n"
    "ds_read_b64_tr_b16 a[160:161], v12 offset:16640\n"
    "v_cvt_pk_bf16_f32 v73, v50, v51\n"
    "v_mfma_f32_16x16x32_bf16 v[32:35], a[88:91], a[24:27], v[32:35]\n"
    "ds_read_b64_tr_b16 a[162:163], v12 offset:24832\n"
    "v_cvt_pk_bf16_f32 v74, v52, v53\n"
    "v_mfma_f32_16x16x32_bf16 v[40:43], a[88:91], a[28:31], v[40:43]\n"
    "ds_read_b64_tr_b16 a[164:165], v13 offset:16640\n"
    "v_cvt_pk_bf16_f32 v75, v54, v55\n"
    "v_mfma_f32_16x16x32_bf16 v[36:39], a[92:95], a[24:27], v[36:39]\n"
    "ds_read_b64_tr_b16 a[166:167], v13 offset:24832\n"
    "v_add_f32 v80, v80, v48\n"
    "v_mfma_f32_16x16x32_bf16 v[44:47], a[92:95], a[28:31], v[44:47]\n"
    "ds_read_b64_tr_b16 a[168:169], v14 offset:16640\n"
    "v_add_f32 v80, v80, v49\n"
    "v_mfma_f32_16x16x32_bf16 v[32:35], a[96:99], a[32:35], v[32:35]\n"
    "buffer_load_dwordx4 v26, s[48:51], s69 offen offset:2048 lds\n"
    "v_mfma_f32_16x16x32_bf16 v[40:43], a[96:99], a[36:39], v[40:43]\n"
    "ds_read_b64_tr_b16 a[170:171], v14 offset:24832\n"
    "v_add_f32 v80, v80, v50\n"
    "v_mfma_f32_16x16x32_bf16 v[36:39], a[100:103], a[32:35], v[36:39]\n"
    "ds_read_b64_tr_b16 a[172:173], v15 offset:16640\n"
    "v_add_f32 v80, v80, v51\n"
    "v_mfma_f32_16x16x32_bf16 v[44:47], a[100:103], a[36:39], v[44:47]\n"
    "ds_read_b64_tr_b16 a[174:175], v15 offset:24832\n"
    "v_exp_f32 v56, v56\n"
    "v_mfma_f32_16x16x32_bf16 v[32:35], a[104:107], a[40:43], v[32:35]\n"
    "ds_read_b64_tr_b16 a[176:177], v16 offset:16640\n"
    "v_exp_f32 v57, v57\n"
    "v_mfma_f32_16x16x32_bf16 v[40:43], a[104:107], a[44:47], v[40:43]\n"
    "ds_read_b64_tr_b16 a[178:179], v16 offset:24832\n"
    "v_add_f32 v80, v80, v52\n"
    "v_mfma_f32_16x16x32_bf16 v[36:39], a[108:111], a[40:43], v[36:39]\n"
    "ds_read_b64_tr_b16 a[180:181], v17 offset:16640\n"
    "v_exp_f32 v58, v58\n"
    "v_mfma_f32_16x16x32_bf16 v[44:47], a[108:111], a[44:47], v[44:47]\n"
    "buffer_load_dwordx4 v27, s[48:51], s69 offen offset:3072 lds\n"
    "v_mfma_f32_16x16x32_bf16 v[32:35], a[112:115], a[48:51], v[32:35]\n"
    "ds_read_b64_tr_b16 a[182:183], v17 offset:24832\n"
    "v_exp_f32 v59, v59\n"
    "v_mfma_f32_16x16x32_bf16 v[40:43], a[112:115], a[52:55], v[40:43]\n"
    "ds_read_b64_tr_b16 a[184:185], v18 offset:16640\n"
    "v_add_f32 v80, v80, v53\n"
    "v_mfma_f32_16x16x32_bf16 v[36:39], a[116:119], a[48:51], v[36:39]\n"
    "ds_read_b64_tr_b16 a[186:187], v18 offset:24832\n"
    "v_exp_f32 v60, v60\n"
    "v_mfma_f32_16x16x32_bf16 v[44:47], a[116:119], a[52:55], v[44:47]\n"
    "ds_read_b64_tr_b16 a[188:189], v19 offset:16640\n"
    "v_exp_f32 v61, v61\n"
    "v_mfma_f32_16x16x32_bf16 v[32:35], a[120:123], a[56:59], v[32:35]\n"
    "ds_read_b64_tr_b16 a[190:191], v19 offset:24832\n"
    "v_add_f32 v80, v80, v54\n"
    "v_mfma_f32_16x16x32_bf16 v[40:43], a[120:123], a[60:63], v[40:43]\n"
    "ds_read_b128 a[64:67], v8 offset:32768\n"
    "v_exp_f32 v62, v62\n"
    "v_mfma_f32_16x16x32_bf16 v[36:39], a[124:127], a[56:59], v[36:39]\n"
    "s_mov_b32 m0, s61\n"
    "s_add_u32 s68, s68, 0x4000\n"
    "buffer_load_dwordx4 v20, s[44:47], s68 offen offset:0 lds\n"
    "v_mfma_f32_16x16x32_bf16 v[44:47], a[124:127], a[60:63], v[44:47]\n"
    "ds_read_b128 a[68:71], v8 offset:40960\n"
    "v_exp_f32 v63, v63\n"
    "s_waitcnt lgkmcnt(2)\n"
    "v_mfma_f32_16x16x32_bf16 v[128:131], a[128:131], v[72:75], v[128:131]\n"
    "ds_read_b128 a[72:75], v9 offset:32768\n"
    "v_add_f32 v80, v80, v55\n"
    "v_mfma_f32_16x16x32_bf16 v[136:139], a[132:135], v[72:75], v[136:139]\n"
    "ds_read_b128 a[76:79], v9 offset:40960\n"
    "v_cvt_pk_bf16_f32 v76, v56, v57\n"
    "v_mfma_f32_16x16x32_bf16 v[144:147], a[136:139], v[72:75], v[144:147]\n"
    "ds_read_b128 a[80:83], v10 offset:32768\n"
    "v_cvt_pk_bf16_f32 v77, v58, v59\n"
    "v_mfma_f32_16x16x32_bf16 v[152:155], a[140:143], v[72:75], v[152:155]\n"
    "ds_read_b128 a[84:87], v10 offset:40960\n"
    "v_cvt_pk_bf16_f32 v78, v60, v61\n"
    "v_mfma_f32_16x16x32_bf16 v[160:163], a[144:147], v[72:75], v[160:163]\n"
    "ds_read_b128 a[88:91], v11 offset:32768\n"
    "v_cvt_pk_bf16_f32 v79, v62, v63\n"
    "v_mfma_f32_16x16x32_bf16 v[168:171], a[148:151], v[72:75], v[168:171]\n"
    "buffer_load_dwordx4 v21, s[44:47], s68 offen offset:1024 lds\n"
    "v_mfma_f32_16x16x32_bf16 v[176:179], a[152:155], v[72:75], v[176:179]\n"
    "ds_read_b128 a[92:95], v11 offset:40960\n"
    "v_max3_f32 v82, v32, v33, v34\n"
    "v_mfma_f32_16x16x32_bf16 v[184:187], a[156:159], v[72:75], v[184:187]\n"
    "ds_read_b128 a[96:99], v8 offset:33024\n"
    "v_add_f32 v81, v81, v56\n"
    "v_mfma_f32_16x16x32_bf16 v[192:195], a[160:163], v[72:75], v[192:195]\n"
    "ds_read_b128 a[100:103], v8 offset:41216\n"
    "v_max3_f32 v83, v40, v41, v42\n"
    "v_mfma_f32_16x16x32_bf16 v[200:203], a[164:167], v[72:75], v[200:203]\n"
    "ds_read_b128 a[104:107], v9 offset:33024\n"
    "v_add_f32 v81, v81, v57\n"
    "v_mfma_f32_16x16x32_bf16 v[208:211], a[168:171], v[72:75], v[208:211]\n"
    "ds_read_b128 a[108:111], v9 offset:41216\n"
    "v_max3_f32 v82, v82, v35, v36\n"
    "v_mfma_f32_16x16x32_bf16 v[216:219], a[172:175], v[72:75], v[216:219]\n"
    "ds_read_b128 a[112:115], v10 offset:33024\n"
    "v_add_f32 v81, v81, v58\n"
    "v_mfma_f32_16x16x32_bf16 v[224:227], a[176:179], v[72:75], v[224:227]\n"
    "buffer_load_dwordx4 v22, s[44:47], s68 offen offset:2048 lds\n"
    "v_mfma_f32_16x16x32_bf16 v[232:235], a[180:183], v[72:75], v[232:235]\n"
    "ds_read_b128 a[116:119], v10 offset:41216\n"
    "v_max3_f32 v83, v83, v43, v44\n"
    "v_mfma_f32_16x16x32_bf16 v[240:243], a[184:187], v[72:75], v[240:243]\n"
    "ds_read_b128 a[120:123], v11 offset:33024\n"
    "v_add_f32 v81, v81, v59\n"
    "v_mfma_f32_16x16x32_bf16 v[248:251], a[188:191], v[72:75], v[248:251]\n"
    "ds_read_b128 a[124:127], v11 offset:41216\n"
    "v_max3_f32 v82, v82, v37, v38\n"
    "v_mfma_f32_16x16x32_bf16 v[132:135], a[128:131], v[76:79], v[132:135]\n"
    "v_add_f32 v81, v81, v60\n"
    "v_mfma_f32_16x16x32_bf16 v[140:143], a[132:135], v[76:79], v[140:143]\n"
    "v_max3_f32 v83, v83, v45, v46\n"
    "v_mfma_f32_16x16x32_bf16 v[148:151], a[136:139], v[76:79], v[148:151]\n"
    "v_add_f32 v81, v81, v61\n"
    "v_mfma_f32_16x16x32_bf16 v[156:159], a[140:143], v[76:79], v[156:159]\n"
    "buffer_load_dwordx4 v23, s[44:47], s68 offen offset:3072 lds\n"
    "v_mfma_f32_16x16x32_bf16 v[164:167], a[144:147], v[76:79], v[164:167]\n"
    "v_max3_f32 v82, v82, v39, v47\n"
    "v_mfma_f32_16x16x32_bf16 v[172:175], a[148:151], v[76:79], v[172:175]\n"
    "v_add_f32 v81, v81, v62\n"
    "v_mfma_f32_16x16x32_bf16 v[180:183], a[152:155], v[76:79], v[180:183]\n"
    "v_max_f32 v82, v82, v83\n"
    "v_mfma_f32_16x16x32_bf16 v[188:191], a[156:159], v[76:79], v[188:191]\n"
    "v_add_f32 v81, v81, v63\n"
    "v_mfma_f32_16x16x32_bf16 v[196:199], a[160:163], v[76:79], v[196:199]\n"
    "v_cmp_lt_f32 vcc, s71, v82\n"
    "v_mfma_f32_16x16x32_bf16 v[204:207], a[164:167], v[76:79], v[204:207]\n"
    "s_waitcnt vmcnt(16) lgkmcnt(0)\n"
    "s_barrier\n"
    "s_cmp_lg_u32 s68, 0xf8000\n"
    "s_cbranch_scc1 LOOP_%=\n"
    "ds_read_b64_tr_b16 a[128:129], v12 offset:32768\n"
    "v_mfma_f32_16x16x32_bf16 v[212:215], a[168:171], v[76:79], v[212:215]\n"
    "ds_read_b64_tr_b16 a[130:131], v12 offset:40960\n"
    "v_mfma_f32_16x16x32_bf16 v[220:223], a[172:175], v[76:79], v[220:223]\n"
    "ds_read_b64_tr_b16 a[132:133], v13 offset:32768\n"
    "v_mfma_f32_16x16x32_bf16 v[228:231], a[176:179], v[76:79], v[228:231]\n"
    "ds_read_b64_tr_b16 a[134:135], v13 offset:40960\n"
    "v_mfma_f32_16x16x32_bf16 v[236:239], a[180:183], v[76:79], v[236:239]\n"
    "ds_read_b64_tr_b16 a[136:137], v14 offset:32768\n"
    "v_mfma_f32_16x16x32_bf16 v[244:247], a[184:187], v[76:79], v[244:247]\n"
    "ds_read_b64_tr_b16 a[138:139], v14 offset:40960\n"
    "v_mfma_f32_16x16x32_bf16 v[252:255], a[188:191], v[76:79], v[252:255]\n"
    "ds_read_b64_tr_b16 a[140:141], v15 offset:32768\n"
    "s_cbranch_vccnz LR65_%=\n"
    "LB65_%=:\n"
    "v_mfma_f32_16x16x32_bf16 v[48:51], a[64:67], a[0:3], v[64:67]\n"
    "ds_read_b64_tr_b16 a[142:143], v15 offset:40960\n"
    "v_exp_f32 v32, v32\n"
    "v_mfma_f32_16x16x32_bf16 v[56:59], a[64:67], a[4:7], v[68:71]\n"
    "ds_read_b64_tr_b16 a[144:145], v16 offset:32768\n"
    "v_exp_f32 v33, v33\n"
    "v_mfma_f32_16x16x32_bf16 v[52:55], a[68:71], a[0:3], v[64:67]\n"
    "s_mov_b32 m0, s65\n"
    "s_add_u32 s69, s69, 0x4000\n"
    "buffer_load_dwordx4 v24, s[48:51], s69 offen offset:0 lds\n"
    "v_mfma_f32_16x16x32_bf16 v[60:63], a[68:71], a[4:7], v[68:71]\n"
    "ds_read_b64_tr_b16 a[146:147], v16 offset:40960\n"
    "v_exp_f32 v34, v34\n"
    "v_mfma_f32_16x16x32_bf16 v[48:51], a[72:75], a[8:11], v[48:51]\n"
    "ds_read_b64_tr_b16 a[148:149], v17 offset:32768\n"
    "v_exp_f32 v35, v35\n"
    "v_mfma_f32_16x16x32_bf16 v[56:59], a[72:75], a[12:15], v[56:59]\n"
    "ds_read_b64_tr_b16 a[150:151], v17 offset:40960\n"
    "v_exp_f32 v36, v36\n"
    "v_mfma_f32_16x16x32_bf16 v[52:55], a[76:79], a[8:11], v[52:55]\n"
    "ds_read_b64_tr_b16 a[152:153], v18 offset:32768\n"
    "v_exp_f32 v37, v37\n"
    "v_mfma_f32_16x16x32_bf16 v[60:63], a[76:79], a[12:15], v[60:63]\n"
    "ds_read_b64_tr_b16 a[154:155], v18 offset:40960\n"
    "v_exp_f32 v38, v38\n"
    "v_mfma_f32_16x16x32_bf16 v[48:51], a[80:83], a[16:19], v[48:51]\n"
    "ds_read_b64_tr_b16 a[156:157], v19 offset:32768\n"
    "v_exp_f32 v39, v39\n"
    "v_mfma_f32_16x16x32_bf16 v[56:59], a[80:83], a[20:23], v[56:59]\n"
    "buffer_load_dwordx4 v25, s[48:51], s69 offen offset:1024 lds\n"
    "v_mfma_f32_16x16x32_bf16 v[52:55], a[84:87], a[16:19], v[52:55]\n"
    "ds_read_b64_tr_b16 a[158:159], v19 offset:40960\n"
    "v_cvt_pk_bf16_f32 v72, v32, v33\n"
    "v_mfma_f32_16x16x32_bf16 v[60:63], a[84:87], a[20:23], v[60:63]\n"
    "ds_read_b64_tr_b16 a[160:161], v12 offset:33024\n"
    "v_cvt_pk_bf16_f32 v73, v34, v35\n"
    "v_mfma_f32_16x16x32_bf16 v[48:51], a[88:91], a[24:27], v[48:51]\n"
    "ds_read_b64_tr_b16 a[162:163], v12 offset:41216\n"
    "v_cvt_pk_bf16_f32 v74, v36, v37\n"
    "v_mfma_f32_16x16x32_bf16 v[56:59], a[88:91], a[28:31], v[56:59]\n"
    "ds_read_b64_tr_b16 a[164:165], v13 offset:33024\n"
    "v_cvt_pk_bf16_f32 v75, v38, v39\n"
    "v_mfma_f32_16x16x32_bf16 v[52:55], a[92:95], a[24:27], v[52:55]\n"
    "ds_read_b64_tr_b16 a[166:167], v13 offset:41216\n"
    "v_add_f32 v80, v80, v32\n"
    "v_mfma_f32_16x16x32_bf16 v[60:63], a[92:95], a[28:31], v[60:63]\n"
    "ds_read_b64_tr_b16 a[168:169], v14 offset:33024\n"
    "v_add_f32 v80, v80, v33\n"
    "v_mfma_f32_16x16x32_bf16 v[48:51], a[96:99], a[32:35], v[48:51]\n"
    "buffer_load_dwordx4 v26, s[48:51], s69 offen offset:2048 lds\n"
    "v_mfma_f32_16x16x32_bf16 v[56:59], a[96:99], a[36:39], v[56:59]\n"
    "ds_read_b64_tr_b16 a[170:171], v14 offset:41216\n"
    "v_add_f32 v80, v80, v34\n"
    "v_mfma_f32_16x16x32_bf16 v[52:55], a[100:103], a[32:35], v[52:55]\n"
    "ds_read_b64_tr_b16 a[172:173], v15 offset:33024\n"
    "v_add_f32 v80, v80, v35\n"
    "v_mfma_f32_16x16x32_bf16 v[60:63], a[100:103], a[36:39], v[60:63]\n"
    "ds_read_b64_tr_b16 a[174:175], v15 offset:41216\n"
    "v_exp_f32 v40, v40\n"
    "v_mfma_f32_16x16x32_bf16 v[48:51], a[104:107], a[40:43], v[48:51]\n"
    "ds_read_b64_tr_b16 a[176:177], v16 offset:33024\n"
    "v_exp_f32 v41, v41\n"
    "v_mfma_f32_16x16x32_bf16 v[56:59], a[104:107], a[44:47], v[56:59]\n"
    "ds_read_b64_tr_b16 a[178:179], v16 offset:41216\n"
    "v_add_f32 v80, v80, v36\n"
    "v_mfma_f32_16x16x32_bf16 v[52:55], a[108:111], a[40:43], v[52:55]\n"
    "ds_read_b64_tr_b16 a[180:181], v17 offset:33024\n"
    "v_exp_f32 v42, v42\n"
    "v_mfma_f32_16x16x32_bf16 v[60:63], a[108:111], a[44:47], v[60:63]\n"
    "buffer_load_dwordx4 v27, s[48:51], s69 offen offset:3072 lds\n"
    "v_mfma_f32_16x16x32_bf16 v[48:51], a[112:115], a[48:51], v[48:51]\n"
    "ds_read_b64_tr_b16 a[182:183], v17 offset:41216\n"
    "v_exp_f32 v43, v43\n"
    "v_mfma_f32_16x16x32_bf16 v[56:59], a[112:115], a[52:55], v[56:59]\n"
    "ds_read_b64_tr_b16 a[184:185], v18 offset:33024\n"
    "v_add_f32 v80, v80, v37\n"
    "v_mfma_f32_16x16x32_bf16 v[52:55], a[116:119], a[48:51], v[52:55]\n"
    "ds_read_b64_tr_b16 a[186:187], v18 offset:41216\n"
    "v_exp_f32 v44, v44\n"
    "v_mfma_f32_16x16x32_bf16 v[60:63], a[116:119], a[52:55], v[60:63]\n"
    "ds_read_b64_tr_b16 a[188:189], v19 offset:33024\n"
    "v_exp_f32 v45, v45\n"
    "v_mfma_f32_16x16x32_bf16 v[48:51], a[120:123], a[56:59], v[48:51]\n"
    "ds_read_b64_tr_b16 a[190:191], v19 offset:41216\n"
    "v_add_f32 v80, v80, v38\n"
    "v_mfma_f32_16x16x32_bf16 v[56:59], a[120:123], a[60:63], v[56:59]\n"
    "ds_read_b128 a[64:67], v8 offset:49152\n"
    "v_exp_f32 v46, v46\n"
    "v_mfma_f32_16x16x32_bf16 v[52:55], a[124:127], a[56:59], v[52:55]\n"
    "s_mov_b32 m0, s62\n"
    "s_add_u32 s68, s68, 0x4000\n"
    "buffer_load_dwordx4 v20, s[44:47], s68 offen offset:0 lds\n"
    "v_mfma_f32_16x16x32_bf16 v[60:63], a[124:127], a[60:63], v[60:63]\n"
    "ds_read_b128 a[68:71], v8 offset:57344\n"
    "v_exp_f32 v47, v47\n"
    "s_waitcnt lgkmcnt(2)\n"
    "v_mfma_f32_16x16x32_bf16 v[128:131], a[128:131], v[72:75], v[128:131]\n"
    "ds_read_b128 a[72:75], v9 offset:49152\n"
    "v_add_f32 v80, v80, v39\n"
    "v_mfma_f32_16x16x32_bf16 v[136:139], a[132:135], v[72:75], v[136:139]\n"
    "ds_read_b128 a[76:79], v9 offset:57344\n"
    "v_cvt_pk_bf16_f32 v76, v40, v41\n"
    "v_mfma_f32_16x16x32_bf16 v[144:147], a[136:139], v[72:75], v[144:147]\n"
    "ds_read_b128 a[80:83], v10 offset:49152\n"
    "v_cvt_pk_bf16_f32 v77, v42, v43\n"
    "v_mfma_f32_16x16x32_bf16 v[152:155], a[140:143], v[72:75], v[152:155]\n"
    "ds_read_b128 a[84:87], v10 offset:57344\n"
    "v_cvt_pk_bf16_f32 v78, v44, v45\n"
    "v_mfma_f32_16x16x32_bf16 v[160:163], a[144:147], v[72:75], v[160:163]\n"
    "ds_read_b128 a[88:91], v11 offset:49152\n"
    "v_cvt_pk_bf16_f32 v79, v46, v47\n"
    "v_mfma_f32_16x16x32_bf16 v[168:171], a[148:151], v[72:75], v[168:171]\n"
    "buffer_load_dwordx4 v21, s[44:47], s68 offen offset:1024 lds\n"
    "v_mfma_f32_16x16x32_bf16 v[176:179], a[152:155], v[72:75], v[176:179]\n"
    "ds_read_b128 a[92:95], v11 offset:57344\n"
    "v_max3_f32 v82, v48, v49, v50\n"
    "v_mfma_f32_16x16x32_bf16 v[184:187], a[156:159], v[72:75], v[184:187]\n"
    "ds_read_b128 a[96:99], v8 offset:49408\n"
    "v_add_f32 v81, v81, v40\n"
    "v_mfma_f32_16x16x32_bf16 v[192:195], a[160:163], v[72:75], v[192:195]\n"
    "ds_read_b128 a[100:103], v8 offset:57600\n"
    "v_max3_f32 v83, v56, v57, v58\n"
    "v_mfma_f32_16x16x32_bf16 v[200:203], a[164:167], v[72:75], v[200:203]\n"
    "ds_read_b128 a[104:107], v9 offset:49408\n"
    "v_add_f32 v81, v81, v41\n"
    "v_mfma_f32_16x16x32_bf16 v[208:211], a[168:171], v[72:75], v[208:211]\n"
    "ds_read_b128 a[108:111], v9 offset:57600\n"
    "v_max3_f32 v82, v82, v51, v52\n"
    "v_mfma_f32_16x16x32_bf16 v[216:219], a[172:175], v[72:75], v[216:219]\n"
    "ds_read_b128 a[112:115], v10 offset:49408\n"
    "v_add_f32 v81, v81, v42\n"
    "v_mfma_f32_16x16x32_bf16 v[224:227], a[176:179], v[72:75], v[224:227]\n"
    "buffer_load_dwordx4 v22, s[44:47], s68 offen offset:2048 lds\n"
    "v_mfma_f32_16x16x32_bf16 v[232:235], a[180:183], v[72:75], v[232:235]\n"
    "ds_read_b128 a[116:119], v10 offset:57600\n"
    "v_max3_f32 v83, v83, v59, v60\n"
    "v_mfma_f32_16x16x32_bf16 v[240:243], a[184:187], v[72:75], v[240:243]\n"
    "ds_read_b128 a[120:123], v11 offset:49408\n"
    "v_add_f32 v81, v81, v43\n"
    "v_mfma_f32_16x16x32_bf16 v[248:251], a[188:191], v[72:75], v[248:251]\n"
    "ds_read_b128 a[124:127], v11 offset:57600\n"
    "v_max3_f32 v82, v82, v53, v54\n"
    "v_mfma_f32_16x16x32_bf16 v[132:135], a[128:131], v[76:79], v[132:135]\n"
    "v_add_f32 v81, v81, v44\n"
    "v_mfma_f32_16x16x32_bf16 v[140:143], a[132:135], v[76:79], v[140:143]\n"
    "v_max3_f32 v83, v83, v61, v62\n"
    "v_mfma_f32_16x16x32_bf16 v[148:151], a[136:139], v[76:79], v[148:151]\n"
    "v_add_f32 v81, v81, v45\n"
    "v_mfma_f32_16x16x32_bf16 v[156:159], a[140:143], v[76:79], v[156:159]\n"
    "buffer_load_dwordx4 v23, s[44:47], s68 offen offset:3072 lds\n"
    "v_mfma_f32_16x16x32_bf16 v[164:167], a[144:147], v[76:79], v[164:167]\n"
    "v_max3_f32 v82, v82, v55, v63\n"
    "v_mfma_f32_16x16x32_bf16 v[172:175], a[148:151], v[76:79], v[172:175]\n"
    "v_add_f32 v81, v81, v46\n"
    "v_mfma_f32_16x16x32_bf16 v[180:183], a[152:155], v[76:79], v[180:183]\n"
    "v_max_f32 v82, v82, v83\n"
    "v_mfma_f32_16x16x32_bf16 v[188:191], a[156:159], v[76:79], v[188:191]\n"
    "v_add_f32 v81, v81, v47\n"
    "v_mfma_f32_16x16x32_bf16 v[196:199], a[160:163], v[76:79], v[196:199]\n"
    "v_cmp_lt_f32 vcc, s71, v82\n"
    "v_mfma_f32_16x16x32_bf16 v[204:207], a[164:167], v[76:79], v[204:207]\n"
    "s_waitcnt vmcnt(16) lgkmcnt(0)\n"
    "s_barrier\n"
    "ds_read_b64_tr_b16 a[128:129], v12 offset:49152\n"
    "v_mfma_f32_16x16x32_bf16 v[212:215], a[168:171], v[76:79], v[212:215]\n"
    "ds_read_b64_tr_b16 a[130:131], v12 offset:57344\n"
    "v_mfma_f32_16x16x32_bf16 v[220:223], a[172:175], v[76:79], v[220:223]\n"
    "ds_read_b64_tr_b16 a[132:133], v13 offset:49152\n"
    "v_mfma_f32_16x16x32_bf16 v[228:231], a[176:179], v[76:79], v[228:231]\n"
    "ds_read_b64_tr_b16 a[134:135], v13 offset:57344\n"
    "v_mfma_f32_16x16x32_bf16 v[236:239], a[180:183], v[76:79], v[236:239]\n"
    "ds_read_b64_tr_b16 a[136:137], v14 offset:49152\n"
    "v_mfma_f32_16x16x32_bf16 v[244:247], a[184:187], v[76:79], v[244:247]\n"
    "ds_read_b64_tr_b16 a[138:139], v14 offset:57344\n"
    "v_mfma_f32_16x16x32_bf16 v[252:255], a[188:191], v[76:79], v[252:255]\n"
    "ds_read_b64_tr_b16 a[140:141], v15 offset:49152\n"
    "s_cbranch_vccnz LR66_%=\n"
    "LB66_%=:\n"
    "v_mfma_f32_16x16x32_bf16 v[32:35], a[64:67], a[0:3], v[64:67]\n"
    "ds_read_b64_tr_b16 a[142:143], v15 offset:57344\n"
    "v_exp_f32 v48, v48\n"
    "v_mfma_f32_16x16x32_bf16 v[40:43], a[64:67], a[4:7], v[68:71]\n"
    "ds_read_b64_tr_b16 a[144:145], v16 offset:49152\n"
    "v_exp_f32 v49, v49\n"
    "v_mfma_f32_16x16x32_bf16 v[36:39], a[68:71], a[0:3], v[64:67]\n"
    "s_mov_b32 m0, s66\n"
    "s_add_u32 s69, s69, 0x4000\n"
    "buffer_load_dwordx4 v24, s[48:51], s69 offen offset:0 lds\n"
    "v_mfma_f32_16x16x32_bf16 v[44:47], a[68:71], a[4:7], v[68:71]\n"
    "ds_read_b64_tr_b16 a[146:147], v16 offset:57344\n"
    "v_exp_f32 v50, v50\n"
    "v_mfma_f32_16x16x32_bf16 v[32:35], a[72:75], a[8:11], v[32:35]\n"
    "ds_read_b64_tr_b16 a[148:149], v17 offset:49152\n"
    "v_exp_f32 v51, v51\n"
    "v_mfma_f32_16x16x32_bf16 v[40:43], a[72:75], a[12:15], v[40:43]\n"
    "ds_read_b64_tr_b16 a[150:151], v17 offset:57344\n"
    "v_exp_f32 v52, v52\n"
    "v_mfma_f32_16x16x32_bf16 v[36:39], a[76:79], a[8:11], v[36:39]\n"
    "ds_read_b64_tr_b16 a[152:153], v18 offset:49152\n"
    "v_exp_f32 v53, v53\n"
    "v_mfma_f32_16x16x32_bf16 v[44:47], a[76:79], a[12:15], v[44:47]\n"
    "ds_read_b64_tr_b16 a[154:155], v18 offset:57344\n"
    "v_exp_f32 v54, v54\n"
    "v_mfma_f32_16x16x32_bf16 v[32:35], a[80:83], a[16:19], v[32:35]\n"
    "ds_read_b64_tr_b16 a[156:157], v19 offset:49152\n"
    "v_exp_f32 v55, v55\n"
    "v_mfma_f32_16x16x32_bf16 v[40:43], a[80:83], a[20:23], v[40:43]\n"
    "buffer_load_dwordx4 v25, s[48:51], s69 offen offset:1024 lds\n"
    "v_mfma_f32_16x16x32_bf16 v[36:39], a[84:87], a[16:19], v[36:39]\n"
    "ds_read_b64_tr_b16 a[158:159], v19 offset:57344\n"
    "v_cvt_pk_bf16_f32 v72, v48, v49\n"
    "v_mfma_f32_16x16x32_bf16 v[44:47], a[84:87], a[20:23], v[44:47]\n"
    "ds_read_b64_tr_b16 a[160:161], v12 offset:49408\n"
    "v_cvt_pk_bf16_f32 v73, v50, v51\n"
    "v_mfma_f32_16x16x32_bf16 v[32:35], a[88:91], a[24:27], v[32:35]\n"
    "ds_read_b64_tr_b16 a[162:163], v12 offset:57600\n"
    "v_cvt_pk_bf16_f32 v74, v52, v53\n"
    "v_mfma_f32_16x16x32_bf16 v[40:43], a[88:91], a[28:31], v[40:43]\n"
    "ds_read_b64_tr_b16 a[164:165], v13 offset:49408\n"
    "v_cvt_pk_bf16_f32 v75, v54, v55\n"
    "v_mfma_f32_16x16x32_bf16 v[36:39], a[92:95], a[24:27], v[36:39]\n"
    "ds_read_b64_tr_b16 a[166:167], v13 offset:57600\n"
    "v_add_f32 v80, v80, v48\n"
    "v_mfma_f32_16x16x32_bf16 v[44:47], a[92:95], a[28:31], v[44:47]\n"
    "ds_read_b64_tr_b16 a[168:169], v14 offset:49408\n"
    "v_add_f32 v80, v80, v49\n"
    "v_mfma_f32_16x16x32_bf16 v[32:35], a[96:99], a[32:35], v[32:35]\n"
    "buffer_load_dwordx4 v26, s[48:51], s69 offen offset:2048 lds\n"
    "v_mfma_f32_16x16x32_bf16 v[40:43], a[96:99], a[36:39], v[40:43]\n"
    "ds_read_b64_tr_b16 a[170:171], v14 offset:57600\n"
    "v_add_f32 v80, v80, v50\n"
    "v_mfma_f32_16x16x32_bf16 v[36:39], a[100:103], a[32:35], v[36:39]\n"
    "ds_read_b64_tr_b16 a[172:173], v15 offset:49408\n"
    "v_add_f32 v80, v80, v51\n"
    "v_mfma_f32_16x16x32_bf16 v[44:47], a[100:103], a[36:39], v[44:47]\n"
    "ds_read_b64_tr_b16 a[174:175], v15 offset:57600\n"
    "v_exp_f32 v56, v56\n"
    "v_mfma_f32_16x16x32_bf16 v[32:35], a[104:107], a[40:43], v[32:35]\n"
    "ds_read_b64_tr_b16 a[176:177], v16 offset:49408\n"
    "v_exp_f32 v57, v57\n"
    "v_mfma_f32_16x16x32_bf16 v[40:43], a[104:107], a[44:47], v[40:43]\n"
    "ds_read_b64_tr_b16 a[178:179], v16 offset:57600\n"
    "v_add_f32 v80, v80, v52\n"
    "v_mfma_f32_16x16x32_bf16 v[36:39], a[108:111], a[40:43], v[36:39]\n"
    "ds_read_b64_tr_b16 a[180:181], v17 offset:49408\n"
    "v_exp_f32 v58, v58\n"
    "v_mfma_f32_16x16x32_bf16 v[44:47], a[108:111], a[44:47], v[44:47]\n"
    "buffer_load_dwordx4 v27, s[48:51], s69 offen offset:3072 lds\n"
    "v_mfma_f32_16x16x32_bf16 v[32:35], a[112:115], a[48:51], v[32:35]\n"
    "ds_read_b64_tr_b16 a[182:183], v17 offset:57600\n"
    "v_exp_f32 v59, v59\n"
    "v_mfma_f32_16x16x32_bf16 v[40:43], a[112:115], a[52:55], v[40:43]\n"
    "ds_read_b64_tr_b16 a[184:185], v18 offset:49408\n"
    "v_add_f32 v80, v80, v53\n"
    "v_mfma_f32_16x16x32_bf16 v[36:39], a[116:119], a[48:51], v[36:39]\n"
    "ds_read_b64_tr_b16 a[186:187], v18 offset:57600\n"
    "v_exp_f32 v60, v60\n"
    "v_mfma_f32_16x16x32_bf16 v[44:47], a[116:119], a[52:55], v[44:47]\n"
    "ds_read_b64_tr_b16 a[188:189], v19 offset:49408\n"
    "v_exp_f32 v61, v61\n"
    "v_mfma_f32_16x16x32_bf16 v[32:35], a[120:123], a[56:59], v[32:35]\n"
    "ds_read_b64_tr_b16 a[190:191], v19 offset:57600\n"
    "v_add_f32 v80, v80, v54\n"
    "v_mfma_f32_16x16x32_bf16 v[40:43], a[120:123], a[60:63], v[40:43]\n"
    "ds_read_b128 a[64:67], v8 offset:0\n"
    "v_exp_f32 v62, v62\n"
    "v_mfma_f32_16x16x32_bf16 v[36:39], a[124:127], a[56:59], v[36:39]\n"
    "ds_read_b128 a[68:71], v8 offset:8192\n"
    "v_exp_f32 v63, v63\n"
    "v_mfma_f32_16x16x32_bf16 v[44:47], a[124:127], a[60:63], v[44:47]\n"
    "ds_read_b128 a[72:75], v9 offset:0\n"
    "v_add_f32 v80, v80, v55\n"
    "s_waitcnt lgkmcnt(3)\n"
    "v_mfma_f32_16x16x32_bf16 v[128:131], a[128:131], v[72:75], v[128:131]\n"
    "ds_read_b128 a[76:79], v9 offset:8192\n"
    "v_cvt_pk_bf16_f32 v76, v56, v57\n"
    "v_mfma_f32_16x16x32_bf16 v[136:139], a[132:135], v[72:75], v[136:139]\n"
    "ds_read_b128 a[80:83], v10 offset:0\n"
    "v_cvt_pk_bf16_f32 v77, v58, v59\n"
    "v_mfma_f32_16x16x32_bf16 v[144:147], a[136:139], v[72:75], v[144:147]\n"
    "ds_read_b128 a[84:87], v10 offset:8192\n"
    "v_cvt_pk_bf16_f32 v78, v60, v61\n"
    "v_mfma_f32_16x16x32_bf16 v[152:155], a[140:143], v[72:75], v[152:155]\n"
    "ds_read_b128 a[88:91], v11 offset:0\n"
    "v_cvt_pk_bf16_f32 v79, v62, v63\n"
    "v_mfma_f32_16x16x32_bf16 v[160:163], a[144:147], v[72:75], v[160:163]\n"
    "ds_read_b128 a[92:95], v11 offset:8192\n"
    "v_max3_f32 v82, v32, v33, v34\n"
    "v_mfma_f32_16x16x32_bf16 v[168:171], a[148:151], v[72:75], v[168:171]\n"
    "ds_read_b128 a[96:99], v8 offset:256\n"
    "v_add_f32 v81, v81, v56\n"
    "v_mfma_f32_16x16x32_bf16 v[176:179], a[152:155], v[72:75], v[176:179]\n"
    "ds_read_b128 a[100:103], v8 offset:8448\n"
    "v_max3_f32 v83, v40, v41, v42\n"
    "v_mfma_f32_16x16x32_bf16 v[184:187], a[156:159], v[72:75], v[184:187]\n"
    "ds_read_b128 a[104:107], v9 offset:256\n"
    "v_add_f32 v81, v81, v57\n"
    "v_mfma_f32_16x16x32_bf16 v[192:195], a[160:163], v[72:75], v[192:195]\n"
    "ds_read_b128 a[108:111], v9 offset:8448\n"
    "v_max3_f32 v82, v82, v35, v36\n"
    "v_mfma_f32_16x16x32_bf16 v[200:203], a[164:167], v[72:75], v[200:203]\n"
    "ds_read_b128 a[112:115], v10 offset:256\n"
    "v_add_f32 v81, v81, v58\n"
    "v_mfma_f32_16x16x32_bf16 v[208:211], a[168:171], v[72:75], v[208:211]\n"
    "ds_read_b128 a[116:119], v10 offset:8448\n"
    "v_max3_f32 v83, v83, v43, v44\n"
    "v_mfma_f32_16x16x32_bf16 v[216:219], a[172:175], v[72:75], v[216:219]\n"
    "ds_read_b128 a[120:123], v11 offset:256\n"
    "v_add_f32 v81, v81, v59\n"
    "v_mfma_f32_16x16x32_bf16 v[224:227], a[176:179], v[72:75], v[224:227]\n"
    "ds_read_b128 a[124:127], v11 offset:8448\n"
    "v_max3_f32 v82, v82, v37, v38\n"
    "v_mfma_f32_16x16x32_bf16 v[232:235], a[180:183], v[72:75], v[232:235]\n"
    "v_add_f32 v81, v81, v60\n"
    "v_mfma_f32_16x16x32_bf16 v[240:243], a[184:187], v[72:75], v[240:243]\n"
    "v_max3_f32 v83, v83, v45, v46\n"
    "v_mfma_f32_16x16x32_bf16 v[248:251], a[188:191], v[72:75], v[248:251]\n"
    "v_add_f32 v81, v81, v61\n"
    "v_mfma_f32_16x16x32_bf16 v[132:135], a[128:131], v[76:79], v[132:135]\n"
    "v_max3_f32 v82, v82, v39, v47\n"
    "v_mfma_f32_16x16x32_bf16 v[140:143], a[132:135], v[76:79], v[140:143]\n"
    "v_add_f32 v81, v81, v62\n"
    "v_mfma_f32_16x16x32_bf16 v[148:151], a[136:139], v[76:79], v[148:151]\n"
    "v_max_f32 v82, v82, v83\n"
    "v_mfma_f32_16x16x32_bf16 v[156:159], a[140:143], v[76:79], v[156:159]\n"
    "v_add_f32 v81, v81, v63\n"
    "v_mfma_f32_16x16x32_bf16 v[164:167], a[144:147], v[76:79], v[164:167]\n"
    "v_cmp_lt_f32 vcc, s71, v82\n"
    "v_mfma_f32_16x16x32_bf16 v[172:175], a[148:151], v[76:79], v[172:175]\n"
    "v_mfma_f32_16x16x32_bf16 v[180:183], a[152:155], v[76:79], v[180:183]\n"
    "v_mfma_f32_16x16x32_bf16 v[188:191], a[156:159], v[76:79], v[188:191]\n"
    "v_mfma_f32_16x16x32_bf16 v[196:199], a[160:163], v[76:79], v[196:199]\n"
    "v_mfma_f32_16x16x32_bf16 v[204:207], a[164:167], v[76:79], v[204:207]\n"
    "s_waitcnt vmcnt(12) lgkmcnt(0)\n"
    "s_barrier\n"
    "ds_read_b64_tr_b16 a[128:129], v12 offset:0\n"
    "v_mfma_f32_16x16x32_bf16 v[212:215], a[168:171], v[76:79], v[212:215]\n"
    "ds_read_b64_tr_b16 a[130:131], v12 offset:8192\n"
    "v_mfma_f32_16x16x32_bf16 v[220:223], a[172:175], v[76:79], v[220:223]\n"
    "ds_read_b64_tr_b16 a[132:133], v13 offset:0\n"
    "v_mfma_f32_16x16x32_bf16 v[228:231], a[176:179], v[76:79], v[228:231]\n"
    "ds_read_b64_tr_b16 a[134:135], v13 offset:8192\n"
    "v_mfma_f32_16x16x32_bf16 v[236:239], a[180:183], v[76:79], v[236:239]\n"
    "ds_read_b64_tr_b16 a[136:137], v14 offset:0\n"
    "v_mfma_f32_16x16x32_bf16 v[244:247], a[184:187], v[76:79], v[244:247]\n"
    "ds_read_b64_tr_b16 a[138:139], v14 offset:8192\n"
    "v_mfma_f32_16x16x32_bf16 v[252:255], a[188:191], v[76:79], v[252:255]\n"
    "ds_read_b64_tr_b16 a[140:141], v15 offset:0\n"
    "s_cbranch_vccnz LR67_%=\n"
    "LB67_%=:\n"
    "v_mfma_f32_16x16x32_bf16 v[48:51], a[64:67], a[0:3], v[64:67]\n"
    "ds_read_b64_tr_b16 a[142:143], v15 offset:8192\n"
    "v_exp_f32 v32, v32\n"
    "v_mfma_f32_16x16x32_bf16 v[56:59], a[64:67], a[4:7], v[68:71]\n"
    "ds_read_b64_tr_b16 a[144:145], v16 offset:0\n"
    "v_exp_f32 v33, v33\n"
    "v_mfma_f32_16x16x32_bf16 v[52:55], a[68:71], a[0:3], v[64:67]\n"
    "s_mov_b32 m0, s67\n"
    "s_mov_b32 s69, 0xfc000\n"
    "buffer_load_dwordx4 v24, s[48:51], s69 offen offset:0 lds\n"
    "v_mfma_f32_16x16x32_bf16 v[60:63], a[68:71], a[4:7], v[68:71]\n"
    "ds_read_b64_tr_b16 a[146:147], v16 offset:8192\n"
    "v_exp_f32 v34, v34\n"
    "v_mfma_f32_16x16x32_bf16 v[48:51], a[72:75], a[8:11], v[48:51]\n"
    "ds_read_b64_tr_b16 a[148:149], v17 offset:0\n"
    "v_exp_f32 v35, v35\n"
    "v_mfma_f32_16x16x32_bf16 v[56:59], a[72:75], a[12:15], v[56:59]\n"
    "ds_read_b64_tr_b16 a[150:151], v17 offset:8192\n"
    "v_exp_f32 v36, v36\n"
    "v_mfma_f32_16x16x32_bf16 v[52:55], a[76:79], a[8:11], v[52:55]\n"
    "ds_read_b64_tr_b16 a[152:153], v18 offset:0\n"
    "v_exp_f32 v37, v37\n"
    "v_mfma_f32_16x16x32_bf16 v[60:63], a[76:79], a[12:15], v[60:63]\n"
    "ds_read_b64_tr_b16 a[154:155], v18 offset:8192\n"
    "v_exp_f32 v38, v38\n"
    "v_mfma_f32_16x16x32_bf16 v[48:51], a[80:83], a[16:19], v[48:51]\n"
    "ds_read_b64_tr_b16 a[156:157], v19 offset:0\n"
    "v_exp_f32 v39, v39\n"
    "v_mfma_f32_16x16x32_bf16 v[56:59], a[80:83], a[20:23], v[56:59]\n"
    "buffer_load_dwordx4 v25, s[48:51], s69 offen offset:1024 lds\n"
    "v_mfma_f32_16x16x32_bf16 v[52:55], a[84:87], a[16:19], v[52:55]\n"
    "ds_read_b64_tr_b16 a[158:159], v19 offset:8192\n"
    "v_cvt_pk_bf16_f32 v72, v32, v33\n"
    "v_mfma_f32_16x16x32_bf16 v[60:63], a[84:87], a[20:23], v[60:63]\n"
    "ds_read_b64_tr_b16 a[160:161], v12 offset:256\n"
    "v_cvt_pk_bf16_f32 v73, v34, v35\n"
    "v_mfma_f32_16x16x32_bf16 v[48:51], a[88:91], a[24:27], v[48:51]\n"
    "ds_read_b64_tr_b16 a[162:163], v12 offset:8448\n"
    "v_cvt_pk_bf16_f32 v74, v36, v37\n"
    "v_mfma_f32_16x16x32_bf16 v[56:59], a[88:91], a[28:31], v[56:59]\n"
    "ds_read_b64_tr_b16 a[164:165], v13 offset:256\n"
    "v_cvt_pk_bf16_f32 v75, v38, v39\n"
    "v_mfma_f32_16x16x32_bf16 v[52:55], a[92:95], a[24:27], v[52:55]\n"
    "ds_read_b64_tr_b16 a[166:167], v13 offset:8448\n"
    "v_add_f32 v80, v80, v32\n"
    "v_mfma_f32_16x16x32_bf16 v[60:63], a[92:95], a[28:31], v[60:63]\n"
    "ds_read_b64_tr_b16 a[168:169], v14 offset:256\n"
    "v_add_f32 v80, v80, v33\n"
    "v_mfma_f32_16x16x32_bf16 v[48:51], a[96:99], a[32:35], v[48:51]\n"
    "buffer_load_dwordx4 v26, s[48:51], s69 offen offset:2048 lds\n"
    "v_mfma_f32_16x16x32_bf16 v[56:59], a[96:99], a[36:39], v[56:59]\n"
    "ds_read_b64_tr_b16 a[170:171], v14 offset:8448\n"
    "v_add_f32 v80, v80, v34\n"
    "v_mfma_f32_16x16x32_bf16 v[52:55], a[100:103], a[32:35], v[52:55]\n"
    "ds_read_b64_tr_b16 a[172:173], v15 offset:256\n"
    "v_add_f32 v80, v80, v35\n"
    "v_mfma_f32_16x16x32_bf16 v[60:63], a[100:103], a[36:39], v[60:63]\n"
    "ds_read_b64_tr_b16 a[174:175], v15 offset:8448\n"
    "v_exp_f32 v40, v40\n"
    "v_mfma_f32_16x16x32_bf16 v[48:51], a[104:107], a[40:43], v[48:51]\n"
    "ds_read_b64_tr_b16 a[176:177], v16 offset:256\n"
    "v_exp_f32 v41, v41\n"
    "v_mfma_f32_16x16x32_bf16 v[56:59], a[104:107], a[44:47], v[56:59]\n"
    "ds_read_b64_tr_b16 a[178:179], v16 offset:8448\n"
    "v_add_f32 v80, v80, v36\n"
    "v_mfma_f32_16x16x32_bf16 v[52:55], a[108:111], a[40:43], v[52:55]\n"
    "ds_read_b64_tr_b16 a[180:181], v17 offset:256\n"
    "v_exp_f32 v42, v42\n"
    "v_mfma_f32_16x16x32_bf16 v[60:63], a[108:111], a[44:47], v[60:63]\n"
    "buffer_load_dwordx4 v27, s[48:51], s69 offen offset:3072 lds\n"
    "v_mfma_f32_16x16x32_bf16 v[48:51], a[112:115], a[48:51], v[48:51]\n"
    "ds_read_b64_tr_b16 a[182:183], v17 offset:8448\n"
    "v_exp_f32 v43, v43\n"
    "v_mfma_f32_16x16x32_bf16 v[56:59], a[112:115], a[52:55], v[56:59]\n"
    "ds_read_b64_tr_b16 a[184:185], v18 offset:256\n"
    "v_add_f32 v80, v80, v37\n"
    "v_mfma_f32_16x16x32_bf16 v[52:55], a[116:119], a[48:51], v[52:55]\n"
    "ds_read_b64_tr_b16 a[186:187], v18 offset:8448\n"
    "v_exp_f32 v44, v44\n"
    "v_mfma_f32_16x16x32_bf16 v[60:63], a[116:119], a[52:55], v[60:63]\n"
    "ds_read_b64_tr_b16 a[188:189], v19 offset:256\n"
    "v_exp_f32 v45, v45\n"
    "v_mfma_f32_16x16x32_bf16 v[48:51], a[120:123], a[56:59], v[48:51]\n"
    "ds_read_b64_tr_b16 a[190:191], v19 offset:8448\n"
    "v_add_f32 v80, v80, v38\n"
    "v_mfma_f32_16x16x32_bf16 v[56:59], a[120:123], a[60:63], v[56:59]\n"
    "ds_read_b128 a[64:67], v8 offset:16384\n"
    "v_exp_f32 v46, v46\n"
    "v_mfma_f32_16x16x32_bf16 v[52:55], a[124:127], a[56:59], v[52:55]\n"
    "s_mov_b32 m0, s60\n"
    "s_mov_b32 s70, 0\n"
    "buffer_load_dwordx4 v28, s[52:55], s70 offen offset:0 lds\n"
    "v_mfma_f32_16x16x32_bf16 v[60:63], a[124:127], a[60:63], v[60:63]\n"
    "ds_read_b128 a[68:71], v8 offset:24576\n"
    "v_exp_f32 v47, v47\n"
    "s_waitcnt lgkmcnt(2)\n"
    "v_mfma_f32_16x16x32_bf16 v[128:131], a[128:131], v[72:75], v[128:131]\n"
    "ds_read_b128 a[72:75], v9 offset:16384\n"
    "v_add_f32 v80, v80, v39\n"
    "v_mfma_f32_16x16x32_bf16 v[136:139], a[132:135], v[72:75], v[136:139]\n"
    "ds_read_b128 a[76:79], v9 offset:24576\n"
    "v_cvt_pk_bf16_f32 v76, v40, v41\n"
    "v_mfma_f32_16x16x32_bf16 v[144:147], a[136:139], v[72:75], v[144:147]\n"
    "ds_read_b128 a[80:83], v10 offset:16384\n"
    "v_cvt_pk_bf16_f32 v77, v42, v43\n"
    "v_mfma_f32_16x16x32_bf16 v[152:155], a[140:143], v[72:75], v[152:155]\n"
    "ds_read_b128 a[84:87], v10 offset:24576\n"
    "v_cvt_pk_bf16_f32 v78, v44, v45\n"
    "v_mfma_f32_16x16x32_bf16 v[160:163], a[144:147], v[72:75], v[160:163]\n"
    "ds_read_b128 a[88:91], v11 offset:16384\n"
    "v_cvt_pk_bf16_f32 v79, v46, v47\n"
    "v_mfma_f32_16x16x32_bf16 v[168:171], a[148:151], v[72:75], v[168:171]\n"
    "buffer_load_dwordx4 v29, s[52:55], s70 offen offset:1024 lds\n"
    "v_mfma_f32_16x16x32_bf16 v[176:179], a[152:155], v[72:75], v[176:179]\n"
    "ds_read_b128 a[92:95], v11 offset:24576\n"
    "v_max3_f32 v82, v48, v49, v50\n"
    "v_mfma_f32_16x16x32_bf16 v[184:187], a[156:159], v[72:75], v[184:187]\n"
    "ds_read_b128 a[96:99], v8 offset:16640\n"
    "v_add_f32 v81, v81, v40\n"
    "v_mfma_f32_16x16x32_bf16 v[192:195], a[160:163], v[72:75], v[192:195]\n"
    "ds_read_b128 a[100:103], v8 offset:24832\n"
    "v_max3_f32 v83, v56, v57, v58\n"
    "v_mfma_f32_16x16x32_bf16 v[200:203], a[164:167], v[72:75], v[200:203]\n"
    "ds_read_b128 a[104:107], v9 offset:16640\n"
    "v_add_f32 v81, v81, v41\n"
    "v_mfma_f32_16x16x32_bf16 v[208:211], a[168:171], v[72:75], v[208:211]\n"
    "ds_read_b128 a[108:111], v9 offset:24832\n"
    "v_max3_f32 v82, v82, v51, v52\n"
    "v_mfma_f32_16x16x32_bf16 v[216:219], a[172:175], v[72:75], v[216:219]\n"
    "ds_read_b128 a[112:115], v10 offset:16640\n"
    "v_add_f32 v81, v81, v42\n"
    "v_mfma_f32_16x16x32_bf16 v[224:227], a[176:179], v[72:75], v[224:227]\n"
    "buffer_load_dwordx4 v30, s[52:55], s70 offen offset:2048 lds\n"
    "v_mfma_f32_16x16x32_bf16 v[232:235], a[180:183], v[72:75], v[232:235]\n"
    "ds_read_b128 a[116:119], v10 offset:24832\n"
    "v_max3_f32 v83, v83, v59, v60\n"
    "v_mfma_f32_16x16x32_bf16 v[240:243], a[184:187], v[72:75], v[240:243]\n"
    "ds_read_b128 a[120:123], v11 offset:16640\n"
    "v_add_f32 v81, v81, v43\n"
    "v_mfma_f32_16x16x32_bf16 v[248:251], a[188:191], v[72:75], v[248:251]\n"
    "ds_read_b128 a[124:127], v11 offset:24832\n"
    "v_max3_f32 v82, v82, v53, v54\n"
    "v_mfma_f32_16x16x32_bf16 v[132:135], a[128:131], v[76:79], v[132:135]\n"
    "v_add_f32 v81, v81, v44\n"
    "v_mfma_f32_16x16x32_bf16 v[140:143], a[132:135], v[76:79], v[140:143]\n"
    "v_max3_f32 v83, v83, v61, v62\n"
    "v_mfma_f32_16x16x32_bf16 v[148:151], a[136:139], v[76:79], v[148:151]\n"
    "v_add_f32 v81, v81, v45\n"
    "v_mfma_f32_16x16x32_bf16 v[156:159], a[140:143], v[76:79], v[156:159]\n"
    "buffer_load_dwordx4 v31, s[52:55], s70 offen offset:3072 lds\n"
    "v_mfma_f32_16x16x32_bf16 v[164:167], a[144:147], v[76:79], v[164:167]\n"
    "v_max3_f32 v82, v82, v55, v63\n"
    "v_mfma_f32_16x16x32_bf16 v[172:175], a[148:151], v[76:79], v[172:175]\n"
    "v_add_f32 v81, v81, v46\n"
    "v_mfma_f32_16x16x32_bf16 v[180:183], a[152:155], v[76:79], v[180:183]\n"
    "v_max_f32 v82, v82, v83\n"
    "v_mfma_f32_16x16x32_bf16 v[188:191], a[156:159], v[76:79], v[188:191]\n"
    "v_add_f32 v81, v81, v47\n"
    "v_mfma_f32_16x16x32_bf16 v[196:199], a[160:163], v[76:79], v[196:199]\n"
    "v_cmp_lt_f32 vcc, s71, v82\n"
    "v_mfma_f32_16x16x32_bf16 v[204:207], a[164:167], v[76:79], v[204:207]\n"
    "s_waitcnt vmcnt(12) lgkmcnt(0)\n"
    "s_barrier\n"
    "ds_read_b64_tr_b16 a[128:129], v12 offset:16384\n"
    "v_mfma_f32_16x16x32_bf16 v[212:215], a[168:171], v[76:79], v[212:215]\n"
    "ds_read_b64_tr_b16 a[130:131], v12 offset:24576\n"
    "v_mfma_f32_16x16x32_bf16 v[220:223], a[172:175], v[76:79], v[220:223]\n"
    "ds_read_b64_tr_b16 a[132:133], v13 offset:16384\n"
    "v_mfma_f32_16x16x32_bf16 v[228:231], a[176:179], v[76:79], v[228:231]\n"
    "ds_read_b64_tr_b16 a[134:135], v13 offset:24576\n"
    "v_mfma_f32_16x16x32_bf16 v[236:239], a[180:183], v[76:79], v[236:239]\n"
    "ds_read_b64_tr_b16 a[136:137], v14 offset:16384\n"
    "v_mfma_f32_16x16x32_bf16 v[244:247], a[184:187], v[76:79], v[244:247]\n"
    "ds_read_b64_tr_b16 a[138:139], v14 offset:24576\n"
    "v_mfma_f32_16x16x32_bf16 v[252:255], a[188:191], v[76:79], v[252:255]\n"
    "ds_read_b64_tr_b16 a[140:141], v15 offset:16384\n"
    "s_cbranch_vccnz LR68_%=\n"
    "LB68_%=:\n"
    "v_mfma_f32_16x16x32_bf16 v[32:35], a[64:67], a[0:3], v[64:67]\n"
    "ds_read_b64_tr_b16 a[142:143], v15 offset:24576\n"
    "v_exp_f32 v48, v48\n"
    "v_mfma_f32_16x16x32_bf16 v[40:43], a[64:67], a[4:7], v[68:71]\n"
    "ds_read_b64_tr_b16 a[144:145], v16 offset:16384\n"
    "v_exp_f32 v49, v49\n"
    "v_mfma_f32_16x16x32_bf16 v[36:39], a[68:71], a[0:3], v[64:67]\n"
    "s_mov_b32 m0, s64\n"
    "s_mov_b32 s70, 0x1c000\n"
    "buffer_load_dwordx4 v28, s[52:55], s70 offen offset:0 lds\n"
    "v_mfma_f32_16x16x32_bf16 v[44:47], a[68:71], a[4:7], v[68:71]\n"
    "ds_read_b64_tr_b16 a[146:147], v16 offset:24576\n"
    "v_exp_f32 v50, v50\n"
    "v_mfma_f32_16x16x32_bf16 v[32:35], a[72:75], a[8:11], v[32:35]\n"
    "ds_read_b64_tr_b16 a[148:149], v17 offset:16384\n"
    "v_exp_f32 v51, v51\n"
    "v_mfma_f32_16x16x32_bf16 v[40:43], a[72:75], a[12:15], v[40:43]\n"
    "ds_read_b64_tr_b16 a[150:151], v17 offset:24576\n"
    "v_exp_f32 v52, v52\n"
    "v_mfma_f32_16x16x32_bf16 v[36:39], a[76:79], a[8:11], v[36:39]\n"
    "ds_read_b64_tr_b16 a[152:153], v18 offset:16384\n"
    "v_exp_f32 v53, v53\n"
    "v_mfma_f32_16x16x32_bf16 v[44:47], a[76:79], a[12:15], v[44:47]\n"
    "ds_read_b64_tr_b16 a[154:155], v18 offset:24576\n"
    "v_exp_f32 v54, v54\n"
    "v_mfma_f32_16x16x32_bf16 v[32:35], a[80:83], a[16:19], v[32:35]\n"
    "ds_read_b64_tr_b16 a[156:157], v19 offset:16384\n"
    "v_exp_f32 v55, v55\n"
    "v_mfma_f32_16x16x32_bf16 v[40:43], a[80:83], a[20:23], v[40:43]\n"
    "buffer_load_dwordx4 v29, s[52:55], s70 offen offset:1024 lds\n"
    "v_mfma_f32_16x16x32_bf16 v[36:39], a[84:87], a[16:19], v[36:39]\n"
    "ds_read_b64_tr_b16 a[158:159], v19 offset:24576\n"
    "v_cvt_pk_bf16_f32 v72, v48, v49\n"
    "v_mfma_f32_16x16x32_bf16 v[44:47], a[84:87], a[20:23], v[44:47]\n"
    "ds_read_b64_tr_b16 a[160:161], v12 offset:16640\n"
    "v_cvt_pk_bf16_f32 v73, v50, v51\n"
    "v_mfma_f32_16x16x32_bf16 v[32:35], a[88:91], a[24:27], v[32:35]\n"
    "ds_read_b64_tr_b16 a[162:163], v12 offset:24832\n"
    "v_cvt_pk_bf16_f32 v74, v52, v53\n"
    "v_mfma_f32_16x16x32_bf16 v[40:43], a[88:91], a[28:31], v[40:43]\n"
    "ds_read_b64_tr_b16 a[164:165], v13 offset:16640\n"
    "v_cvt_pk_bf16_f32 v75, v54, v55\n"
    "v_mfma_f32_16x16x32_bf16 v[36:39], a[92:95], a[24:27], v[36:39]\n"
    "ds_read_b64_tr_b16 a[166:167], v13 offset:24832\n"
    "v_add_f32 v80, v80, v48\n"
    "v_mfma_f32_16x16x32_bf16 v[44:47], a[92:95], a[28:31], v[44:47]\n"
    "ds_read_b64_tr_b16 a[168:169], v14 offset:16640\n"
    "v_add_f32 v80, v80, v49\n"
    "v_mfma_f32_16x16x32_bf16 v[32:35], a[96:99], a[32:35], v[32:35]\n"
    "buffer_load_dwordx4 v30, s[52:55], s70 offen offset:2048 lds\n"
    "v_mfma_f32_16x16x32_bf16 v[40:43], a[96:99], a[36:39], v[40:43]\n"
    "ds_read_b64_tr_b16 a[170:171], v14 offset:24832\n"
    "v_add_f32 v80, v80, v50\n"
    "v_mfma_f32_16x16x32_bf16 v[36:39], a[100:103], a[32:35], v[36:39]\n"
    "ds_read_b64_tr_b16 a[172:173], v15 offset:16640\n"
    "v_add_f32 v80, v80, v51\n"
    "v_mfma_f32_16x16x32_bf16 v[44:47], a[100:103], a[36:39], v[44:47]\n"
    "ds_read_b64_tr_b16 a[174:175], v15 offset:24832\n"
    "v_exp_f32 v56, v56\n"
    "v_mfma_f32_16x16x32_bf16 v[32:35], a[104:107], a[40:43], v[32:35]\n"
    "ds_read_b64_tr_b16 a[176:177], v16 offset:16640\n"
    "v_exp_f32 v57, v57\n"
    "v_mfma_f32_16x16x32_bf16 v[40:43], a[104:107], a[44:47], v[40:43]\n"
    "ds_read_b64_tr_b16 a[178:179], v16 offset:24832\n"
    "v_add_f32 v80, v80, v52\n"
    "v_mfma_f32_16x16x32_bf16 v[36:39], a[108:111], a[40:43], v[36:39]\n"
    "ds_read_b64_tr_b16 a[180:181], v17 offset:16640\n"
    "v_exp_f32 v58, v58\n"
    "v_mfma_f32_16x16x32_bf16 v[44:47], a[108:111], a[44:47], v[44:47]\n"
    "buffer_load_dwordx4 v31, s[52:55], s70 offen offset:3072 lds\n"
    "v_mfma_f32_16x16x32_bf16 v[32:35], a[112:115], a[48:51], v[32:35]\n"
    "ds_read_b64_tr_b16 a[182:183], v17 offset:24832\n"
    "v_exp_f32 v59, v59\n"
    "v_mfma_f32_16x16x32_bf16 v[40:43], a[112:115], a[52:55], v[40:43]\n"
    "ds_read_b64_tr_b16 a[184:185], v18 offset:16640\n"
    "v_add_f32 v80, v80, v53\n"
    "v_mfma_f32_16x16x32_bf16 v[36:39], a[116:119], a[48:51], v[36:39]\n"
    "ds_read_b64_tr_b16 a[186:187], v18 offset:24832\n"
    "v_exp_f32 v60, v60\n"
    "v_mfma_f32_16x16x32_bf16 v[44:47], a[116:119], a[52:55], v[44:47]\n"
    "ds_read_b64_tr_b16 a[188:189], v19 offset:16640\n"
    "v_exp_f32 v61, v61\n"
    "v_mfma_f32_16x16x32_bf16 v[32:35], a[120:123], a[56:59], v[32:35]\n"
    "ds_read_b64_tr_b16 a[190:191], v19 offset:24832\n"
    "v_add_f32 v80, v80, v54\n"
    "v_mfma_f32_16x16x32_bf16 v[40:43], a[120:123], a[60:63], v[40:43]\n"
    "ds_read_b128 a[64:67], v8 offset:32768\n"
    "v_exp_f32 v62, v62\n"
    "v_mfma_f32_16x16x32_bf16 v[36:39], a[124:127], a[56:59], v[36:39]\n"
    "s_mov_b32 m0, s61\n"
    "s_mov_b32 s70, 0x80\n"
    "buffer_load_dwordx4 v28, s[52:55], s70 offen offset:0 lds\n"
    "v_mfma_f32_16x16x32_bf16 v[44:47], a[124:127], a[60:63], v[44:47]\n"
    "ds_read_b128 a[68:71], v8 offset:40960\n"
    "v_exp_f32 v63, v63\n"
    "s_waitcnt lgkmcnt(2)\n"
    "v_mfma_f32_16x16x32_bf16 v[128:131], a[128:131], v[72:75], v[128:131]\n"
    "ds_read_b128 a[72:75], v9 offset:32768\n"
    "v_add_f32 v80, v80, v55\n"
    "v_mfma_f32_16x16x32_bf16 v[136:139], a[132:135], v[72:75], v[136:139]\n"
    "ds_read_b128 a[76:79], v9 offset:40960\n"
    "v_cvt_pk_bf16_f32 v76, v56, v57\n"
    "v_mfma_f32_16x16x32_bf16 v[144:147], a[136:139], v[72:75], v[144:147]\n"
    "ds_read_b128 a[80:83], v10 offset:32768\n"
    "v_cvt_pk_bf16_f32 v77, v58, v59\n"
    "v_mfma_f32_16x16x32_bf16 v[152:155], a[140:143], v[72:75], v[152:155]\n"
    "ds_read_b128 a[84:87], v10 offset:40960\n"
    "v_cvt_pk_bf16_f32 v78, v60, v61\n"
    "v_mfma_f32_16x16x32_bf16 v[160:163], a[144:147], v[72:75], v[160:163]\n"
    "ds_read_b128 a[88:91], v11 offset:32768\n"
    "v_cvt_pk_bf16_f32 v79, v62, v63\n"
    "v_mfma_f32_16x16x32_bf16 v[168:171], a[148:151], v[72:75], v[168:171]\n"
    "buffer_load_dwordx4 v29, s[52:55], s70 offen offset:1024 lds\n"
    "v_mfma_f32_16x16x32_bf16 v[176:179], a[152:155], v[72:75], v[176:179]\n"
    "ds_read_b128 a[92:95], v11 offset:40960\n"
    "v_max3_f32 v82, v32, v33, v34\n"
    "v_mfma_f32_16x16x32_bf16 v[184:187], a[156:159], v[72:75], v[184:187]\n"
    "ds_read_b128 a[96:99], v8 offset:33024\n"
    "v_add_f32 v81, v81, v56\n"
    "v_mfma_f32_16x16x32_bf16 v[192:195], a[160:163], v[72:75], v[192:195]\n"
    "ds_read_b128 a[100:103], v8 offset:41216\n"
    "v_max3_f32 v83, v40, v41, v42\n"
    "v_mfma_f32_16x16x32_bf16 v[200:203], a[164:167], v[72:75], v[200:203]\n"
    "ds_read_b128 a[104:107], v9 offset:33024\n"
    "v_add_f32 v81, v81, v57\n"
    "v_mfma_f32_16x16x32_bf16 v[208:211], a[168:171], v[72:75], v[208:211]\n"
    "ds_read_b128 a[108:111], v9 offset:41216\n"
    "v_max3_f32 v82, v82, v35, v36\n"
    "v_mfma_f32_16x16x32_bf16 v[216:219], a[172:175], v[72:75], v[216:219]\n"
    "ds_read_b128 a[112:115], v10 offset:33024\n"
    "v_add_f32 v81, v81, v58\n"
    "v_mfma_f32_16x16x32_bf16 v[224:227], a[176:179], v[72:75], v[224:227]\n"
    "buffer_load_dwordx4 v30, s[52:55], s70 offen offset:2048 lds\n"
    "v_mfma_f32_16x16x32_bf16 v[232:235], a[180:183], v[72:75], v[232:235]\n"
    "ds_read_b128 a[116:119], v10 offset:41216\n"
    "v_max3_f32 v83, v83, v43, v44\n"
    "v_mfma_f32_16x16x32_bf16 v[240:243], a[184:187], v[72:75], v[240:243]\n"
    "ds_read_b128 a[120:123], v11 offset:33024\n"
    "v_add_f32 v81, v81, v59\n"
    "v_mfma_f32_16x16x32_bf16 v[248:251], a[188:191], v[72:75], v[248:251]\n"
    "ds_read_b128 a[124:127], v11 offset:41216\n"
    "v_max3_f32 v82, v82, v37, v38\n"
    "v_mfma_f32_16x16x32_bf16 v[132:135], a[128:131], v[76:79], v[132:135]\n"
    "v_add_f32 v81, v81, v60\n"
    "v_mfma_f32_16x16x32_bf16 v[140:143], a[132:135], v[76:79], v[140:143]\n"
    "v_max3_f32 v83, v83, v45, v46\n"
    "v_mfma_f32_16x16x32_bf16 v[148:151], a[136:139], v[76:79], v[148:151]\n"
    "v_add_f32 v81, v81, v61\n"
    "v_mfma_f32_16x16x32_bf16 v[156:159], a[140:143], v[76:79], v[156:159]\n"
    "buffer_load_dwordx4 v31, s[52:55], s70 offen offset:3072 lds\n"
    "v_mfma_f32_16x16x32_bf16 v[164:167], a[144:147], v[76:79], v[164:167]\n"
    "v_max3_f32 v82, v82, v39, v47\n"
    "v_mfma_f32_16x16x32_bf16 v[172:175], a[148:151], v[76:79], v[172:175]\n"
    "v_add_f32 v81, v81, v62\n"
    "v_mfma_f32_16x16x32_bf16 v[180:183], a[152:155], v[76:79], v[180:183]\n"
    "v_max_f32 v82, v82, v83\n"
    "v_mfma_f32_16x16x32_bf16 v[188:191], a[156:159], v[76:79], v[188:191]\n"
    "v_add_f32 v81, v81, v63\n"
    "v_mfma_f32_16x16x32_bf16 v[196:199], a[160:163], v[76:79], v[196:199]\n"
    "v_cmp_lt_f32 vcc, s71, v82\n"
    "v_mfma_f32_16x16x32_bf16 v[204:207], a[164:167], v[76:79], v[204:207]\n"
    "s_waitcnt vmcnt(16) lgkmcnt(0)\n"
    "s_barrier\n"
    "ds_read_b64_tr_b16 a[128:129], v12 offset:32768\n"
    "v_mfma_f32_16x16x32_bf16 v[212:215], a[168:171], v[76:79], v[212:215]\n"
    "ds_read_b64_tr_b16 a[130:131], v12 offset:40960\n"
    "v_mfma_f32_16x16x32_bf16 v[220:223], a[172:175], v[76:79], v[220:223]\n"
    "ds_read_b64_tr_b16 a[132:133], v13 offset:32768\n"
    "v_mfma_f32_16x16x32_bf16 v[228:231], a[176:179], v[76:79], v[228:231]\n"
    "ds_read_b64_tr_b16 a[134:135], v13 offset:40960\n"
    "v_mfma_f32_16x16x32_bf16 v[236:239], a[180:183], v[76:79], v[236:239]\n"
    "ds_read_b64_tr_b16 a[136:137], v14 offset:32768\n"
    "v_mfma_f32_16x16x32_bf16 v[244:247], a[184:187], v[76:79], v[244:247]\n"
    "ds_read_b64_tr_b16 a[138:139], v14 offset:40960\n"
    "v_mfma_f32_16x16x32_bf16 v[252:255], a[188:191], v[76:79], v[252:255]\n"
    "ds_read_b64_tr_b16 a[140:141], v15 offset:32768\n"
    "s_cbranch_vccnz LR69_%=\n"
    "LB69_%=:\n"
    "v_mfma_f32_16x16x32_bf16 v[48:51], a[64:67], a[0:3], v[64:67]\n"
    "ds_read_b64_tr_b16 a[142:143], v15 offset:40960\n"
    "v_exp_f32 v32, v32\n"
    "v_mfma_f32_16x16x32_bf16 v[56:59], a[64:67], a[4:7], v[68:71]\n"
    "ds_read_b64_tr_b16 a[144:145], v16 offset:32768\n"
    "v_exp_f32 v33, v33\n"
    "v_mfma_f32_16x16x32_bf16 v[52:55], a[68:71], a[0:3], v[64:67]\n"
    "s_mov_b32 m0, s65\n"
    "s_mov_b32 s70, 0x1c080\n"
    "buffer_load_dwordx4 v28, s[52:55], s70 offen offset:0 lds\n"
    "v_mfma_f32_16x16x32_bf16 v[60:63], a[68:71], a[4:7], v[68:71]\n"
    "ds_read_b64_tr_b16 a[146:147], v16 offset:40960\n"
    "v_exp_f32 v34, v34\n"
    "v_mfma_f32_16x16x32_bf16 v[48:51], a[72:75], a[8:11], v[48:51]\n"
    "ds_read_b64_tr_b16 a[148:149], v17 offset:32768\n"
    "v_exp_f32 v35, v35\n"
    "v_mfma_f32_16x16x32_bf16 v[56:59], a[72:75], a[12:15], v[56:59]\n"
    "ds_read_b64_tr_b16 a[150:151], v17 offset:40960\n"
    "v_exp_f32 v36, v36\n"
    "v_mfma_f32_16x16x32_bf16 v[52:55], a[76:79], a[8:11], v[52:55]\n"
    "ds_read_b64_tr_b16 a[152:153], v18 offset:32768\n"
    "v_exp_f32 v37, v37\n"
    "v_mfma_f32_16x16x32_bf16 v[60:63], a[76:79], a[12:15], v[60:63]\n"
    "ds_read_b64_tr_b16 a[154:155], v18 offset:40960\n"
    "v_exp_f32 v38, v38\n"
    "v_mfma_f32_16x16x32_bf16 v[48:51], a[80:83], a[16:19], v[48:51]\n"
    "ds_read_b64_tr_b16 a[156:157], v19 offset:32768\n"
    "v_exp_f32 v39, v39\n"
    "v_mfma_f32_16x16x32_bf16 v[56:59], a[80:83], a[20:23], v[56:59]\n"
    "buffer_load_dwordx4 v29, s[52:55], s70 offen offset:1024 lds\n"
    "v_mfma_f32_16x16x32_bf16 v[52:55], a[84:87], a[16:19], v[52:55]\n"
    "ds_read_b64_tr_b16 a[158:159], v19 offset:40960\n"
    "v_cvt_pk_bf16_f32 v72, v32, v33\n"
    "v_mfma_f32_16x16x32_bf16 v[60:63], a[84:87], a[20:23], v[60:63]\n"
    "ds_read_b64_tr_b16 a[160:161], v12 offset:33024\n"
    "v_cvt_pk_bf16_f32 v73, v34, v35\n"
    "v_mfma_f32_16x16x32_bf16 v[48:51], a[88:91], a[24:27], v[48:51]\n"
    "ds_read_b64_tr_b16 a[162:163], v12 offset:41216\n"
    "v_cvt_pk_bf16_f32 v74, v36, v37\n"
    "v_mfma_f32_16x16x32_bf16 v[56:59], a[88:91], a[28:31], v[56:59]\n"
    "ds_read_b64_tr_b16 a[164:165], v13 offset:33024\n"
    "v_cvt_pk_bf16_f32 v75, v38, v39\n"
    "v_mfma_f32_16x16x32_bf16 v[52:55], a[92:95], a[24:27], v[52:55]\n"
    "ds_read_b64_tr_b16 a[166:167], v13 offset:41216\n"
    "v_add_f32 v80, v80, v32\n"
    "v_mfma_f32_16x16x32_bf16 v[60:63], a[92:95], a[28:31], v[60:63]\n"
    "ds_read_b64_tr_b16 a[168:169], v14 offset:33024\n"
    "v_add_f32 v80, v80, v33\n"
    "v_mfma_f32_16x16x32_bf16 v[48:51], a[96:99], a[32:35], v[48:51]\n"
    "buffer_load_dwordx4 v30, s[52:55], s70 offen offset:2048 lds\n"
    "v_mfma_f32_16x16x32_bf16 v[56:59], a[96:99], a[36:39], v[56:59]\n"
    "ds_read_b64_tr_b16 a[170:171], v14 offset:41216\n"
    "v_add_f32 v80, v80, v34\n"
    "v_mfma_f32_16x16x32_bf16 v[52:55], a[100:103], a[32:35], v[52:55]\n"
    "ds_read_b64_tr_b16 a[172:173], v15 offset:33024\n"
    "v_add_f32 v80, v80, v35\n"
    "v_mfma_f32_16x16x32_bf16 v[60:63], a[100:103], a[36:39], v[60:63]\n"
    "ds_read_b64_tr_b16 a[174:175], v15 offset:41216\n"
    "v_exp_f32 v40, v40\n"
    "v_mfma_f32_16x16x32_bf16 v[48:51], a[104:107], a[40:43], v[48:51]\n"
    "ds_read_b64_tr_b16 a[176:177], v16 offset:33024\n"
    "v_exp_f32 v41, v41\n"
    "v_mfma_f32_16x16x32_bf16 v[56:59], a[104:107], a[44:47], v[56:59]\n"
    "ds_read_b64_tr_b16 a[178:179], v16 offset:41216\n"
    "v_add_f32 v80, v80, v36\n"
    "v_mfma_f32_16x16x32_bf16 v[52:55], a[108:111], a[40:43], v[52:55]\n"
    "ds_read_b64_tr_b16 a[180:181], v17 offset:33024\n"
    "v_exp_f32 v42, v42\n"
    "v_mfma_f32_16x16x32_bf16 v[60:63], a[108:111], a[44:47], v[60:63]\n"
    "buffer_load_dwordx4 v31, s[52:55], s70 offen offset:3072 lds\n"
    "v_mfma_f32_16x16x32_bf16 v[48:51], a[112:115], a[48:51], v[48:51]\n"
    "ds_read_b64_tr_b16 a[182:183], v17 offset:41216\n"
    "v_exp_f32 v43, v43\n"
    "v_mfma_f32_16x16x32_bf16 v[56:59], a[112:115], a[52:55], v[56:59]\n"
    "ds_read_b64_tr_b16 a[184:185], v18 offset:33024\n"
    "v_add_f32 v80, v80, v37\n"
    "v_mfma_f32_16x16x32_bf16 v[52:55], a[116:119], a[48:51], v[52:55]\n"
    "ds_read_b64_tr_b16 a[186:187], v18 offset:41216\n"
    "v_exp_f32 v44, v44\n"
    "v_mfma_f32_16x16x32_bf16 v[60:63], a[116:119], a[52:55], v[60:63]\n"
    "ds_read_b64_tr_b16 a[188:189], v19 offset:33024\n"
    "v_exp_f32 v45, v45\n"
    "v_mfma_f32_16x16x32_bf16 v[48:51], a[120:123], a[56:59], v[48:51]\n"
    "ds_read_b64_tr_b16 a[190:191], v19 offset:41216\n"
    "v_add_f32 v80, v80, v38\n"
    "v_mfma_f32_16x16x32_bf16 v[56:59], a[120:123], a[60:63], v[56:59]\n"
    "v_exp_f32 v46, v46\n"
    "v_mfma_f32_16x16x32_bf16 v[52:55], a[124:127], a[56:59], v[52:55]\n"
    "s_mov_b32 m0, s62\n"
    "s_mov_b32 s70, 0x100\n"
    "buffer_load_dwordx4 v28, s[52:55], s70 offen offset:0 lds\n"
    "v_mfma_f32_16x16x32_bf16 v[60:63], a[124:127], a[60:63], v[60:63]\n"
    "v_exp_f32 v47, v47\n"
    "s_waitcnt lgkmcnt(0)\n"
    "v_mfma_f32_16x16x32_bf16 v[128:131], a[128:131], v[72:75], v[128:131]\n"
    "v_add_f32 v80, v80, v39\n"
    "v_mfma_f32_16x16x32_bf16 v[136:139], a[132:135], v[72:75], v[136:139]\n"
    "v_cvt_pk_bf16_f32 v76, v40, v41\n"
    "v_mfma_f32_16x16x32_bf16 v[144:147], a[136:139], v[72:75], v[144:147]\n"
    "v_cvt_pk_bf16_f32 v77, v42, v43\n"
    "v_mfma_f32_16x16x32_bf16 v[152:155], a[140:143], v[72:75], v[152:155]\n"
    "v_cvt_pk_bf16_f32 v78, v44, v45\n"
    "v_mfma_f32_16x16x32_bf16 v[160:163], a[144:147], v[72:75], v[160:163]\n"
    "v_cvt_pk_bf16_f32 v79, v46, v47\n"
    "v_mfma_f32_16x16x32_bf16 v[168:171], a[148:151], v[72:75], v[168:171]\n"
    "buffer_load_dwordx4 v29, s[52:55], s70 offen offset:1024 lds\n"
    "v_mfma_f32_16x16x32_bf16 v[176:179], a[152:155], v[72:75], v[176:179]\n"
    "v_max3_f32 v82, v48, v49, v50\n"
    "v_mfma_f32_16x16x32_bf16 v[184:187], a[156:159], v[72:75], v[184:187]\n"
    "v_add_f32 v81, v81, v40\n"
    "v_mfma_f32_16x16x32_bf16 v[192:195], a[160:163], v[72:75], v[192:195]\n"
    "v_max3_f32 v83, v56, v57, v58\n"
    "v_mfma_f32_16x16x32_bf16 v[200:203], a[164:167], v[72:75], v[200:203]\n"
    "v_add_f32 v81, v81, v41\n"
    "v_mfma_f32_16x16x32_bf16 v[208:211], a[168:171], v[72:75], v[208:211]\n"
    "v_max3_f32 v82, v82, v51, v52\n"
    "v_mfma_f32_16x16x32_bf16 v[216:219], a[172:175], v[72:75], v[216:219]\n"
    "v_add_f32 v81, v81, v42\n"
    "v_mfma_f32_16x16x32_bf16 v[224:227], a[176:179], v[72:75], v[224:227]\n"
    "buffer_load_dwordx4 v30, s[52:55], s70 offen offset:2048 lds\n"
    "v_mfma_f32_16x16x32_bf16 v[232:235], a[180:183], v[72:75], v[232:235]\n"
    "v_max3_f32 v83, v83, v59, v60\n"
    "v_mfma_f32_16x16x32_bf16 v[240:243], a[184:187], v[72:75], v[240:243]\n"
    "v_add_f32 v81, v81, v43\n"
    "v_mfma_f32_16x16x32_bf16 v[248:251], a[188:191], v[72:75], v[248:251]\n"
    "v_max3_f32 v82, v82, v53, v54\n"
    "v_mfma_f32_16x16x32_bf16 v[132:135], a[128:131], v[76:79], v[132:135]\n"
    "v_add_f32 v81, v81, v44\n"
    "v_mfma_f32_16x16x32_bf16 v[140:143], a[132:135], v[76:79], v[140:143]\n"
    "v_max3_f32 v83, v83, v61, v62\n"
    "v_mfma_f32_16x16x32_bf16 v[148:151], a[136:139], v[76:79], v[148:151]\n"
    "v_add_f32 v81, v81, v45\n"
    "v_mfma_f32_16x16x32_bf16 v[156:159], a[140:143], v[76:79], v[156:159]\n"
    "buffer_load_dwordx4 v31, s[52:55], s70 offen offset:3072 lds\n"
    "v_mfma_f32_16x16x32_bf16 v[164:167], a[144:147], v[76:79], v[164:167]\n"
    "v_max3_f32 v82, v82, v55, v63\n"
    "v_mfma_f32_16x16x32_bf16 v[172:175], a[148:151], v[76:79], v[172:175]\n"
    "v_add_f32 v81, v81, v46\n"
    "v_mfma_f32_16x16x32_bf16 v[180:183], a[152:155], v[76:79], v[180:183]\n"
    "v_max_f32 v82, v82, v83\n"
    "v_mfma_f32_16x16x32_bf16 v[188:191], a[156:159], v[76:79], v[188:191]\n"
    "v_add_f32 v81, v81, v47\n"
    "v_mfma_f32_16x16x32_bf16 v[196:199], a[160:163], v[76:79], v[196:199]\n"
    "v_cmp_lt_f32 vcc, s71, v82\n"
    "v_mfma_f32_16x16x32_bf16 v[204:207], a[164:167], v[76:79], v[204:207]\n"
    "s_waitcnt vmcnt(16) lgkmcnt(0)\n"
    "s_barrier\n"
    "ds_read_b64_tr_b16 a[128:129], v12 offset:49152\n"
    "v_mfma_f32_16x16x32_bf16 v[212:215], a[168:171], v[76:79], v[212:215]\n"
    "ds_read_b64_tr_b16 a[130:131], v12 offset:57344\n"
    "v_mfma_f32_16x16x32_bf16 v[220:223], a[172:175], v[76:79], v[220:223]\n"
    "ds_read_b64_tr_b16 a[132:133], v13 offset:49152\n"
    "v_mfma_f32_16x16x32_bf16 v[228:231], a[176:179], v[76:79], v[228:231]\n"
    "ds_read_b64_tr_b16 a[134:135], v13 offset:57344\n"
    "v_mfma_f32_16x16x32_bf16 v[236:239], a[180:183], v[76:79], v[236:239]\n"
    "ds_read_b64_tr_b16 a[136:137], v14 offset:49152\n"
    "v_mfma_f32_16x16x32_bf16 v[244:247], a[184:187], v[76:79], v[244:247]\n"
    "ds_read_b64_tr_b16 a[138:139], v14 offset:57344\n"
    "v_mfma_f32_16x16x32_bf16 v[252:255], a[188:191], v[76:79], v[252:255]\n"
    "ds_read_b64_tr_b16 a[140:141], v15 offset:49152\n"
    "s_cbranch_vccnz LR70_%=\n"
    "LB70_%=:\n"
    "ds_read_b64_tr_b16 a[64:65], v88 offset:0\n"
    "ds_read_b64_tr_b16 a[66:67], v88 offset:2048\n"
    "v_exp_f32 v48, v48\n"
    "v_exp_f32 v49, v49\n"
    "ds_read_b64_tr_b16 a[68:69], v89 offset:0\n"
    "ds_read_b64_tr_b16 a[142:143], v15 offset:57344\n"
    "v_exp_f32 v50, v50\n"
    "v_exp_f32 v51, v51\n"
    "s_mov_b32 m0, s66\n"
    "s_mov_b32 s70, 0x1c100\n"
    "buffer_load_dwordx4 v28, s[52:55], s70 offen offset:0 lds\n"
    "ds_read_b64_tr_b16 a[70:71], v89 offset:2048\n"
    "v_exp_f32 v52, v52\n"
    "ds_read_b64_tr_b16 a[144:145], v16 offset:49152\n"
    "ds_read_b64_tr_b16 a[146:147], v16 offset:57344\n"
    "v_exp_f32 v53, v53\n"
    "v_exp_f32 v54, v54\n"
    "ds_read_b64_tr_b16 a[72:73], v88 offset:512\n"
    "ds_read_b64_tr_b16 a[148:149], v17 offset:49152\n"
    "v_exp_f32 v55, v55\n"
    "v_exp_f32 v56, v56\n"
    "ds_read_b64_tr_b16 a[150:151], v17 offset:57344\n"
    "ds_read_b64_tr_b16 a[74:75], v88 offset:2560\n"
    "v_exp_f32 v57, v57\n"
    "v_exp_f32 v58, v58\n"
    "ds_read_b64_tr_b16 a[152:153], v18 offset:49152\n"
    "ds_read_b64_tr_b16 a[154:155], v18 offset:57344\n"
    "v_exp_f32 v59, v59\n"
    "v_exp_f32 v60, v60\n"
    "ds_read_b64_tr_b16 a[76:77], v89 offset:512\n"
    "ds_read_b64_tr_b16 a[156:157], v19 offset:49152\n"
    "v_exp_f32 v61, v61\n"
    "v_exp_f32 v62, v62\n"
    "ds_read_b64_tr_b16 a[158:159], v19 offset:57344\n"
    "ds_read_b64_tr_b16 a[78:79], v89 offset:2560\n"
    "v_exp_f32 v63, v63\n"
    "v_add_f32 v80, v80, v48\n"
    "buffer_load_dwordx4 v29, s[52:55], s70 offen offset:1024 lds\n"
    "ds_read_b64_tr_b16 a[160:161], v12 offset:49408\n"
    "v_add_f32 v80, v80, v49\n"
    "ds_read_b64_tr_b16 a[162:163], v12 offset:57600\n"
    "ds_read_b64_tr_b16 a[80:81], v88 offset:4096\n"
    "v_add_f32 v80, v80, v50\n"
    "v_add_f32 v80, v80, v51\n"
    "ds_read_b64_tr_b16 a[164:165], v13 offset:49408\n"
    "ds_read_b64_tr_b16 a[166:167], v13 offset:57600\n"
    "v_add_f32 v80, v80, v52\n"
    "v_add_f32 v80, v80, v53\n"
    "ds_read_b64_tr_b16 a[82:83], v88 offset:6144\n"
    "ds_read_b64_tr_b16 a[168:169], v14 offset:49408\n"
    "v_add_f32 v80, v80, v54\n"
    "v_add_f32 v80, v80, v55\n"
    "ds_read_b64_tr_b16 a[170:171], v14 offset:57600\n"
    "ds_read_b64_tr_b16 a[84:85], v89 offset:4096\n"
    "v_add_f32 v81, v81, v56\n"
    "v_add_f32 v81, v81, v57\n"
    "ds_read_b64_tr_b16 a[172:173], v15 offset:49408\n"
    "ds_read_b64_tr_b16 a[174:175], v15 offset:57600\n"
    "v_add_f32 v81, v81, v58\n"
    "v_add_f32 v81, v81, v59\n"
    "ds_read_b64_tr_b16 a[86:87], v89 offset:6144\n"
    "ds_read_b64_tr_b16 a[176:177], v16 offset:49408\n"
    "v_add_f32 v81, v81, v60\n"
    "v_add_f32 v81, v81, v61\n"
    "buffer_load_dwordx4 v30, s[52:55], s70 offen offset:2048 lds\n"
    "ds_read_b64_tr_b16 a[178:179], v16 offset:57600\n"
    "v_add_f32 v81, v81, v62\n"
    "ds_read_b64_tr_b16 a[88:89], v88 offset:4608\n"
    "ds_read_b64_tr_b16 a[180:181], v17 offset:49408\n"
    "v_add_f32 v81, v81, v63\n"
    "v_cvt_pk_bf16_f32 v72, v48, v49\n"
    "ds_read_b64_tr_b16 a[182:183], v17 offset:57600\n"
    "ds_read_b64_tr_b16 a[90:91], v88 offset:6656\n"
    "v_cvt_pk_bf16_f32 v73, v50, v51\n"
    "v_cvt_pk_bf16_f32 v74, v52, v53\n"
    "ds_read_b64_tr_b16 a[184:185], v18 offset:49408\n"
    "ds_read_b64_tr_b16 a[186:187], v18 offset:57600\n"
    "v_cvt_pk_bf16_f32 v75, v54, v55\n"
    "v_cvt_pk_bf16_f32 v76, v56, v57\n"
    "ds_read_b64_tr_b16 a[92:93], v89 offset:4608\n"
    "ds_read_b64_tr_b16 a[188:189], v19 offset:49408\n"
    "v_cvt_pk_bf16_f32 v77, v58, v59\n"
    "v_cvt_pk_bf16_f32 v78, v60, v61\n"
    "ds_read_b64_tr_b16 a[190:191], v19 offset:57600\n"
    "ds_read_b64_tr_b16 a[94:95], v89 offset:6656\n"
    "v_cvt_pk_bf16_f32 v79, v62, v63\n"
    "ds_read_b64_tr_b16 a[96:97], v88 offset:8192\n"
    "ds_read_b64_tr_b16 a[98:99], v88 offset:10240\n"
    "buffer_load_dwordx4 v31, s[52:55], s70 offen offset:3072 lds\n"
    "ds_read_b64_tr_b16 a[100:101], v89 offset:8192\n"
    "ds_read_b64_tr_b16 a[102:103], v89 offset:10240\n"
    "ds_read_b64_tr_b16 a[104:105], v88 offset:8704\n"
    "ds_read_b64_tr_b16 a[106:107], v88 offset:10752\n"
    "ds_read_b64_tr_b16 a[108:109], v89 offset:8704\n"
    "v_mov_b32 v92, v80\n"
    "v_mov_b32 v93, v81\n"
    "s_nop 1\n"
    "v_permlane16_swap_b32 v80, v92\n"
    "v_permlane16_swap_b32 v81, v93\n"
    "s_nop 1\n"
    "v_add_f32 v80, v80, v92\n"
    "v_add_f32 v81, v81, v93\n"
    "v_mov_b32 v92, v80\n"
    "v_mov_b32 v93, v81\n"
    "s_nop 1\n"
    "v_permlane32_swap_b32 v80, v92\n"
    "v_permlane32_swap_b32 v81, v93\n"
    "s_nop 1\n"
    "v_add_f32 v80, v80, v92\n"
    "v_add_f32 v81, v81, v93\n"
    "v_rcp_f32 v82, v80\n"
    "v_rcp_f32 v84, v81\n"
    "s_nop 0\n"
    "v_mov_b32 v85, v84\n"
    "ds_read_b64_tr_b16 a[110:111], v89 offset:10752\n"
    "ds_read_b64_tr_b16 a[112:113], v88 offset:12288\n"
    "ds_read_b64_tr_b16 a[114:115], v88 offset:14336\n"
    "ds_read_b64_tr_b16 a[116:117], v89 offset:12288\n"
    "ds_read_b64_tr_b16 a[118:119], v89 offset:14336\n"
    "ds_read_b64_tr_b16 a[120:121], v88 offset:12800\n"
    "ds_read_b64_tr_b16 a[122:123], v88 offset:14848\n"
    "ds_read_b64_tr_b16 a[124:125], v89 offset:12800\n"
    "s_mov_b32 m0, s63\n"
    "s_mov_b32 s70, 0x180\n"
    "buffer_load_dwordx4 v28, s[52:55], s70 offen offset:0 lds\n"
    "ds_read_b64_tr_b16 a[126:127], v89 offset:14848\n"
    "s_waitcnt lgkmcnt(6)\n"
    "v_mfma_f32_16x16x32_bf16 v[128:131], a[128:131], v[72:75], v[128:131]\n"
    "v_mfma_f32_16x16x32_bf16 v[136:139], a[132:135], v[72:75], v[136:139]\n"
    "v_mfma_f32_16x16x32_bf16 v[144:147], a[136:139], v[72:75], v[144:147]\n"
    "v_mfma_f32_16x16x32_bf16 v[152:155], a[140:143], v[72:75], v[152:155]\n"
    "v_mfma_f32_16x16x32_bf16 v[160:163], a[144:147], v[72:75], v[160:163]\n"
    "v_mfma_f32_16x16x32_bf16 v[168:171], a[148:151], v[72:75], v[168:171]\n"
    "buffer_load_dwordx4 v29, s[52:55], s70 offen offset:1024 lds\n"
    "v_mfma_f32_16x16x32_bf16 v[176:179], a[152:155], v[72:75], v[176:179]\n"
    "v_mfma_f32_16x16x32_bf16 v[184:187], a[156:159], v[72:75], v[184:187]\n"
    "v_mfma_f32_16x16x32_bf16 v[192:195], a[160:163], v[72:75], v[192:195]\n"
    "v_mul_f32 v128, v128, v82\n"
    "v_mul_f32 v129, v129, v82\n"
    "v_mul_f32 v130, v130, v82\n"
    "v_mul_f32 v131, v131, v82\n"
    "v_mfma_f32_16x16x32_bf16 v[200:203], a[164:167], v[72:75], v[200:203]\n"
    "v_cvt_pk_bf16_f32 v32, v128, v129\n"
    "v_cvt_pk_bf16_f32 v33, v130, v131\n"
    "v_mul_f32 v136, v136, v82\n"
    "v_mul_f32 v137, v137, v82\n"
    "v_mfma_f32_16x16x32_bf16 v[208:211], a[168:171], v[72:75], v[208:211]\n"
    "v_mul_f32 v138, v138, v82\n"
    "v_mul_f32 v139, v139, v82\n"
    "v_cvt_pk_bf16_f32 v34, v136, v137\n"
    "v_cvt_pk_bf16_f32 v35, v138, v139\n"
    "v_mfma_f32_16x16x32_bf16 v[216:219], a[172:175], v[72:75], v[216:219]\n"
    "v_mul_f32 v144, v144, v82\n"
    "v_mul_f32 v145, v145, v82\n"
    "v_mul_f32 v146, v146, v82\n"
    "v_mul_f32 v147, v147, v82\n"
    "v_mfma_f32_16x16x32_bf16 v[224:227], a[176:179], v[72:75], v[224:227]\n"
    "buffer_load_dwordx4 v30, s[52:55], s70 offen offset:2048 lds\n"
    "v_cvt_pk_bf16_f32 v40, v144, v145\n"
    "v_cvt_pk_bf16_f32 v41, v146, v147\n"
    "v_mul_f32 v152, v152, v82\n"
    "v_mul_f32 v153, v153, v82\n"
    "v_mfma_f32_16x16x32_bf16 v[232:235], a[180:183], v[72:75], v[232:235]\n"
    "v_mul_f32 v154, v154, v82\n"
    "v_mul_f32 v155, v155, v82\n"
    "v_cvt_pk_bf16_f32 v42, v152, v153\n"
    "v_cvt_pk_bf16_f32 v43, v154, v155\n"
    "v_mfma_f32_16x16x32_bf16 v[240:243], a[184:187], v[72:75], v[240:243]\n"
    "v_mul_f32 v160, v160, v82\n"
    "v_mul_f32 v161, v161, v82\n"
    "v_mul_f32 v162, v162, v82\n"
    "v_mul_f32 v163, v163, v82\n"
    "v_mfma_f32_16x16x32_bf16 v[248:251], a[188:191], v[72:75], v[248:251]\n"
    "v_cvt_pk_bf16_f32 v48, v160, v161\n"
    "v_cvt_pk_bf16_f32 v49, v162, v163\n"
    "v_mul_f32 v168, v168, v82\n"
    "v_mul_f32 v169, v169, v82\n"
    "v_mfma_f32_16x16x32_bf16 v[132:135], a[128:131], v[76:79], v[132:135]\n"
    "v_mul_f32 v170, v170, v82\n"
    "v_mul_f32 v171, v171, v82\n"
    "v_cvt_pk_bf16_f32 v50, v168, v169\n"
    "v_cvt_pk_bf16_f32 v51, v170, v171\n"
    "v_mfma_f32_16x16x32_bf16 v[140:143], a[132:135], v[76:79], v[140:143]\n"
    "v_mul_f32 v176, v176, v82\n"
    "v_mul_f32 v177, v177, v82\n"
    "v_mul_f32 v178, v178, v82\n"
    "v_mul_f32 v179, v179, v82\n"
    "v_mfma_f32_16x16x32_bf16 v[148:151], a[136:139], v[76:79], v[148:151]\n"
    "v_cvt_pk_bf16_f32 v56, v176, v177\n"
    "v_cvt_pk_bf16_f32 v57, v178, v179\n"
    "v_mul_f32 v184, v184, v82\n"
    "v_mul_f32 v185, v185, v82\n"
    "v_mfma_f32_16x16x32_bf16 v[156:159], a[140:143], v[76:79], v[156:159]\n"
    "buffer_load_dwordx4 v31, s[52:55], s70 offen offset:3072 lds\n"
    "v_mul_f32 v186, v186, v82\n"
    "v_mul_f32 v187, v187, v82\n"
    "v_cvt_pk_bf16_f32 v58, v184, v185\n"
    "v_cvt_pk_bf16_f32 v59, v186, v187\n"
    "v_mfma_f32_16x16x32_bf16 v[164:167], a[144:147], v[76:79], v[164:167]\n"
    "v_mul_f32 v192, v192, v82\n"
    "v_mul_f32 v193, v193, v82\n"
    "v_mul_f32 v194, v194, v82\n"
    "v_mul_f32 v195, v195, v82\n"
    "v_mfma_f32_16x16x32_bf16 v[172:175], a[148:151], v[76:79], v[172:175]\n"
    "v_cvt_pk_bf16_f32 v96, v192, v193\n"
    "v_cvt_pk_bf16_f32 v97, v194, v195\n"
    "v_mul_f32 v200, v200, v82\n"
    "v_mul_f32 v201, v201, v82\n"
    "v_mfma_f32_16x16x32_bf16 v[180:183], a[152:155], v[76:79], v[180:183]\n"
    "v_mul_f32 v202, v202, v82\n"
    "v_mul_f32 v203, v203, v82\n"
    "v_cvt_pk_bf16_f32 v98, v200, v201\n"
    "v_cvt_pk_bf16_f32 v99, v202, v203\n"
    "v_mfma_f32_16x16x32_bf16 v[188:191], a[156:159], v[76:79], v[188:191]\n"
    "v_mul_f32 v208, v208, v82\n"
    "v_mul_f32 v209, v209, v82\n"
    "v_mul_f32 v210, v210, v82\n"
    "v_mul_f32 v211, v211, v82\n"
    "v_mfma_f32_16x16x32_bf16 v[196:199], a[160:163], v[76:79], v[196:199]\n"
    "v_cvt_pk_bf16_f32 v104, v208, v209\n"
    "v_cvt_pk_bf16_f32 v105, v210, v211\n"
    "v_mul_f32 v216, v216, v82\n"
    "v_mul_f32 v217, v217, v82\n"
    "v_mfma_f32_16x16x32_bf16 v[204:207], a[164:167], v[76:79], v[204:207]\n"
    "s_waitcnt vmcnt(16) lgkmcnt(0)\n"
    "s_barrier\n"
    "ds_read_b64_tr_b16 a[128:129], v90 offset:0\n"
    "v_mul_f32 v218, v218, v82\n"
    "v_mul_f32 v219, v219, v82\n"
    "v_cvt_pk_bf16_f32 v106, v216, v217\n"
    "v_cvt_pk_bf16_f32 v107, v218, v219\n"
    "v_mfma_f32_16x16x32_bf16 v[212:215], a[168:171], v[76:79], v[212:215]\n"
    "ds_read_b64_tr_b16 a[130:131], v90 offset:2048\n"
    "v_mul_f32 v224, v224, v82\n"
    "v_mul_f32 v225, v225, v82\n"
    "v_mul_f32 v226, v226, v82\n"
    "v_mul_f32 v227, v227, v82\n"
    "v_mfma_f32_16x16x32_bf16 v[220:223], a[172:175], v[76:79], v[220:223]\n"
    "ds_read_b64_tr_b16 a[132:133], v91 offset:0\n"
    "v_cvt_pk_bf16_f32 v112, v224, v225\n"
    "v_cvt_pk_bf16_f32 v113, v226, v227\n"
    "v_mul_f32 v232, v232, v82\n"
    "v_mul_f32 v233, v233, v82\n"
    "v_mfma_f32_16x16x32_bf16 v[228:231], a[176:179], v[76:79], v[228:231]\n"
    "ds_read_b64_tr_b16 a[134:135], v91 offset:2048\n"
    "v_mul_f32 v234, v234, v82\n"
    "v_mul_f32 v235, v235, v82\n"
    "v_cvt_pk_bf16_f32 v114, v232, v233\n"
    "v_cvt_pk_bf16_f32 v115, v234, v235\n"
    "v_mfma_f32_16x16x32_bf16 v[236:239], a[180:183], v[76:79], v[236:239]\n"
    "ds_read_b64_tr_b16 a[136:137], v90 offset:512\n"
    "v_mul_f32 v240, v240, v82\n"
    "v_mul_f32 v241, v241, v82\n"
    "v_mul_f32 v242, v242, v82\n"
    "v_mul_f32 v243, v243, v82\n"
    "v_mfma_f32_16x16x32_bf16 v[244:247], a[184:187], v[76:79], v[244:247]\n"
    "ds_read_b64_tr_b16 a[138:139], v90 offset:2560\n"
    "v_cvt_pk_bf16_f32 v120, v240, v241\n"
    "v_cvt_pk_bf16_f32 v121, v242, v243\n"
    "v_mul_f32 v248, v248, v82\n"
    "v_mul_f32 v249, v249, v82\n"
    "v_mfma_f32_16x16x32_bf16 v[252:255], a[188:191], v[76:79], v[252:255]\n"
    "ds_read_b64_tr_b16 a[140:141], v91 offset:512\n"
    "v_mul_f32 v250, v250, v82\n"
    "v_mul_f32 v251, v251, v82\n"
    "v_cvt_pk_bf16_f32 v122, v248, v249\n"
    "v_cvt_pk_bf16_f32 v123, v250, v251\n"
    "v_pk_mul_f32 v[132:133], v[132:133], v[84:85]\n"
    "v_pk_mul_f32 v[134:135], v[134:135], v[84:85]\n"
    "v_pk_mul_f32 v[140:141], v[140:141], v[84:85]\n"
    "v_pk_mul_f32 v[142:143], v[142:143], v[84:85]\n"
    "v_pk_mul_f32 v[148:149], v[148:149], v[84:85]\n"
    "v_pk_mul_f32 v[150:151], v[150:151], v[84:85]\n"
    "v_pk_mul_f32 v[156:157], v[156:157], v[84:85]\n"
    "v_pk_mul_f32 v[158:159], v[158:159], v[84:85]\n"
    "v_cvt_pk_bf16_f32 v36, v132, v133\n"
    "v_cvt_pk_bf16_f32 v37, v134, v135\n"
    "v_cvt_pk_bf16_f32 v38, v140, v141\n"
    "v_cvt_pk_bf16_f32 v39, v142, v143\n"
    "v_pk_mul_f32 v[164:165], v[164:165], v[84:85]\n"
    "v_pk_mul_f32 v[166:167], v[166:167], v[84:85]\n"
    "v_pk_mul_f32 v[172:173], v[172:173], v[84:85]\n"
    "v_pk_mul_f32 v[174:175], v[174:175], v[84:85]\n"
    "v_cvt_pk_bf16_f32 v44, v148, v149\n"
    "v_cvt_pk_bf16_f32 v45, v150, v151\n"
    "v_cvt_pk_bf16_f32 v46, v156, v157\n"
    "v_cvt_pk_bf16_f32 v47, v158, v159\n"
    "v_permlane16_swap_b32 v32, v36\n"
    "v_permlane16_swap_b32 v33, v37\n"
    "v_permlane16_swap_b32 v34, v38\n"
    "v_permlane16_swap_b32 v35, v39\n"
    "v_pk_mul_f32 v[180:181], v[180:181], v[84:85]\n"
    "v_pk_mul_f32 v[182:183], v[182:183], v[84:85]\n"
    "v_pk_mul_f32 v[188:189], v[188:189], v[84:85]\n"
    "v_pk_mul_f32 v[190:191], v[190:191], v[84:85]\n"
    "v_cvt_pk_bf16_f32 v52, v164, v165\n"
    "v_cvt_pk_bf16_f32 v53, v166, v167\n"
    "v_cvt_pk_bf16_f32 v54, v172, v173\n"
    "v_cvt_pk_bf16_f32 v55, v174, v175\n"
    "v_permlane16_swap_b32 v40, v44\n"
    "v_permlane16_swap_b32 v41, v45\n"
    "v_permlane16_swap_b32 v42, v46\n"
    "v_permlane16_swap_b32 v43, v47\n"
    "v_pk_mul_f32 v[196:197], v[196:197], v[84:85]\n"
    "v_pk_mul_f32 v[198:199], v[198:199], v[84:85]\n"
    "v_pk_mul_f32 v[204:205], v[204:205], v[84:85]\n"
    "v_pk_mul_f32 v[206:207], v[206:207], v[84:85]\n"
    "v_cvt_pk_bf16_f32 v60, v180, v181\n"
    "v_cvt_pk_bf16_f32 v61, v182, v183\n"
    "v_cvt_pk_bf16_f32 v62, v188, v189\n"
    "v_cvt_pk_bf16_f32 v63, v190, v191\n"
    "v_permlane16_swap_b32 v48, v52\n"
    "v_permlane16_swap_b32 v49, v53\n"
    "v_permlane16_swap_b32 v50, v54\n"
    "v_permlane16_swap_b32 v51, v55\n"
    "v_pk_mul_f32 v[212:213], v[212:213], v[84:85]\n"
    "v_pk_mul_f32 v[214:215], v[214:215], v[84:85]\n"
    "v_pk_mul_f32 v[220:221], v[220:221], v[84:85]\n"
    "v_pk_mul_f32 v[222:223], v[222:223], v[84:85]\n"
    "v_cvt_pk_bf16_f32 v100, v196, v197\n"
    "v_cvt_pk_bf16_f32 v101, v198, v199\n"
    "v_cvt_pk_bf16_f32 v102, v204, v205\n"
    "v_cvt_pk_bf16_f32 v103, v206, v207\n"
    "v_permlane16_swap_b32 v56, v60\n"
    "v_permlane16_swap_b32 v57, v61\n"
    "v_permlane16_swap_b32 v58, v62\n"
    "v_permlane16_swap_b32 v59, v63\n"
    "v_pk_mul_f32 v[228:229], v[228:229], v[84:85]\n"
    "v_pk_mul_f32 v[230:231], v[230:231], v[84:85]\n"
    "v_pk_mul_f32 v[236:237], v[236:237], v[84:85]\n"
    "v_pk_mul_f32 v[238:239], v[238:239], v[84:85]\n"
    "v_cvt_pk_bf16_f32 v108, v212, v213\n"
    "v_cvt_pk_bf16_f32 v109, v214, v215\n"
    "v_cvt_pk_bf16_f32 v110, v220, v221\n"
    "v_cvt_pk_bf16_f32 v111, v222, v223\n"
    "v_permlane16_swap_b32 v96, v100\n"
    "v_permlane16_swap_b32 v97, v101\n"
    "v_permlane16_swap_b32 v98, v102\n"
    "v_permlane16_swap_b32 v99, v103\n"
    "v_pk_mul_f32 v[244:245], v[244:245], v[84:85]\n"
    "v_pk_mul_f32 v[246:247], v[246:247], v[84:85]\n"
    "v_pk_mul_f32 v[252:253], v[252:253], v[84:85]\n"
    "v_pk_mul_f32 v[254:255], v[254:255], v[84:85]\n"
    "v_cvt_pk_bf16_f32 v116, v228, v229\n"
    "v_cvt_pk_bf16_f32 v117, v230, v231\n"
    "v_cvt_pk_bf16_f32 v118, v236, v237\n"
    "v_cvt_pk_bf16_f32 v119, v238, v239\n"
    "v_permlane16_swap_b32 v104, v108\n"
    "v_permlane16_swap_b32 v105, v109\n"
    "v_permlane16_swap_b32 v106, v110\n"
    "v_permlane16_swap_b32 v107, v111\n"
    "v_cvt_pk_bf16_f32 v124, v244, v245\n"
    "v_cvt_pk_bf16_f32 v125, v246, v247\n"
    "v_cvt_pk_bf16_f32 v126, v252, v253\n"
    "v_cvt_pk_bf16_f32 v127, v254, v255\n"
    "v_permlane16_swap_b32 v112, v116\n"
    "v_permlane16_swap_b32 v113, v117\n"
    "v_permlane16_swap_b32 v114, v118\n"
    "v_permlane16_swap_b32 v115, v119\n"
    "s_nop 1\n"
    "v_permlane16_swap_b32 v120, v124\n"
    "v_permlane16_swap_b32 v121, v125\n"
    "v_permlane16_swap_b32 v122, v126\n"
    "v_permlane16_swap_b32 v123, v127\n"
    "s_nop 1\n"
    "v_mfma_f32_32x32x16_bf16 v[192:207], a[64:67], v[32:35], 0\n"
    "ds_read_b64_tr_b16 a[142:143], v91 offset:2560\n"
    "ds_read_b64_tr_b16 a[64:65], v88 offset:16384\n"
    "ds_read_b64_tr_b16 a[66:67], v88 offset:18432\n"
    "v_mfma_f32_32x32x16_bf16 v[208:223], a[68:71], v[32:35], 0\n"
    "s_mov_b32 m0, s67\n"
    "s_mov_b32 s70, 0x1c180\n"
    "buffer_load_dwordx4 v28, s[52:55], s70 offen offset:0 lds\n"
    "ds_read_b64_tr_b16 a[68:69], v89 offset:16384\n"
    "ds_read_b64_tr_b16 a[70:71], v89 offset:18432\n"
    "v_mfma_f32_32x32x16_bf16 v[192:207], a[72:75], v[36:39], v[192:207]\n"
    "ds_read_b64_tr_b16 a[144:145], v90 offset:4096\n"
    "ds_read_b64_tr_b16 a[72:73], v88 offset:16896\n"
    "ds_read_b64_tr_b16 a[74:75], v88 offset:18944\n"
    "v_mfma_f32_32x32x16_bf16 v[208:223], a[76:79], v[36:39], v[208:223]\n"
    "ds_read_b64_tr_b16 a[146:147], v90 offset:6144\n"
    "ds_read_b64_tr_b16 a[76:77], v89 offset:16896\n"
    "ds_read_b64_tr_b16 a[78:79], v89 offset:18944\n"
    "v_mfma_f32_32x32x16_bf16 v[192:207], a[80:83], v[40:43], v[192:207]\n"
    "ds_read_b64_tr_b16 a[148:149], v91 offset:4096\n"
    "buffer_load_dwordx4 v29, s[52:55], s70 offen offset:1024 lds\n"
    "ds_read_b64_tr_b16 a[80:81], v88 offset:20480\n"
    "v_mfma_f32_32x32x16_bf16 v[208:223], a[84:87], v[40:43], v[208:223]\n"
    "ds_read_b64_tr_b16 a[82:83], v88 offset:22528\n"
    "ds_read_b64_tr_b16 a[150:151], v91 offset:6144\n"
    "ds_read_b64_tr_b16 a[84:85], v89 offset:20480\n"
    "v_mfma_f32_32x32x16_bf16 v[192:207], a[88:91], v[44:47], v[192:207]\n"
    "ds_read_b64_tr_b16 a[86:87], v89 offset:22528\n"
    "ds_read_b64_tr_b16 a[152:153], v90 offset:4608\n"
    "ds_read_b64_tr_b16 a[154:155], v90 offset:6656\n"
    "v_mfma_f32_32x32x16_bf16 v[208:223], a[92:95], v[44:47], v[208:223]\n"
    "ds_read_b64_tr_b16 a[88:89], v88 offset:20992\n"
    "ds_read_b64_tr_b16 a[90:91], v88 offset:23040\n"
    "ds_read_b64_tr_b16 a[156:157], v91 offset:4608\n"
    "v_mfma_f32_32x32x16_bf16 v[192:207], a[96:99], v[48:51], v[192:207]\n"
    "buffer_load_dwordx4 v30, s[52:55], s70 offen offset:2048 lds\n"
    "ds_read_b64_tr_b16 a[158:159], v91 offset:6656\n"
    "ds_read_b64_tr_b16 a[92:93], v89 offset:20992\n"
    "v_mfma_f32_32x32x16_bf16 v[208:223], a[100:103], v[48:51], v[208:223]\n"
    "ds_read_b64_tr_b16 a[94:95], v89 offset:23040\n"
    "ds_read_b64_tr_b16 a[160:161], v90 offset:8192\n"
    "ds_read_b64_tr_b16 a[162:163], v90 offset:10240\n"
    "v_mfma_f32_32x32x16_bf16 v[192:207], a[104:107], v[52:55], v[192:207]\n"
    "ds_read_b64_tr_b16 a[96:97], v88 offset:24576\n"
    "ds_read_b64_tr_b16 a[98:99], v88 offset:26624\n"
    "ds_read_b64_tr_b16 a[164:165], v91 offset:8192\n"
    "v_mfma_f32_32x32x16_bf16 v[208:223], a[108:111], v[52:55], v[208:223]\n"
    "ds_read_b64_tr_b16 a[166:167], v91 offset:10240\n"
    "buffer_load_dwordx4 v31, s[52:55], s70 offen offset:3072 lds\n"
    "ds_read_b64_tr_b16 a[100:101], v89 offset:24576\n"
    "v_mfma_f32_32x32x16_bf16 v[192:207], a[112:115], v[56:59], v[192:207]\n"
    "ds_read_b64_tr_b16 a[102:103], v89 offset:26624\n"
    "ds_read_b64_tr_b16 a[168:169], v90 offset:8704\n"
    "ds_read_b64_tr_b16 a[170:171], v90 offset:10752\n"
    "v_mfma_f32_32x32x16_bf16 v[208:223], a[116:119], v[56:59], v[208:223]\n"
    "ds_read_b64_tr_b16 a[104:105], v88 offset:25088\n"
    "ds_read_b64_tr_b16 a[106:107], v88 offset:27136\n"
    "ds_read_b64_tr_b16 a[172:173], v91 offset:8704\n"
    "v_mfma_f32_32x32x16_bf16 v[192:207], a[120:123], v[60:63], v[192:207]\n"
    "ds_read_b64_tr_b16 a[174:175], v91 offset:10752\n"
    "ds_read_b64_tr_b16 a[108:109], v89 offset:25088\n"
    "ds_read_b64_tr_b16 a[110:111], v89 offset:27136\n"
    "v_mfma_f32_32x32x16_bf16 v[208:223], a[124:127], v[60:63], v[208:223]\n"
    "s_mov_b32 m0, s60\n"
    "s_mov_b32 s70, 0x200\n"
    "buffer_load_dwordx4 v28, s[52:55], s70 offen offset:0 lds\n"
    "ds_read_b64_tr_b16 a[176:177], v90 offset:12288\n"
    "ds_read_b64_tr_b16 a[178:179], v90 offset:14336\n"
    "s_waitcnt lgkmcnt(5)\n"
    "v_mfma_f32_32x32x16_bf16 v[192:207], a[128:131], v[96:99], v[192:207]\n"
    "ds_read_b64_tr_b16 a[112:113], v88 offset:28672\n"
    "ds_read_b64_tr_b16 a[114:115], v88 offset:30720\n"
    "ds_read_b64_tr_b16 a[180:181], v91 offset:12288\n"
    "v_mfma_f32_32x32x16_bf16 v[208:223], a[132:135], v[96:99], v[208:223]\n"
    "ds_read_b64_tr_b16 a[182:183], v91 offset:14336\n"
    "ds_read_b64_tr_b16 a[116:117], v89 offset:28672\n"
    "ds_read_b64_tr_b16 a[118:119], v89 offset:30720\n"
    "v_mfma_f32_32x32x16_bf16 v[192:207], a[136:139], v[100:103], v[192:207]\n"
    "ds_read_b64_tr_b16 a[184:185], v90 offset:12800\n"
    "buffer_load_dwordx4 v29, s[52:55], s70 offen offset:1024 lds\n"
    "ds_read_b64_tr_b16 a[186:187], v90 offset:14848\n"
    "v_mfma_f32_32x32x16_bf16 v[208:223], a[140:143], v[100:103], v[208:223]\n"
    "ds_read_b64_tr_b16 a[120:121], v88 offset:29184\n"
    "ds_read_b64_tr_b16 a[122:123], v88 offset:31232\n"
    "ds_read_b64_tr_b16 a[188:189], v91 offset:12800\n"
    "v_mfma_f32_32x32x16_bf16 v[192:207], a[144:147], v[104:107], v[192:207]\n"
    "ds_read_b64_tr_b16 a[190:191], v91 offset:14848\n"
    "ds_read_b64_tr_b16 a[124:125], v89 offset:29184\n"
    "ds_read_b64_tr_b16 a[126:127], v89 offset:31232\n"
    "v_mfma_f32_32x32x16_bf16 v[208:223], a[148:151], v[104:107], v[208:223]\n"
    "v_mfma_f32_32x32x16_bf16 v[192:207], a[152:155], v[108:111], v[192:207]\n"
    "buffer_load_dwordx4 v30, s[52:55], s70 offen offset:2048 lds\n"
    "v_mfma_f32_32x32x16_bf16 v[208:223], a[156:159], v[108:111], v[208:223]\n"
    "v_mfma_f32_32x32x16_bf16 v[192:207], a[160:163], v[112:115], v[192:207]\n"
    "v_mfma_f32_32x32x16_bf16 v[208:223], a[164:167], v[112:115], v[208:223]\n"
    "buffer_load_dwordx4 v31, s[52:55], s70 offen offset:3072 lds\n"
    "v_mfma_f32_32x32x16_bf16 v[192:207], a[168:171], v[116:119], v[192:207]\n"
    "s_waitcnt lgkmcnt(1)\n"
    "v_mfma_f32_32x32x16_bf16 v[208:223], a[172:175], v[116:119], v[208:223]\n"
    "v_mfma_f32_32x32x16_bf16 v[192:207], a[176:179], v[120:123], v[192:207]\n"
    "s_waitcnt vmcnt(16) lgkmcnt(0)\n"
    "s_barrier\n"
    "ds_read_b64_tr_b16 a[128:129], v90 offset:16384\n"
    "ds_read_b64_tr_b16 a[130:131], v90 offset:18432\n"
    "v_mfma_f32_32x32x16_bf16 v[208:223], a[180:183], v[120:123], v[208:223]\n"
    "ds_read_b64_tr_b16 a[132:133], v91 offset:16384\n"
    "ds_read_b64_tr_b16 a[134:135], v91 offset:18432\n"
    "ds_read_b64_tr_b16 a[136:137], v90 offset:16896\n"
    "v_mfma_f32_32x32x16_bf16 v[192:207], a[184:187], v[124:127], v[192:207]\n"
    "ds_read_b64_tr_b16 a[138:139], v90 offset:18944\n"
    "ds_read_b64_tr_b16 a[140:141], v91 offset:16896\n"
    "ds_read_b64_tr_b16 a[142:143], v91 offset:18944\n"
    "v_mfma_f32_32x32x16_bf16 v[208:223], a[188:191], v[124:127], v[208:223]\n"
    "ds_read_b64_tr_b16 a[144:145], v90 offset:20480\n"
    "ds_read_b64_tr_b16 a[146:147], v90 offset:22528\n"
    "ds_read_b64_tr_b16 a[148:149], v91 offset:20480\n"
    "v_mfma_f32_32x32x16_bf16 v[224:239], a[64:67], v[32:35], 0\n"
    "ds_read_b64_tr_b16 a[150:151], v91 offset:22528\n"
    "ds_read_b64_tr_b16 a[64:65], v88 offset:32768\n"
    "ds_read_b64_tr_b16 a[66:67], v88 offset:34816\n"
    "v_mfma_f32_32x32x16_bf16 v[240:255], a[68:71], v[32:35], 0\n"
    "s_mov_b32 m0, s64\n"
    "s_mov_b32 s70, 0x1c200\n"
    "buffer_load_dwordx4 v28, s[52:55], s70 offen offset:0 lds\n"
    "ds_read_b64_tr_b16 a[68:69], v89 offset:32768\n"
    "ds_read_b64_tr_b16 a[70:71], v89 offset:34816\n"
    "v_mfma_f32_32x32x16_bf16 v[224:239], a[72:75], v[36:39], v[224:239]\n"
    "ds_read_b64_tr_b16 a[152:153], v90 offset:20992\n"
    "ds_read_b64_tr_b16 a[72:73], v88 offset:33280\n"
    "ds_read_b64_tr_b16 a[74:75], v88 offset:35328\n"
    "v_mfma_f32_32x32x16_bf16 v[240:255], a[76:79], v[36:39], v[240:255]\n"
    "ds_read_b64_tr_b16 a[154:155], v90 offset:23040\n"
    "s_mov_b32 s72, 0\n"
    "v_cvt_pk_bf16_f32 v128, v192, v193\n"
    "ds_read_b64_tr_b16 a[76:77], v89 offset:33280\n"
    "ds_read_b64_tr_b16 a[78:79], v89 offset:35328\n"
    "v_cvt_pk_bf16_f32 v129, v194, v195\n"
    "v_cvt_pk_bf16_f32 v130, v196, v197\n"
    "v_mfma_f32_32x32x16_bf16 v[224:239], a[80:83], v[40:43], v[224:239]\n"
    "ds_read_b64_tr_b16 a[156:157], v91 offset:20992\n"
    "v_cvt_pk_bf16_f32 v131, v198, v199\n"
    "buffer_load_dwordx4 v29, s[52:55], s70 offen offset:1024 lds\n"
    "ds_read_b64_tr_b16 a[80:81], v88 offset:36864\n"
    "v_mfma_f32_32x32x16_bf16 v[240:255], a[84:87], v[40:43], v[240:255]\n"
    "ds_read_b64_tr_b16 a[82:83], v88 offset:38912\n"
    "ds_read_b64_tr_b16 a[84:85], v89 offset:36864\n"
    "ds_read_b64_tr_b16 a[86:87], v89 offset:38912\n"
    "buffer_store_dwordx4 v[128:131], v1, s[56:59], s72 offen offset:0 sc1\n"
    "v_mfma_f32_32x32x16_bf16 v[224:239], a[88:91], v[44:47], v[224:239]\n"
    "ds_read_b64_tr_b16 a[158:159], v91 offset:23040\n"
    "v_cvt_pk_bf16_f32 v132, v200, v201\n"
    "ds_read_b64_tr_b16 a[88:89], v88 offset:37376\n"
    "ds_read_b64_tr_b16 a[90:91], v88 offset:39424\n"
    "v_cvt_pk_bf16_f32 v133, v202, v203\n"
    "v_cvt_pk_bf16_f32 v134, v204, v205\n"
    "v_mfma_f32_32x32x16_bf16 v[240:255], a[92:95], v[44:47], v[240:255]\n"
    "ds_read_b64_tr_b16 a[160:161], v90 offset:24576\n"
    "v_cvt_pk_bf16_f32 v135, v206, v207\n"
    "ds_read_b64_tr_b16 a[92:93], v89 offset:37376\n"
    "ds_read_b64_tr_b16 a[94:95], v89 offset:39424\n"
    "buffer_store_dwordx4 v[132:135], v1, s[56:59], s72 offen offset:1024 sc1\n"
    "v_mfma_f32_32x32x16_bf16 v[224:239], a[96:99], v[48:51], v[224:239]\n"
    "buffer_load_dwordx4 v30, s[52:55], s70 offen offset:2048 lds\n"
    "ds_read_b64_tr_b16 a[162:163], v90 offset:26624\n"
    "ds_read_b64_tr_b16 a[96:97], v88 offset:40960\n"
    "v_cvt_pk_bf16_f32 v136, v208, v209\n"
    "v_cvt_pk_bf16_f32 v137, v210, v211\n"
    "v_mfma_f32_32x32x16_bf16 v[240:255], a[100:103], v[48:51], v[240:255]\n"
    "ds_read_b64_tr_b16 a[98:99], v88 offset:43008\n"
    "v_cvt_pk_bf16_f32 v138, v212, v213\n"
    "ds_read_b64_tr_b16 a[164:165], v91 offset:24576\n"
    "ds_read_b64_tr_b16 a[166:167], v91 offset:26624\n"
    "v_cvt_pk_bf16_f32 v139, v214, v215\n"
    "v_mfma_f32_32x32x16_bf16 v[224:239], a[104:107], v[52:55], v[224:239]\n"
    "ds_read_b64_tr_b16 a[100:101], v89 offset:40960\n"
    "ds_read_b64_tr_b16 a[102:103], v89 offset:43008\n"
    "ds_read_b64_tr_b16 a[168:169], v90 offset:25088\n"
    "buffer_store_dwordx4 v[136:139], v1, s[56:59], s72 offen offset:2048 sc1\n"
    "v_mfma_f32_32x32x16_bf16 v[240:255], a[108:111], v[52:55], v[240:255]\n"
    "ds_read_b64_tr_b16 a[170:171], v90 offset:27136\n"
    "v_cvt_pk_bf16_f32 v140, v216, v217\n"
    "buffer_load_dwordx4 v31, s[52:55], s70 offen offset:3072 lds\n"
    "ds_read_b64_tr_b16 a[104:105], v88 offset:41472\n"
    "v_cvt_pk_bf16_f32 v141, v218, v219\n"
    "v_mfma_f32_32x32x16_bf16 v[224:239], a[112:115], v[56:59], v[224:239]\n"
    "ds_read_b64_tr_b16 a[106:107], v88 offset:43520\n"
    "v_cvt_pk_bf16_f32 v142, v220, v221\n"
    "ds_read_b64_tr_b16 a[172:173], v91 offset:25088\n"
    "ds_read_b64_tr_b16 a[174:175], v91 offset:27136\n"
    "v_cvt_pk_bf16_f32 v143, v222, v223\n"
    "v_mfma_f32_32x32x16_bf16 v[240:255], a[116:119], v[56:59], v[240:255]\n"
    "ds_read_b64_tr_b16 a[108:109], v89 offset:41472\n"
    "ds_read_b64_tr_b16 a[110:111], v89 offset:43520\n"
    "ds_read_b64_tr_b16 a[176:177], v90 offset:28672\n"
    "buffer_store_dwordx4 v[140:143], v1, s[56:59], s72 offen offset:3072 sc1\n"
    "v_mfma_f32_32x32x16_bf16 v[224:239], a[120:123], v[60:63], v[224:239]\n"
    "ds_read_b64_tr_b16 a[178:179], v90 offset:30720\n"
    "ds_read_b64_tr_b16 a[112:113], v88 offset:45056\n"
    "ds_read_b64_tr_b16 a[114:115], v88 offset:47104\n"
    "v_mfma_f32_32x32x16_bf16 v[240:255], a[124:127], v[60:63], v[240:255]\n"
    "s_mov_b32 m0, s61\n"
    "s_mov_b32 s70, 0x280\n"
    "buffer_load_dwordx4 v28, s[52:55], s70 offen offset:0 lds\n"
    "ds_read_b64_tr_b16 a[180:181], v91 offset:28672\n"
    "ds_read_b64_tr_b16 a[182:183], v91 offset:30720\n"
    "s_waitcnt lgkmcnt(5)\n"
    "v_mfma_f32_32x32x16_bf16 v[224:239], a[128:131], v[96:99], v[224:239]\n"
    "ds_read_b64_tr_b16 a[116:117], v89 offset:45056\n"
    "ds_read_b64_tr_b16 a[118:119], v89 offset:47104\n"
    "ds_read_b64_tr_b16 a[184:185], v90 offset:29184\n"
    "v_mfma_f32_32x32x16_bf16 v[240:255], a[132:135], v[96:99], v[240:255]\n"
    "ds_read_b64_tr_b16 a[186:187], v90 offset:31232\n"
    "ds_read_b64_tr_b16 a[120:121], v88 offset:45568\n"
    "ds_read_b64_tr_b16 a[122:123], v88 offset:47616\n"
    "v_mfma_f32_32x32x16_bf16 v[224:239], a[136:139], v[100:103], v[224:239]\n"
    "ds_read_b64_tr_b16 a[188:189], v91 offset:29184\n"
    "buffer_load_dwordx4 v29, s[52:55], s70 offen offset:1024 lds\n"
    "ds_read_b64_tr_b16 a[190:191], v91 offset:31232\n"
    "v_mfma_f32_32x32x16_bf16 v[240:255], a[140:143], v[100:103], v[240:255]\n"
    "ds_read_b64_tr_b16 a[124:125], v89 offset:45568\n"
    "ds_read_b64_tr_b16 a[126:127], v89 offset:47616\n"
    "v_mfma_f32_32x32x16_bf16 v[224:239], a[144:147], v[104:107], v[224:239]\n"
    "v_mfma_f32_32x32x16_bf16 v[240:255], a[148:151], v[104:107], v[240:255]\n"
    "v_mfma_f32_32x32x16_bf16 v[224:239], a[152:155], v[108:111], v[224:239]\n"
    "buffer_load_dwordx4 v30, s[52:55], s70 offen offset:2048 lds\n"
    "v_mfma_f32_32x32x16_bf16 v[240:255], a[156:159], v[108:111], v[240:255]\n"
    "v_mfma_f32_32x32x16_bf16 v[224:239], a[160:163], v[112:115], v[224:239]\n"
    "v_mfma_f32_32x32x16_bf16 v[240:255], a[164:167], v[112:115], v[240:255]\n"
    "buffer_load_dwordx4 v31, s[52:55], s70 offen offset:3072 lds\n"
    "v_mfma_f32_32x32x16_bf16 v[224:239], a[168:171], v[116:119], v[224:239]\n"
    "v_mfma_f32_32x32x16_bf16 v[240:255], a[172:175], v[116:119], v[240:255]\n"
    "s_waitcnt lgkmcnt(0)\n"
    "v_mfma_f32_32x32x16_bf16 v[224:239], a[176:179], v[120:123], v[224:239]\n"
    "s_waitcnt vmcnt(20) lgkmcnt(0)\n"
    "s_barrier\n"
    "ds_read_b64_tr_b16 a[128:129], v90 offset:32768\n"
    "ds_read_b64_tr_b16 a[130:131], v90 offset:34816\n"
    "v_mfma_f32_32x32x16_bf16 v[240:255], a[180:183], v[120:123], v[240:255]\n"
    "ds_read_b64_tr_b16 a[132:133], v91 offset:32768\n"
    "ds_read_b64_tr_b16 a[134:135], v91 offset:34816\n"
    "ds_read_b64_tr_b16 a[136:137], v90 offset:33280\n"
    "v_mfma_f32_32x32x16_bf16 v[224:239], a[184:187], v[124:127], v[224:239]\n"
    "ds_read_b64_tr_b16 a[138:139], v90 offset:35328\n"
    "ds_read_b64_tr_b16 a[140:141], v91 offset:33280\n"
    "ds_read_b64_tr_b16 a[142:143], v91 offset:35328\n"
    "v_mfma_f32_32x32x16_bf16 v[240:255], a[188:191], v[124:127], v[240:255]\n"
    "ds_read_b64_tr_b16 a[144:145], v90 offset:36864\n"
    "ds_read_b64_tr_b16 a[146:147], v90 offset:38912\n"
    "ds_read_b64_tr_b16 a[148:149], v91 offset:36864\n"
    "v_mfma_f32_32x32x16_bf16 v[192:207], a[64:67], v[32:35], 0\n"
    "ds_read_b64_tr_b16 a[150:151], v91 offset:38912\n"
    "ds_read_b64_tr_b16 a[64:65], v88 offset:49152\n"
    "ds_read_b64_tr_b16 a[66:67], v88 offset:51200\n"
    "v_mfma_f32_32x32x16_bf16 v[208:223], a[68:71], v[32:35], 0\n"
    "s_mov_b32 m0, s65\n"
    "s_mov_b32 s70, 0x1c280\n"
    "buffer_load_dwordx4 v28, s[52:55], s70 offen offset:0 lds\n"
    "ds_read_b64_tr_b16 a[68:69], v89 offset:49152\n"
    "ds_read_b64_tr_b16 a[70:71], v89 offset:51200\n"
    "v_mfma_f32_32x32x16_bf16 v[192:207], a[72:75], v[36:39], v[192:207]\n"
    "ds_read_b64_tr_b16 a[152:153], v90 offset:37376\n"
    "ds_read_b64_tr_b16 a[72:73], v88 offset:49664\n"
    "ds_read_b64_tr_b16 a[74:75], v88 offset:51712\n"
    "v_mfma_f32_32x32x16_bf16 v[208:223], a[76:79], v[36:39], v[208:223]\n"
    "ds_read_b64_tr_b16 a[154:155], v90 offset:39424\n"
    "s_mov_b32 s72, 0x1000\n"
    "v_cvt_pk_bf16_f32 v128, v224, v225\n"
    "ds_read_b64_tr_b16 a[76:77], v89 offset:49664\n"
    "ds_read_b64_tr_b16 a[78:79], v89 offset:51712\n"
    "v_cvt_pk_bf16_f32 v129, v226, v227\n"
    "v_cvt_pk_bf16_f32 v130, v228, v229\n"
    "v_mfma_f32_32x32x16_bf16 v[192:207], a[80:83], v[40:43], v[192:207]\n"
    "ds_read_b64_tr_b16 a[156:157], v91 offset:37376\n"
    "v_cvt_pk_bf16_f32 v131, v230, v231\n"
    "buffer_load_dwordx4 v29, s[52:55], s70 offen offset:1024 lds\n"
    "ds_read_b64_tr_b16 a[80:81], v88 offset:53248\n"
    "v_mfma_f32_32x32x16_bf16 v[208:223], a[84:87], v[40:43], v[208:223]\n"
    "ds_read_b64_tr_b16 a[82:83], v88 offset:55296\n"
    "ds_read_b64_tr_b16 a[84:85], v89 offset:53248\n"
    "ds_read_b64_tr_b16 a[86:87], v89 offset:55296\n"
    "buffer_store_dwordx4 v[128:131], v1, s[56:59], s72 offen offset:0 sc1\n"
    "v_mfma_f32_32x32x16_bf16 v[192:207], a[88:91], v[44:47], v[192:207]\n"
    "ds_read_b64_tr_b16 a[158:159], v91 offset:39424\n"
    "v_cvt_pk_bf16_f32 v132, v232, v233\n"
    "ds_read_b64_tr_b16 a[88:89], v88 offset:53760\n"
    "ds_read_b64_tr_b16 a[90:91], v88 offset:55808\n"
    "v_cvt_pk_bf16_f32 v133, v234, v235\n"
    "v_cvt_pk_bf16_f32 v134, v236, v237\n"
    "v_mfma_f32_32x32x16_bf16 v[208:223], a[92:95], v[44:47], v[208:223]\n"
    "ds_read_b64_tr_b16 a[160:161], v90 offset:40960\n"
    "v_cvt_pk_bf16_f32 v135, v238, v239\n"
    "ds_read_b64_tr_b16 a[92:93], v89 offset:53760\n"
    "ds_read_b64_tr_b16 a[94:95], v89 offset:55808\n"
    "buffer_store_dwordx4 v[132:135], v1, s[56:59], s72 offen offset:1024 sc1\n"
    "v_mfma_f32_32x32x16_bf16 v[192:207], a[96:99], v[48:51], v[192:207]\n"
    "buffer_load_dwordx4 v30, s[52:55], s70 offen offset:2048 lds\n"
    "ds_read_b64_tr_b16 a[162:163], v90 offset:43008\n"
    "ds_read_b64_tr_b16 a[96:97], v88 offset:57344\n"
    "v_cvt_pk_bf16_f32 v136, v240, v241\n"
    "v_cvt_pk_bf16_f32 v137, v242, v243\n"
    "v_mfma_f32_32x32x16_bf16 v[208:223], a[100:103], v[48:51], v[208:223]\n"
    "ds_read_b64_tr_b16 a[98:99], v88 offset:59392\n"
    "v_cvt_pk_bf16_f32 v138, v244, v245\n"
    "ds_read_b64_tr_b16 a[164:165], v91 offset:40960\n"
    "ds_read_b64_tr_b16 a[166:167], v91 offset:43008\n"
    "v_cvt_pk_bf16_f32 v139, v246, v247\n"
    "v_mfma_f32_32x32x16_bf16 v[192:207], a[104:107], v[52:55], v[192:207]\n"
    "ds_read_b64_tr_b16 a[100:101], v89 offset:57344\n"
    "ds_read_b64_tr_b16 a[102:103], v89 offset:59392\n"
    "ds_read_b64_tr_b16 a[168:169], v90 offset:41472\n"
    "buffer_store_dwordx4 v[136:139], v1, s[56:59], s72 offen offset:2048 sc1\n"
    "v_mfma_f32_32x32x16_bf16 v[208:223], a[108:111], v[52:55], v[208:223]\n"
    "ds_read_b64_tr_b16 a[170:171], v90 offset:43520\n"
    "v_cvt_pk_bf16_f32 v140, v248, v249\n"
    "buffer_load_dwordx4 v31, s[52:55], s70 offen offset:3072 lds\n"
    "ds_read_b64_tr_b16 a[104:105], v88 offset:57856\n"
    "v_cvt_pk_bf16_f32 v141, v250, v251\n"
    "v_mfma_f32_32x32x16_bf16 v[192:207], a[112:115], v[56:59], v[192:207]\n"
    "ds_read_b64_tr_b16 a[106:107], v88 offset:59904\n"
    "v_cvt_pk_bf16_f32 v142, v252, v253\n"
    "ds_read_b64_tr_b16 a[172:173], v91 offset:41472\n"
    "ds_read_b64_tr_b16 a[174:175], v91 offset:43520\n"
    "v_cvt_pk_bf16_f32 v143, v254, v255\n"
    "v_mfma_f32_32x32x16_bf16 v[208:223], a[116:119], v[56:59], v[208:223]\n"
    "ds_read_b64_tr_b16 a[108:109], v89 offset:57856\n"
    "ds_read_b64_tr_b16 a[110:111], v89 offset:59904\n"
    "ds_read_b64_tr_b16 a[176:177], v90 offset:45056\n"
    "buffer_store_dwordx4 v[140:143], v1, s[56:59], s72 offen offset:3072 sc1\n"
    "v_mfma_f32_32x32x16_bf16 v[192:207], a[120:123], v[60:63], v[192:207]\n"
    "ds_read_b64_tr_b16 a[178:179], v90 offset:47104\n"
    "ds_read_b64_tr_b16 a[112:113], v88 offset:61440\n"
    "ds_read_b64_tr_b16 a[114:115], v88 offset:63488\n"
    "v_mfma_f32_32x32x16_bf16 v[208:223], a[124:127], v[60:63], v[208:223]\n"
    "s_mov_b32 m0, s62\n"
    "s_mov_b32 s70, 0x300\n"
    "buffer_load_dwordx4 v28, s[52:55], s70 offen offset:0 lds\n"
    "ds_read_b64_tr_b16 a[180:181], v91 offset:45056\n"
    "ds_read_b64_tr_b16 a[182:183], v91 offset:47104\n"
    "s_waitcnt lgkmcnt(5)\n"
    "v_mfma_f32_32x32x16_bf16 v[192:207], a[128:131], v[96:99], v[192:207]\n"
    "ds_read_b64_tr_b16 a[116:117], v89 offset:61440\n"
    "ds_read_b64_tr_b16 a[118:119], v89 offset:63488\n"
    "ds_read_b64_tr_b16 a[184:185], v90 offset:45568\n"
    "v_mfma_f32_32x32x16_bf16 v[208:223], a[132:135], v[96:99], v[208:223]\n"
    "ds_read_b64_tr_b16 a[186:187], v90 offset:47616\n"
    "ds_read_b64_tr_b16 a[120:121], v88 offset:61952\n"
    "ds_read_b64_tr_b16 a[122:123], v88 offset:64000\n"
    "v_mfma_f32_32x32x16_bf16 v[192:207], a[136:139], v[100:103], v[192:207]\n"
    "ds_read_b64_tr_b16 a[188:189], v91 offset:45568\n"
    "buffer_load_dwordx4 v29, s[52:55], s70 offen offset:1024 lds\n"
    "ds_read_b64_tr_b16 a[190:191], v91 offset:47616\n"
    "v_mfma_f32_32x32x16_bf16 v[208:223], a[140:143], v[100:103], v[208:223]\n"
    "ds_read_b64_tr_b16 a[124:125], v89 offset:61952\n"
    "ds_read_b64_tr_b16 a[126:127], v89 offset:64000\n"
    "v_mfma_f32_32x32x16_bf16 v[192:207], a[144:147], v[104:107], v[192:207]\n"
    "v_mfma_f32_32x32x16_bf16 v[208:223], a[148:151], v[104:107], v[208:223]\n"
    "v_mfma_f32_32x32x16_bf16 v[192:207], a[152:155], v[108:111], v[192:207]\n"
    "buffer_load_dwordx4 v30, s[52:55], s70 offen offset:2048 lds\n"
    "v_mfma_f32_32x32x16_bf16 v[208:223], a[156:159], v[108:111], v[208:223]\n"
    "v_mfma_f32_32x32x16_bf16 v[192:207], a[160:163], v[112:115], v[192:207]\n"
    "v_mfma_f32_32x32x16_bf16 v[208:223], a[164:167], v[112:115], v[208:223]\n"
    "buffer_load_dwordx4 v31, s[52:55], s70 offen offset:3072 lds\n"
    "v_mfma_f32_32x32x16_bf16 v[192:207], a[168:171], v[116:119], v[192:207]\n"
    "v_mfma_f32_32x32x16_bf16 v[208:223], a[172:175], v[116:119], v[208:223]\n"
    "s_waitcnt lgkmcnt(0)\n"
    "v_mfma_f32_32x32x16_bf16 v[192:207], a[176:179], v[120:123], v[192:207]\n"
    "s_waitcnt vmcnt(24) lgkmcnt(0)\n"
    "s_barrier\n"
    "ds_read_b64_tr_b16 a[128:129], v90 offset:49152\n"
    "ds_read_b64_tr_b16 a[130:131], v90 offset:51200\n"
    "v_mfma_f32_32x32x16_bf16 v[208:223], a[180:183], v[120:123], v[208:223]\n"
    "ds_read_b64_tr_b16 a[132:133], v91 offset:49152\n"
    "ds_read_b64_tr_b16 a[134:135], v91 offset:51200\n"
    "ds_read_b64_tr_b16 a[136:137], v90 offset:49664\n"
    "v_mfma_f32_32x32x16_bf16 v[192:207], a[184:187], v[124:127], v[192:207]\n"
    "ds_read_b64_tr_b16 a[138:139], v90 offset:51712\n"
    "ds_read_b64_tr_b16 a[140:141], v91 offset:49664\n"
    "ds_read_b64_tr_b16 a[142:143], v91 offset:51712\n"
    "v_mfma_f32_32x32x16_bf16 v[208:223], a[188:191], v[124:127], v[208:223]\n"
    "ds_read_b64_tr_b16 a[144:145], v90 offset:53248\n"
    "ds_read_b64_tr_b16 a[146:147], v90 offset:55296\n"
    "ds_read_b64_tr_b16 a[148:149], v91 offset:53248\n"
    "v_mfma_f32_32x32x16_bf16 v[224:239], a[64:67], v[32:35], 0\n"
    "ds_read_b64_tr_b16 a[150:151], v91 offset:55296\n"
    "ds_read_b64_tr_b16 a[64:65], v88 offset:0\n"
    "ds_read_b64_tr_b16 a[66:67], v88 offset:2048\n"
    "v_mfma_f32_32x32x16_bf16 v[240:255], a[68:71], v[32:35], 0\n"
    "s_mov_b32 m0, s66\n"
    "s_mov_b32 s70, 0x1c300\n"
    "buffer_load_dwordx4 v28, s[52:55], s70 offen offset:0 lds\n"
    "ds_read_b64_tr_b16 a[68:69], v89 offset:0\n"
    "ds_read_b64_tr_b16 a[70:71], v89 offset:2048\n"
    "v_mfma_f32_32x32x16_bf16 v[224:239], a[72:75], v[36:39], v[224:239]\n"
    "ds_read_b64_tr_b16 a[152:153], v90 offset:53760\n"
    "ds_read_b64_tr_b16 a[72:73], v88 offset:512\n"
    "ds_read_b64_tr_b16 a[74:75], v88 offset:2560\n"
    "v_mfma_f32_32x32x16_bf16 v[240:255], a[76:79], v[36:39], v[240:255]\n"
    "ds_read_b64_tr_b16 a[154:155], v90 offset:55808\n"
    "s_mov_b32 s72, 0x2000\n"
    "v_cvt_pk_bf16_f32 v128, v192, v193\n"
    "ds_read_b64_tr_b16 a[76:77], v89 offset:512\n"
    "ds_read_b64_tr_b16 a[78:79], v89 offset:2560\n"
    "v_cvt_pk_bf16_f32 v129, v194, v195\n"
    "v_cvt_pk_bf16_f32 v130, v196, v197\n"
    "v_mfma_f32_32x32x16_bf16 v[224:239], a[80:83], v[40:43], v[224:239]\n"
    "ds_read_b64_tr_b16 a[156:157], v91 offset:53760\n"
    "v_cvt_pk_bf16_f32 v131, v198, v199\n"
    "buffer_load_dwordx4 v29, s[52:55], s70 offen offset:1024 lds\n"
    "ds_read_b64_tr_b16 a[80:81], v88 offset:4096\n"
    "v_mfma_f32_32x32x16_bf16 v[240:255], a[84:87], v[40:43], v[240:255]\n"
    "ds_read_b64_tr_b16 a[82:83], v88 offset:6144\n"
    "ds_read_b64_tr_b16 a[84:85], v89 offset:4096\n"
    "ds_read_b64_tr_b16 a[86:87], v89 offset:6144\n"
    "buffer_store_dwordx4 v[128:131], v1, s[56:59], s72 offen offset:0 sc1\n"
    "v_mfma_f32_32x32x16_bf16 v[224:239], a[88:91], v[44:47], v[224:239]\n"
    "ds_read_b64_tr_b16 a[158:159], v91 offset:55808\n"
    "v_cvt_pk_bf16_f32 v132, v200, v201\n"
    "ds_read_b64_tr_b16 a[88:89], v88 offset:4608\n"
    "ds_read_b64_tr_b16 a[90:91], v88 offset:6656\n"
    "v_cvt_pk_bf16_f32 v133, v202, v203\n"
    "v_cvt_pk_bf16_f32 v134, v204, v205\n"
    "v_mfma_f32_32x32x16_bf16 v[240:255], a[92:95], v[44:47], v[240:255]\n"
    "ds_read_b64_tr_b16 a[160:161], v90 offset:57344\n"
    "v_cvt_pk_bf16_f32 v135, v206, v207\n"
    "ds_read_b64_tr_b16 a[92:93], v89 offset:4608\n"
    "ds_read_b64_tr_b16 a[94:95], v89 offset:6656\n"
    "buffer_store_dwordx4 v[132:135], v1, s[56:59], s72 offen offset:1024 sc1\n"
    "v_mfma_f32_32x32x16_bf16 v[224:239], a[96:99], v[48:51], v[224:239]\n"
    "buffer_load_dwordx4 v30, s[52:55], s70 offen offset:2048 lds\n"
    "ds_read_b64_tr_b16 a[162:163], v90 offset:59392\n"
    "ds_read_b64_tr_b16 a[96:97], v88 offset:8192\n"
    "v_cvt_pk_bf16_f32 v136, v208, v209\n"
    "v_cvt_pk_bf16_f32 v137, v210, v211\n"
    "v_mfma_f32_32x32x16_bf16 v[240:255], a[100:103], v[48:51], v[240:255]\n"
    "ds_read_b64_tr_b16 a[98:99], v88 offset:10240\n"
    "v_cvt_pk_bf16_f32 v138, v212, v213\n"
    "ds_read_b64_tr_b16 a[164:165], v91 offset:57344\n"
    "ds_read_b64_tr_b16 a[166:167], v91 offset:59392\n"
    "v_cvt_pk_bf16_f32 v139, v214, v215\n"
    "v_mfma_f32_32x32x16_bf16 v[224:239], a[104:107], v[52:55], v[224:239]\n"
    "ds_read_b64_tr_b16 a[100:101], v89 offset:8192\n"
    "ds_read_b64_tr_b16 a[102:103], v89 offset:10240\n"
    "ds_read_b64_tr_b16 a[168:169], v90 offset:57856\n"
    "buffer_store_dwordx4 v[136:139], v1, s[56:59], s72 offen offset:2048 sc1\n"
    "v_mfma_f32_32x32x16_bf16 v[240:255], a[108:111], v[52:55], v[240:255]\n"
    "ds_read_b64_tr_b16 a[170:171], v90 offset:59904\n"
    "v_cvt_pk_bf16_f32 v140, v216, v217\n"
    "buffer_load_dwordx4 v31, s[52:55], s70 offen offset:3072 lds\n"
    "ds_read_b64_tr_b16 a[104:105], v88 offset:8704\n"
    "v_cvt_pk_bf16_f32 v141, v218, v219\n"
    "v_mfma_f32_32x32x16_bf16 v[224:239], a[112:115], v[56:59], v[224:239]\n"
    "ds_read_b64_tr_b16 a[106:107], v88 offset:10752\n"
    "v_cvt_pk_bf16_f32 v142, v220, v221\n"
    "ds_read_b64_tr_b16 a[172:173], v91 offset:57856\n"
    "ds_read_b64_tr_b16 a[174:175], v91 offset:59904\n"
    "v_cvt_pk_bf16_f32 v143, v222, v223\n"
    "v_mfma_f32_32x32x16_bf16 v[240:255], a[116:119], v[56:59], v[240:255]\n"
    "ds_read_b64_tr_b16 a[108:109], v89 offset:8704\n"
    "ds_read_b64_tr_b16 a[110:111], v89 offset:10752\n"
    "ds_read_b64_tr_b16 a[176:177], v90 offset:61440\n"
    "buffer_store_dwordx4 v[140:143], v1, s[56:59], s72 offen offset:3072 sc1\n"
    "v_mfma_f32_32x32x16_bf16 v[224:239], a[120:123], v[60:63], v[224:239]\n"
    "ds_read_b64_tr_b16 a[178:179], v90 offset:63488\n"
    "ds_read_b64_tr_b16 a[112:113], v88 offset:12288\n"
    "ds_read_b64_tr_b16 a[114:115], v88 offset:14336\n"
    "v_mfma_f32_32x32x16_bf16 v[240:255], a[124:127], v[60:63], v[240:255]\n"
    "ds_read_b64_tr_b16 a[180:181], v91 offset:61440\n"
    "ds_read_b64_tr_b16 a[182:183], v91 offset:63488\n"
    "ds_read_b64_tr_b16 a[116:117], v89 offset:12288\n"
    "s_waitcnt lgkmcnt(6)\n"
    "v_mfma_f32_32x32x16_bf16 v[224:239], a[128:131], v[96:99], v[224:239]\n"
    "ds_read_b64_tr_b16 a[118:119], v89 offset:14336\n"
    "ds_read_b64_tr_b16 a[184:185], v90 offset:61952\n"
    "ds_read_b64_tr_b16 a[186:187], v90 offset:64000\n"
    "v_mfma_f32_32x32x16_bf16 v[240:255], a[132:135], v[96:99], v[240:255]\n"
    "ds_read_b64_tr_b16 a[120:121], v88 offset:12800\n"
    "ds_read_b64_tr_b16 a[122:123], v88 offset:14848\n"
    "ds_read_b64_tr_b16 a[188:189], v91 offset:61952\n"
    "v_mfma_f32_32x32x16_bf16 v[224:239], a[136:139], v[100:103], v[224:239]\n"
    "ds_read_b64_tr_b16 a[190:191], v91 offset:64000\n"
    "ds_read_b64_tr_b16 a[124:125], v89 offset:12800\n"
    "ds_read_b64_tr_b16 a[126:127], v89 offset:14848\n"
    "v_mfma_f32_32x32x16_bf16 v[240:255], a[140:143], v[100:103], v[240:255]\n"
    "v_mfma_f32_32x32x16_bf16 v[224:239], a[144:147], v[104:107], v[224:239]\n"
    "v_mfma_f32_32x32x16_bf16 v[240:255], a[148:151], v[104:107], v[240:255]\n"
    "v_mfma_f32_32x32x16_bf16 v[224:239], a[152:155], v[108:111], v[224:239]\n"
    "v_mfma_f32_32x32x16_bf16 v[240:255], a[156:159], v[108:111], v[240:255]\n"
    "v_mfma_f32_32x32x16_bf16 v[224:239], a[160:163], v[112:115], v[224:239]\n"
    "v_mfma_f32_32x32x16_bf16 v[240:255], a[164:167], v[112:115], v[240:255]\n"
    "v_mfma_f32_32x32x16_bf16 v[224:239], a[168:171], v[116:119], v[224:239]\n"
    "v_mfma_f32_32x32x16_bf16 v[240:255], a[172:175], v[116:119], v[240:255]\n"
    "s_waitcnt lgkmcnt(0)\n"
    "v_mfma_f32_32x32x16_bf16 v[224:239], a[176:179], v[120:123], v[224:239]\n"
    "s_waitcnt vmcnt(20) lgkmcnt(0)\n"
    "s_barrier\n"
    "ds_read_b64_tr_b16 a[128:129], v90 offset:0\n"
    "ds_read_b64_tr_b16 a[130:131], v90 offset:2048\n"
    "v_mfma_f32_32x32x16_bf16 v[240:255], a[180:183], v[120:123], v[240:255]\n"
    "ds_read_b64_tr_b16 a[132:133], v91 offset:0\n"
    "ds_read_b64_tr_b16 a[134:135], v91 offset:2048\n"
    "ds_read_b64_tr_b16 a[136:137], v90 offset:512\n"
    "v_mfma_f32_32x32x16_bf16 v[224:239], a[184:187], v[124:127], v[224:239]\n"
    "ds_read_b64_tr_b16 a[138:139], v90 offset:2560\n"
    "ds_read_b64_tr_b16 a[140:141], v91 offset:512\n"
    "ds_read_b64_tr_b16 a[142:143], v91 offset:2560\n"
    "v_mfma_f32_32x32x16_bf16 v[240:255], a[188:191], v[124:127], v[240:255]\n"
    "ds_read_b64_tr_b16 a[144:145], v90 offset:4096\n"
    "ds_read_b64_tr_b16 a[146:147], v90 offset:6144\n"
    "ds_read_b64_tr_b16 a[148:149], v91 offset:4096\n"
    "v_mfma_f32_32x32x16_bf16 v[192:207], a[64:67], v[32:35], 0\n"
    "ds_read_b64_tr_b16 a[150:151], v91 offset:6144\n"
    "ds_read_b64_tr_b16 a[64:65], v88 offset:16384\n"
    "ds_read_b64_tr_b16 a[66:67], v88 offset:18432\n"
    "v_mfma_f32_32x32x16_bf16 v[208:223], a[68:71], v[32:35], 0\n"
    "ds_read_b64_tr_b16 a[152:153], v90 offset:4608\n"
    "ds_read_b64_tr_b16 a[68:69], v89 offset:16384\n"
    "ds_read_b64_tr_b16 a[70:71], v89 offset:18432\n"
    "v_mfma_f32_32x32x16_bf16 v[192:207], a[72:75], v[36:39], v[192:207]\n"
    "ds_read_b64_tr_b16 a[154:155], v90 offset:6656\n"
    "ds_read_b64_tr_b16 a[72:73], v88 offset:16896\n"
    "ds_read_b64_tr_b16 a[74:75], v88 offset:18944\n"
    "v_mfma_f32_32x32x16_bf16 v[208:223], a[76:79], v[36:39], v[208:223]\n"
    "ds_read_b64_tr_b16 a[156:157], v91 offset:4608\n"
    "s_mov_b32 s72, 0x3000\n"
    "v_cvt_pk_bf16_f32 v128, v224, v225\n"
    "ds_read_b64_tr_b16 a[76:77], v89 offset:16896\n"
    "ds_read_b64_tr_b16 a[78:79], v89 offset:18944\n"
    "v_cvt_pk_bf16_f32 v129, v226, v227\n"
    "v_cvt_pk_bf16_f32 v130, v228, v229\n"
    "v_mfma_f32_32x32x16_bf16 v[192:207], a[80:83], v[40:43], v[192:207]\n"
    "ds_read_b64_tr_b16 a[158:159], v91 offset:6656\n"
    "v_cvt_pk_bf16_f32 v131, v230, v231\n"
    "ds_read_b64_tr_b16 a[80:81], v88 offset:20480\n"
    "ds_read_b64_tr_b16 a[82:83], v88 offset:22528\n"
    "buffer_store_dwordx4 v[128:131], v1, s[56:59], s72 offen offset:0 sc1\n"
    "v_mfma_f32_32x32x16_bf16 v[208:223], a[84:87], v[40:43], v[208:223]\n"
    "ds_read_b64_tr_b16 a[160:161], v90 offset:8192\n"
    "v_cvt_pk_bf16_f32 v132, v232, v233\n"
    "ds_read_b64_tr_b16 a[84:85], v89 offset:20480\n"
    "ds_read_b64_tr_b16 a[86:87], v89 offset:22528\n"
    "v_cvt_pk_bf16_f32 v133, v234, v235\n"
    "v_cvt_pk_bf16_f32 v134, v236, v237\n"
    "v_mfma_f32_32x32x16_bf16 v[192:207], a[88:91], v[44:47], v[192:207]\n"
    "ds_read_b64_tr_b16 a[162:163], v90 offset:10240\n"
    "v_cvt_pk_bf16_f32 v135, v238, v239\n"
    "ds_read_b64_tr_b16 a[88:89], v88 offset:20992\n"
    "ds_read_b64_tr_b16 a[90:91], v88 offset:23040\n"
    "buffer_store_dwordx4 v[132:135], v1, s[56:59], s72 offen offset:1024 sc1\n"
    "v_mfma_f32_32x32x16_bf16 v[208:223], a[92:95], v[44:47], v[208:223]\n"
    "ds_read_b64_tr_b16 a[164:165], v91 offset:8192\n"
    "v_cvt_pk_bf16_f32 v136, v240, v241\n"
    "ds_read_b64_tr_b16 a[92:93], v89 offset:20992\n"
    "ds_read_b64_tr_b16 a[94:95], v89 offset:23040\n"
    "v_cvt_pk_bf16_f32 v137, v242, v243\n"
    "v_cvt_pk_bf16_f32 v138, v244, v245\n"
    "v_mfma_f32_32x32x16_bf16 v[192:207], a[96:99], v[48:51], v[192:207]\n"
    "ds_read_b64_tr_b16 a[166:167], v91 offset:10240\n"
    "v_cvt_pk_bf16_f32 v139, v246, v247\n"
    "ds_read_b64_tr_b16 a[96:97], v88 offset:24576\n"
    "ds_read_b64_tr_b16 a[98:99], v88 offset:26624\n"
    "buffer_store_dwordx4 v[136:139], v1, s[56:59], s72 offen offset:2048 sc1\n"
    "v_mfma_f32_32x32x16_bf16 v[208:223], a[100:103], v[48:51], v[208:223]\n"
    "ds_read_b64_tr_b16 a[168:169], v90 offset:8704\n"
    "v_cvt_pk_bf16_f32 v140, v248, v249\n"
    "ds_read_b64_tr_b16 a[100:101], v89 offset:24576\n"
    "ds_read_b64_tr_b16 a[102:103], v89 offset:26624\n"
    "v_cvt_pk_bf16_f32 v141, v250, v251\n"
    "v_cvt_pk_bf16_f32 v142, v252, v253\n"
    "v_mfma_f32_32x32x16_bf16 v[192:207], a[104:107], v[52:55], v[192:207]\n"
    "ds_read_b64_tr_b16 a[170:171], v90 offset:10752\n"
    "v_cvt_pk_bf16_f32 v143, v254, v255\n"
    "ds_read_b64_tr_b16 a[104:105], v88 offset:25088\n"
    "ds_read_b64_tr_b16 a[106:107], v88 offset:27136\n"
    "buffer_store_dwordx4 v[140:143], v1, s[56:59], s72 offen offset:3072 sc1\n"
    "v_mfma_f32_32x32x16_bf16 v[208:223], a[108:111], v[52:55], v[208:223]\n"
    "ds_read_b64_tr_b16 a[172:173], v91 offset:8704\n"
    "ds_read_b64_tr_b16 a[174:175], v91 offset:10752\n"
    "ds_read_b64_tr_b16 a[108:109], v89 offset:25088\n"
    "v_mfma_f32_32x32x16_bf16 v[192:207], a[112:115], v[56:59], v[192:207]\n"
    "ds_read_b64_tr_b16 a[110:111], v89 offset:27136\n"
    "ds_read_b64_tr_b16 a[176:177], v90 offset:12288\n"
    "ds_read_b64_tr_b16 a[178:179], v90 offset:14336\n"
    "v_mfma_f32_32x32x16_bf16 v[208:223], a[116:119], v[56:59], v[208:223]\n"
    "ds_read_b64_tr_b16 a[112:113], v88 offset:28672\n"
    "ds_read_b64_tr_b16 a[114:115], v88 offset:30720\n"
    "ds_read_b64_tr_b16 a[180:181], v91 offset:12288\n"
    "v_mfma_f32_32x32x16_bf16 v[192:207], a[120:123], v[60:63], v[192:207]\n"
    "ds_read_b64_tr_b16 a[182:183], v91 offset:14336\n"
    "ds_read_b64_tr_b16 a[116:117], v89 offset:28672\n"
    "ds_read_b64_tr_b16 a[118:119], v89 offset:30720\n"
    "v_mfma_f32_32x32x16_bf16 v[208:223], a[124:127], v[60:63], v[208:223]\n"
    "ds_read_b64_tr_b16 a[184:185], v90 offset:12800\n"
    "ds_read_b64_tr_b16 a[186:187], v90 offset:14848\n"
    "ds_read_b64_tr_b16 a[120:121], v88 offset:29184\n"
    "s_waitcnt lgkmcnt(7)\n"
    "v_mfma_f32_32x32x16_bf16 v[192:207], a[128:131], v[96:99], v[192:207]\n"
    "ds_read_b64_tr_b16 a[122:123], v88 offset:31232\n"
    "ds_read_b64_tr_b16 a[188:189], v91 offset:12800\n"
    "ds_read_b64_tr_b16 a[190:191], v91 offset:14848\n"
    "v_mfma_f32_32x32x16_bf16 v[208:223], a[132:135], v[96:99], v[208:223]\n"
    "ds_read_b64_tr_b16 a[124:125], v89 offset:29184\n"
    "ds_read_b64_tr_b16 a[126:127], v89 offset:31232\n"
    "v_mfma_f32_32x32x16_bf16 v[192:207], a[136:139], v[100:103], v[192:207]\n"
    "v_mfma_f32_32x32x16_bf16 v[208:223], a[140:143], v[100:103], v[208:223]\n"
    "v_mfma_f32_32x32x16_bf16 v[192:207], a[144:147], v[104:107], v[192:207]\n"
    "v_mfma_f32_32x32x16_bf16 v[208:223], a[148:151], v[104:107], v[208:223]\n"
    "v_mfma_f32_32x32x16_bf16 v[192:207], a[152:155], v[108:111], v[192:207]\n"
    "v_mfma_f32_32x32x16_bf16 v[208:223], a[156:159], v[108:111], v[208:223]\n"
    "v_mfma_f32_32x32x16_bf16 v[192:207], a[160:163], v[112:115], v[192:207]\n"
    "v_mfma_f32_32x32x16_bf16 v[208:223], a[164:167], v[112:115], v[208:223]\n"
    "v_mfma_f32_32x32x16_bf16 v[192:207], a[168:171], v[116:119], v[192:207]\n"
    "v_mfma_f32_32x32x16_bf16 v[208:223], a[172:175], v[116:119], v[208:223]\n"
    "v_mfma_f32_32x32x16_bf16 v[192:207], a[176:179], v[120:123], v[192:207]\n"
    "s_waitcnt vmcnt(12) lgkmcnt(0)\n"
    "s_barrier\n"
    "ds_read_b64_tr_b16 a[128:129], v90 offset:16384\n"
    "ds_read_b64_tr_b16 a[130:131], v90 offset:18432\n"
    "v_mfma_f32_32x32x16_bf16 v[208:223], a[180:183], v[120:123], v[208:223]\n"
    "ds_read_b64_tr_b16 a[132:133], v91 offset:16384\n"
    "ds_read_b64_tr_b16 a[134:135], v91 offset:18432\n"
    "ds_read_b64_tr_b16 a[136:137], v90 offset:16896\n"
    "v_mfma_f32_32x32x16_bf16 v[192:207], a[184:187], v[124:127], v[192:207]\n"
    "ds_read_b64_tr_b16 a[138:139], v90 offset:18944\n"
    "ds_read_b64_tr_b16 a[140:141], v91 offset:16896\n"
    "ds_read_b64_tr_b16 a[142:143], v91 offset:18944\n"
    "v_mfma_f32_32x32x16_bf16 v[208:223], a[188:191], v[124:127], v[208:223]\n"
    "ds_read_b64_tr_b16 a[144:145], v90 offset:20480\n"
    "ds_read_b64_tr_b16 a[146:147], v90 offset:22528\n"
    "ds_read_b64_tr_b16 a[148:149], v91 offset:20480\n"
    "v_mfma_f32_32x32x16_bf16 v[224:239], a[64:67], v[32:35], 0\n"
    "ds_read_b64_tr_b16 a[150:151], v91 offset:22528\n"
    "ds_read_b64_tr_b16 a[64:65], v88 offset:32768\n"
    "ds_read_b64_tr_b16 a[66:67], v88 offset:34816\n"
    "v_mfma_f32_32x32x16_bf16 v[240:255], a[68:71], v[32:35], 0\n"
    "ds_read_b64_tr_b16 a[152:153], v90 offset:20992\n"
    "ds_read_b64_tr_b16 a[68:69], v89 offset:32768\n"
    "ds_read_b64_tr_b16 a[70:71], v89 offset:34816\n"
    "v_mfma_f32_32x32x16_bf16 v[224:239], a[72:75], v[36:39], v[224:239]\n"
    "ds_read_b64_tr_b16 a[154:155], v90 offset:23040\n"
    "ds_read_b64_tr_b16 a[72:73], v88 offset:33280\n"
    "ds_read_b64_tr_b16 a[74:75], v88 offset:35328\n"
    "v_mfma_f32_32x32x16_bf16 v[240:255], a[76:79], v[36:39], v[240:255]\n"
    "ds_read_b64_tr_b16 a[156:157], v91 offset:20992\n"
    "s_mov_b32 s72, 0x4000\n"
    "v_cvt_pk_bf16_f32 v128, v192, v193\n"
    "ds_read_b64_tr_b16 a[76:77], v89 offset:33280\n"
    "ds_read_b64_tr_b16 a[78:79], v89 offset:35328\n"
    "v_cvt_pk_bf16_f32 v129, v194, v195\n"
    "v_cvt_pk_bf16_f32 v130, v196, v197\n"
    "v_mfma_f32_32x32x16_bf16 v[224:239], a[80:83], v[40:43], v[224:239]\n"
    "ds_read_b64_tr_b16 a[158:159], v91 offset:23040\n"
    "v_cvt_pk_bf16_f32 v131, v198, v199\n"
    "ds_read_b64_tr_b16 a[80:81], v88 offset:36864\n"
    "ds_read_b64_tr_b16 a[82:83], v88 offset:38912\n"
    "buffer_store_dwordx4 v[128:131], v1, s[56:59], s72 offen offset:0 sc1\n"
    "v_mfma_f32_32x32x16_bf16 v[240:255], a[84:87], v[40:43], v[240:255]\n"
    "ds_read_b64_tr_b16 a[160:161], v90 offset:24576\n"
    "v_cvt_pk_bf16_f32 v132, v200, v201\n"
    "ds_read_b64_tr_b16 a[84:85], v89 offset:36864\n"
    "ds_read_b64_tr_b16 a[86:87], v89 offset:38912\n"
    "v_cvt_pk_bf16_f32 v133, v202, v203\n"
    "v_cvt_pk_bf16_f32 v134, v204, v205\n"
    "v_mfma_f32_32x32x16_bf16 v[224:239], a[88:91], v[44:47], v[224:239]\n"
    "ds_read_b64_tr_b16 a[162:163], v90 offset:26624\n"
    "v_cvt_pk_bf16_f32 v135, v206, v207\n"
    "ds_read_b64_tr_b16 a[88:89], v88 offset:37376\n"
    "ds_read_b64_tr_b16 a[90:91], v88 offset:39424\n"
    "buffer_store_dwordx4 v[132:135], v1, s[56:59], s72 offen offset:1024 sc1\n"
    "v_mfma_f32_32x32x16_bf16 v[240:255], a[92:95], v[44:47], v[240:255]\n"
    "ds_read_b64_tr_b16 a[164:165], v91 offset:24576\n"
    "v_cvt_pk_bf16_f32 v136, v208, v209\n"
    "ds_read_b64_tr_b16 a[92:93], v89 offset:37376\n"
    "ds_read_b64_tr_b16 a[94:95], v89 offset:39424\n"
    "v_cvt_pk_bf16_f32 v137, v210, v211\n"
    "v_cvt_pk_bf16_f32 v138, v212, v213\n"
    "v_mfma_f32_32x32x16_bf16 v[224:239], a[96:99], v[48:51], v[224:239]\n"
    "ds_read_b64_tr_b16 a[166:167], v91 offset:26624\n"
    "v_cvt_pk_bf16_f32 v139, v214, v215\n"
    "ds_read_b64_tr_b16 a[96:97], v88 offset:40960\n"
    "ds_read_b64_tr_b16 a[98:99], v88 offset:43008\n"
    "buffer_store_dwordx4 v[136:139], v1, s[56:59], s72 offen offset:2048 sc1\n"
    "v_mfma_f32_32x32x16_bf16 v[240:255], a[100:103], v[48:51], v[240:255]\n"
    "ds_read_b64_tr_b16 a[168:169], v90 offset:25088\n"
    "v_cvt_pk_bf16_f32 v140, v216, v217\n"
    "ds_read_b64_tr_b16 a[100:101], v89 offset:40960\n"
    "ds_read_b64_tr_b16 a[102:103], v89 offset:43008\n"
    "v_cvt_pk_bf16_f32 v141, v218, v219\n"
    "v_cvt_pk_bf16_f32 v142, v220, v221\n"
    "v_mfma_f32_32x32x16_bf16 v[224:239], a[104:107], v[52:55], v[224:239]\n"
    "ds_read_b64_tr_b16 a[170:171], v90 offset:27136\n"
    "v_cvt_pk_bf16_f32 v143, v222, v223\n"
    "ds_read_b64_tr_b16 a[104:105], v88 offset:41472\n"
    "ds_read_b64_tr_b16 a[106:107], v88 offset:43520\n"
    "buffer_store_dwordx4 v[140:143], v1, s[56:59], s72 offen offset:3072 sc1\n"
    "v_mfma_f32_32x32x16_bf16 v[240:255], a[108:111], v[52:55], v[240:255]\n"
    "ds_read_b64_tr_b16 a[172:173], v91 offset:25088\n"
    "ds_read_b64_tr_b16 a[174:175], v91 offset:27136\n"
    "ds_read_b64_tr_b16 a[108:109], v89 offset:41472\n"
    "v_mfma_f32_32x32x16_bf16 v[224:239], a[112:115], v[56:59], v[224:239]\n"
    "ds_read_b64_tr_b16 a[110:111], v89 offset:43520\n"
    "ds_read_b64_tr_b16 a[176:177], v90 offset:28672\n"
    "ds_read_b64_tr_b16 a[178:179], v90 offset:30720\n"
    "v_mfma_f32_32x32x16_bf16 v[240:255], a[116:119], v[56:59], v[240:255]\n"
    "ds_read_b64_tr_b16 a[112:113], v88 offset:45056\n"
    "ds_read_b64_tr_b16 a[114:115], v88 offset:47104\n"
    "ds_read_b64_tr_b16 a[180:181], v91 offset:28672\n"
    "v_mfma_f32_32x32x16_bf16 v[224:239], a[120:123], v[60:63], v[224:239]\n"
    "ds_read_b64_tr_b16 a[182:183], v91 offset:30720\n"
    "ds_read_b64_tr_b16 a[116:117], v89 offset:45056\n"
    "ds_read_b64_tr_b16 a[118:119], v89 offset:47104\n"
    "v_mfma_f32_32x32x16_bf16 v[240:255], a[124:127], v[60:63], v[240:255]\n"
    "ds_read_b64_tr_b16 a[184:185], v90 offset:29184\n"
    "ds_read_b64_tr_b16 a[186:187], v90 offset:31232\n"
    "ds_read_b64_tr_b16 a[120:121], v88 offset:45568\n"
    "s_waitcnt lgkmcnt(7)\n"
    "v_mfma_f32_32x32x16_bf16 v[224:239], a[128:131], v[96:99], v[224:239]\n"
    "ds_read_b64_tr_b16 a[122:123], v88 offset:47616\n"
    "ds_read_b64_tr_b16 a[188:189], v91 offset:29184\n"
    "ds_read_b64_tr_b16 a[190:191], v91 offset:31232\n"
    "v_mfma_f32_32x32x16_bf16 v[240:255], a[132:135], v[96:99], v[240:255]\n"
    "ds_read_b64_tr_b16 a[124:125], v89 offset:45568\n"
    "ds_read_b64_tr_b16 a[126:127], v89 offset:47616\n"
    "v_mfma_f32_32x32x16_bf16 v[224:239], a[136:139], v[100:103], v[224:239]\n"
    "v_mfma_f32_32x32x16_bf16 v[240:255], a[140:143], v[100:103], v[240:255]\n"
    "v_mfma_f32_32x32x16_bf16 v[224:239], a[144:147], v[104:107], v[224:239]\n"
    "v_mfma_f32_32x32x16_bf16 v[240:255], a[148:151], v[104:107], v[240:255]\n"
    "v_mfma_f32_32x32x16_bf16 v[224:239], a[152:155], v[108:111], v[224:239]\n"
    "v_mfma_f32_32x32x16_bf16 v[240:255], a[156:159], v[108:111], v[240:255]\n"
    "v_mfma_f32_32x32x16_bf16 v[224:239], a[160:163], v[112:115], v[224:239]\n"
    "v_mfma_f32_32x32x16_bf16 v[240:255], a[164:167], v[112:115], v[240:255]\n"
    "v_mfma_f32_32x32x16_bf16 v[224:239], a[168:171], v[116:119], v[224:239]\n"
    "v_mfma_f32_32x32x16_bf16 v[240:255], a[172:175], v[116:119], v[240:255]\n"
    "v_mfma_f32_32x32x16_bf16 v[224:239], a[176:179], v[120:123], v[224:239]\n"
    "s_waitcnt vmcnt(9) lgkmcnt(0)\n"
    "s_barrier\n"
    "ds_read_b64_tr_b16 a[128:129], v90 offset:32768\n"
    "ds_read_b64_tr_b16 a[130:131], v90 offset:34816\n"
    "v_mfma_f32_32x32x16_bf16 v[240:255], a[180:183], v[120:123], v[240:255]\n"
    "ds_read_b64_tr_b16 a[132:133], v91 offset:32768\n"
    "ds_read_b64_tr_b16 a[134:135], v91 offset:34816\n"
    "ds_read_b64_tr_b16 a[136:137], v90 offset:33280\n"
    "v_mfma_f32_32x32x16_bf16 v[224:239], a[184:187], v[124:127], v[224:239]\n"
    "ds_read_b64_tr_b16 a[138:139], v90 offset:35328\n"
    "ds_read_b64_tr_b16 a[140:141], v91 offset:33280\n"
    "ds_read_b64_tr_b16 a[142:143], v91 offset:35328\n"
    "v_mfma_f32_32x32x16_bf16 v[240:255], a[188:191], v[124:127], v[240:255]\n"
    "ds_read_b64_tr_b16 a[144:145], v90 offset:36864\n"
    "ds_read_b64_tr_b16 a[146:147], v90 offset:38912\n"
    "ds_read_b64_tr_b16 a[148:149], v91 offset:36864\n"
    "v_mfma_f32_32x32x16_bf16 v[192:207], a[64:67], v[32:35], 0\n"
    "ds_read_b64_tr_b16 a[150:151], v91 offset:38912\n"
    "ds_read_b64_tr_b16 a[152:153], v90 offset:37376\n"
    "ds_read_b64_tr_b16 a[154:155], v90 offset:39424\n"
    "v_mfma_f32_32x32x16_bf16 v[208:223], a[68:71], v[32:35], 0\n"
    "ds_read_b64_tr_b16 a[156:157], v91 offset:37376\n"
    "ds_read_b64_tr_b16 a[158:159], v91 offset:39424\n"
    "ds_read_b64_tr_b16 a[160:161], v90 offset:40960\n"
    "v_mfma_f32_32x32x16_bf16 v[192:207], a[72:75], v[36:39], v[192:207]\n"
    "ds_read_b64_tr_b16 a[162:163], v90 offset:43008\n"
    "ds_read_b64_tr_b16 a[164:165], v91 offset:40960\n"
    "ds_read_b64_tr_b16 a[166:167], v91 offset:43008\n"
    "v_mfma_f32_32x32x16_bf16 v[208:223], a[76:79], v[36:39], v[208:223]\n"
    "ds_read_b64_tr_b16 a[168:169], v90 offset:41472\n"
    "s_mov_b32 s72, 0x5000\n"
    "v_cvt_pk_bf16_f32 v128, v224, v225\n"
    "ds_read_b64_tr_b16 a[170:171], v90 offset:43520\n"
    "ds_read_b64_tr_b16 a[172:173], v91 offset:41472\n"
    "v_cvt_pk_bf16_f32 v129, v226, v227\n"
    "v_cvt_pk_bf16_f32 v130, v228, v229\n"
    "v_mfma_f32_32x32x16_bf16 v[192:207], a[80:83], v[40:43], v[192:207]\n"
    "ds_read_b64_tr_b16 a[174:175], v91 offset:43520\n"
    "v_cvt_pk_bf16_f32 v131, v230, v231\n"
    "ds_read_b64_tr_b16 a[176:177], v90 offset:45056\n"
    "ds_read_b64_tr_b16 a[178:179], v90 offset:47104\n"
    "buffer_store_dwordx4 v[128:131], v1, s[56:59], s72 offen offset:0 sc1\n"
    "v_mfma_f32_32x32x16_bf16 v[208:223], a[84:87], v[40:43], v[208:223]\n"
    "ds_read_b64_tr_b16 a[180:181], v91 offset:45056\n"
    "v_cvt_pk_bf16_f32 v132, v232, v233\n"
    "ds_read_b64_tr_b16 a[182:183], v91 offset:47104\n"
    "ds_read_b64_tr_b16 a[184:185], v90 offset:45568\n"
    "v_cvt_pk_bf16_f32 v133, v234, v235\n"
    "v_cvt_pk_bf16_f32 v134, v236, v237\n"
    "v_mfma_f32_32x32x16_bf16 v[192:207], a[88:91], v[44:47], v[192:207]\n"
    "ds_read_b64_tr_b16 a[186:187], v90 offset:47616\n"
    "v_cvt_pk_bf16_f32 v135, v238, v239\n"
    "ds_read_b64_tr_b16 a[188:189], v91 offset:45568\n"
    "ds_read_b64_tr_b16 a[190:191], v91 offset:47616\n"
    "buffer_store_dwordx4 v[132:135], v1, s[56:59], s72 offen offset:1024 sc1\n"
    "v_mfma_f32_32x32x16_bf16 v[208:223], a[92:95], v[44:47], v[208:223]\n"
    "v_cvt_pk_bf16_f32 v136, v240, v241\n"
    "v_cvt_pk_bf16_f32 v137, v242, v243\n"
    "v_cvt_pk_bf16_f32 v138, v244, v245\n"
    "v_cvt_pk_bf16_f32 v139, v246, v247\n"
    "buffer_store_dwordx4 v[136:139], v1, s[56:59], s72 offen offset:2048 sc1\n"
    "v_mfma_f32_32x32x16_bf16 v[192:207], a[96:99], v[48:51], v[192:207]\n"
    "v_cvt_pk_bf16_f32 v140, v248, v249\n"
    "v_cvt_pk_bf16_f32 v141, v250, v251\n"
    "v_cvt_pk_bf16_f32 v142, v252, v253\n"
    "v_cvt_pk_bf16_f32 v143, v254, v255\n"
    "buffer_store_dwordx4 v[140:143], v1, s[56:59], s72 offen offset:3072 sc1\n"
    "v_mfma_f32_32x32x16_bf16 v[208:223], a[100:103], v[48:51], v[208:223]\n"
    "v_mfma_f32_32x32x16_bf16 v[192:207], a[104:107], v[52:55], v[192:207]\n"
    "v_mfma_f32_32x32x16_bf16 v[208:223], a[108:111], v[52:55], v[208:223]\n"
    "v_mfma_f32_32x32x16_bf16 v[192:207], a[112:115], v[56:59], v[192:207]\n"
    "v_mfma_f32_32x32x16_bf16 v[208:223], a[116:119], v[56:59], v[208:223]\n"
    "v_mfma_f32_32x32x16_bf16 v[192:207], a[120:123], v[60:63], v[192:207]\n"
    "v_mfma_f32_32x32x16_bf16 v[208:223], a[124:127], v[60:63], v[208:223]\n"
    "s_waitcnt lgkmcnt(0)\n"
    "v_mfma_f32_32x32x16_bf16 v[192:207], a[128:131], v[96:99], v[192:207]\n"
    "v_mfma_f32_32x32x16_bf16 v[208:223], a[132:135], v[96:99], v[208:223]\n"
    "v_mfma_f32_32x32x16_bf16 v[192:207], a[136:139], v[100:103], v[192:207]\n"
    "v_mfma_f32_32x32x16_bf16 v[208:223], a[140:143], v[100:103], v[208:223]\n"
    "v_mfma_f32_32x32x16_bf16 v[192:207], a[144:147], v[104:107], v[192:207]\n"
    "v_mfma_f32_32x32x16_bf16 v[208:223], a[148:151], v[104:107], v[208:223]\n"
    "v_mfma_f32_32x32x16_bf16 v[192:207], a[152:155], v[108:111], v[192:207]\n"
    "v_mfma_f32_32x32x16_bf16 v[208:223], a[156:159], v[108:111], v[208:223]\n"
    "v_mfma_f32_32x32x16_bf16 v[192:207], a[160:163], v[112:115], v[192:207]\n"
    "v_mfma_f32_32x32x16_bf16 v[208:223], a[164:167], v[112:115], v[208:223]\n"
    "v_mfma_f32_32x32x16_bf16 v[192:207], a[168:171], v[116:119], v[192:207]\n"
    "v_mfma_f32_32x32x16_bf16 v[208:223], a[172:175], v[116:119], v[208:223]\n"
    "v_mfma_f32_32x32x16_bf16 v[192:207], a[176:179], v[120:123], v[192:207]\n"
    "v_mfma_f32_32x32x16_bf16 v[208:223], a[180:183], v[120:123], v[208:223]\n"
    "v_mfma_f32_32x32x16_bf16 v[192:207], a[184:187], v[124:127], v[192:207]\n"
    "v_mfma_f32_32x32x16_bf16 v[208:223], a[188:191], v[124:127], v[208:223]\n"
    "s_nop 11\n"
    "s_mov_b32 s72, 0x6000\n"
    "v_cvt_pk_bf16_f32 v128, v192, v193\n"
    "v_cvt_pk_bf16_f32 v129, v194, v195\n"
    "v_cvt_pk_bf16_f32 v130, v196, v197\n"
    "v_cvt_pk_bf16_f32 v131, v198, v199\n"
    "buffer_store_dwordx4 v[128:131], v1, s[56:59], s72 offen offset:0 sc1\n"
    "v_cvt_pk_bf16_f32 v132, v200, v201\n"
    "v_cvt_pk_bf16_f32 v133, v202, v203\n"
    "v_cvt_pk_bf16_f32 v134, v204, v205\n"
    "v_cvt_pk_bf16_f32 v135, v206, v207\n"
    "buffer_store_dwordx4 v[132:135], v1, s[56:59], s72 offen offset:1024 sc1\n"
    "v_cvt_pk_bf16_f32 v136, v208, v209\n"
    "v_cvt_pk_bf16_f32 v137, v210, v211\n"
    "v_cvt_pk_bf16_f32 v138, v212, v213\n"
    "v_cvt_pk_bf16_f32 v139, v214, v215\n"
    "buffer_store_dwordx4 v[136:139], v1, s[56:59], s72 offen offset:2048 sc1\n"
    "v_cvt_pk_bf16_f32 v140, v216, v217\n"
    "v_cvt_pk_bf16_f32 v141, v218, v219\n"
    "v_cvt_pk_bf16_f32 v142, v220, v221\n"
    "v_cvt_pk_bf16_f32 v143, v222, v223\n"
    "buffer_store_dwordx4 v[140:143], v1, s[56:59], s72 offen offset:3072 sc1\n"
    "s_waitcnt vmcnt(0)\n"
    "s_branch LEND_%=\n"
    "LR8_%=:\n"
    "s_nop 8\n"
    "v_max3_f32 v82, v48, v49, v50\n"
    "v_max3_f32 v83, v56, v57, v58\n"
    "v_max3_f32 v82, v82, v51, v52\n"
    "v_max3_f32 v83, v83, v59, v60\n"
    "v_max3_f32 v82, v82, v53, v54\n"
    "v_max3_f32 v83, v83, v61, v62\n"
    "v_max_f32 v82, v82, v55\n"
    "v_max_f32 v83, v83, v63\n"
    "s_nop 1\n"
    "v_mov_b32 v92, v82\n"
    "v_mov_b32 v93, v83\n"
    "s_nop 1\n"
    "v_permlane16_swap_b32 v82, v92\n"
    "v_permlane16_swap_b32 v83, v93\n"
    "s_nop 1\n"
    "v_max_f32 v82, v82, v92\n"
    "v_max_f32 v83, v83, v93\n"
    "v_mov_b32 v92, v82\n"
    "v_mov_b32 v93, v83\n"
    "s_nop 1\n"
    "v_permlane32_swap_b32 v82, v92\n"
    "v_permlane32_swap_b32 v83, v93\n"
    "s_nop 1\n"
    "v_max_f32 v92, v82, v92\n"
    "v_max_f32 v93, v83, v93\n"
    "v_add_f32 v92, 0x42c80000, v92\n"
    "v_ceil_f32 v92, v92\n"
    "v_max_f32 v92, 0, v92\n"
    "v_add_f32 v93, 0x42c80000, v93\n"
    "v_ceil_f32 v93, v93\n"
    "v_max_f32 v93, 0, v93\n"
    "v_sub_f32 v48, v48, v92\n"
    "v_sub_f32 v49, v49, v92\n"
    "v_sub_f32 v50, v50, v92\n"
    "v_sub_f32 v51, v51, v92\n"
    "v_sub_f32 v52, v52, v92\n"
    "v_sub_f32 v53, v53, v92\n"
    "v_sub_f32 v54, v54, v92\n"
    "v_sub_f32 v55, v55, v92\n"
    "v_sub_f32 v56, v56, v93\n"
    "v_sub_f32 v57, v57, v93\n"
    "v_sub_f32 v58, v58, v93\n"
    "v_sub_f32 v59, v59, v93\n"
    "v_sub_f32 v60, v60, v93\n"
    "v_sub_f32 v61, v61, v93\n"
    "v_sub_f32 v62, v62, v93\n"
    "v_sub_f32 v63, v63, v93\n"
    "v_sub_f32 v64, v64, v92\n"
    "v_sub_f32 v65, v65, v92\n"
    "v_sub_f32 v66, v66, v92\n"
    "v_sub_f32 v67, v67, v92\n"
    "v_sub_f32 v68, v68, v93\n"
    "v_sub_f32 v69, v69, v93\n"
    "v_sub_f32 v70, v70, v93\n"
    "v_sub_f32 v71, v71, v93\n"
    "v_sub_f32 v94, 0, v92\n"
    "v_sub_f32 v95, 0, v93\n"
    "v_cvt_i32_f32 v94, v94\n"
    "v_cvt_i32_f32 v95, v95\n"
    "v_ldexp_f32 v80, v80, v94\n"
    "v_ldexp_f32 v81, v81, v95\n"
    "v_ldexp_f32 v128, v128, v94\n"
    "v_ldexp_f32 v129, v129, v94\n"
    "v_ldexp_f32 v130, v130, v94\n"
    "v_ldexp_f32 v131, v131, v94\n"
    "v_ldexp_f32 v132, v132, v95\n"
    "v_ldexp_f32 v133, v133, v95\n"
    "v_ldexp_f32 v134, v134, v95\n"
    "v_ldexp_f32 v135, v135, v95\n"
    "v_ldexp_f32 v136, v136, v94\n"
    "v_ldexp_f32 v137, v137, v94\n"
    "v_ldexp_f32 v138, v138, v94\n"
    "v_ldexp_f32 v139, v139, v94\n"
    "v_ldexp_f32 v140, v140, v95\n"
    "v_ldexp_f32 v141, v141, v95\n"
    "v_ldexp_f32 v142, v142, v95\n"
    "v_ldexp_f32 v143, v143, v95\n"
    "v_ldexp_f32 v144, v144, v94\n"
    "v_ldexp_f32 v145, v145, v94\n"
    "v_ldexp_f32 v146, v146, v94\n"
    "v_ldexp_f32 v147, v147, v94\n"
    "v_ldexp_f32 v148, v148, v95\n"
    "v_ldexp_f32 v149, v149, v95\n"
    "v_ldexp_f32 v150, v150, v95\n"
    "v_ldexp_f32 v151, v151, v95\n"
    "v_ldexp_f32 v152, v152, v94\n"
    "v_ldexp_f32 v153, v153, v94\n"
    "v_ldexp_f32 v154, v154, v94\n"
    "v_ldexp_f32 v155, v155, v94\n"
    "v_ldexp_f32 v156, v156, v95\n"
    "v_ldexp_f32 v157, v157, v95\n"
    "v_ldexp_f32 v158, v158, v95\n"
    "v_ldexp_f32 v159, v159, v95\n"
    "v_ldexp_f32 v160, v160, v94\n"
    "v_ldexp_f32 v161, v161, v94\n"
    "v_ldexp_f32 v162, v162, v94\n"
    "v_ldexp_f32 v163, v163, v94\n"
    "v_ldexp_f32 v164, v164, v95\n"
    "v_ldexp_f32 v165, v165, v95\n"
    "v_ldexp_f32 v166, v166, v95\n"
    "v_ldexp_f32 v167, v167, v95\n"
    "v_ldexp_f32 v168, v168, v94\n"
    "v_ldexp_f32 v169, v169, v94\n"
    "v_ldexp_f32 v170, v170, v94\n"
    "v_ldexp_f32 v171, v171, v94\n"
    "v_ldexp_f32 v172, v172, v95\n"
    "v_ldexp_f32 v173, v173, v95\n"
    "v_ldexp_f32 v174, v174, v95\n"
    "v_ldexp_f32 v175, v175, v95\n"
    "v_ldexp_f32 v176, v176, v94\n"
    "v_ldexp_f32 v177, v177, v94\n"
    "v_ldexp_f32 v178, v178, v94\n"
    "v_ldexp_f32 v179, v179, v94\n"
    "v_ldexp_f32 v180, v180, v95\n"
    "v_ldexp_f32 v181, v181, v95\n"
    "v_ldexp_f32 v182, v182, v95\n"
    "v_ldexp_f32 v183, v183, v95\n"
    "v_ldexp_f32 v184, v184, v94\n"
    "v_ldexp_f32 v185, v185, v94\n"
    "v_ldexp_f32 v186, v186, v94\n"
    "v_ldexp_f32 v187, v187, v94\n"
    "v_ldexp_f32 v188, v188, v95\n"
    "v_ldexp_f32 v189, v189, v95\n"
    "v_ldexp_f32 v190, v190, v95\n"
    "v_ldexp_f32 v191, v191, v95\n"
    "v_ldexp_f32 v192, v192, v94\n"
    "v_ldexp_f32 v193, v193, v94\n"
    "v_ldexp_f32 v194, v194, v94\n"
    "v_ldexp_f32 v195, v195, v94\n"
    "v_ldexp_f32 v196, v196, v95\n"
    "v_ldexp_f32 v197, v197, v95\n"
    "v_ldexp_f32 v198, v198, v95\n"
    "v_ldexp_f32 v199, v199, v95\n"
    "v_ldexp_f32 v200, v200, v94\n"
    "v_ldexp_f32 v201, v201, v94\n"
    "v_ldexp_f32 v202, v202, v94\n"
    "v_ldexp_f32 v203, v203, v94\n"
    "v_ldexp_f32 v204, v204, v95\n"
    "v_ldexp_f32 v205, v205, v95\n"
    "v_ldexp_f32 v206, v206, v95\n"
    "v_ldexp_f32 v207, v207, v95\n"
    "v_ldexp_f32 v208, v208, v94\n"
    "v_ldexp_f32 v209, v209, v94\n"
    "v_ldexp_f32 v210, v210, v94\n"
    "v_ldexp_f32 v211, v211, v94\n"
    "v_ldexp_f32 v212, v212, v95\n"
    "v_ldexp_f32 v213, v213, v95\n"
    "v_ldexp_f32 v214, v214, v95\n"
    "v_ldexp_f32 v215, v215, v95\n"
    "v_ldexp_f32 v216, v216, v94\n"
    "v_ldexp_f32 v217, v217, v94\n"
    "v_ldexp_f32 v218, v218, v94\n"
    "v_ldexp_f32 v219, v219, v94\n"
    "v_ldexp_f32 v220, v220, v95\n"
    "v_ldexp_f32 v221, v221, v95\n"
    "v_ldexp_f32 v222, v222, v95\n"
    "v_ldexp_f32 v223, v223, v95\n"
    "v_ldexp_f32 v224, v224, v94\n"
    "v_ldexp_f32 v225, v225, v94\n"
    "v_ldexp_f32 v226, v226, v94\n"
    "v_ldexp_f32 v227, v227, v94\n"
    "v_ldexp_f32 v228, v228, v95\n"
    "v_ldexp_f32 v229, v229, v95\n"
    "v_ldexp_f32 v230, v230, v95\n"
    "v_ldexp_f32 v231, v231, v95\n"
    "v_ldexp_f32 v232, v232, v94\n"
    "v_ldexp_f32 v233, v233, v94\n"
    "v_ldexp_f32 v234, v234, v94\n"
    "v_ldexp_f32 v235, v235, v94\n"
    "v_ldexp_f32 v236, v236, v95\n"
    "v_ldexp_f32 v237, v237, v95\n"
    "v_ldexp_f32 v238, v238, v95\n"
    "v_ldexp_f32 v239, v239, v95\n"
    "v_ldexp_f32 v240, v240, v94\n"
    "v_ldexp_f32 v241, v241, v94\n"
    "v_ldexp_f32 v242, v242, v94\n"
    "v_ldexp_f32 v243, v243, v94\n"
    "v_ldexp_f32 v244, v244, v95\n"
    "v_ldexp_f32 v245, v245, v95\n"
    "v_ldexp_f32 v246, v246, v95\n"
    "v_ldexp_f32 v247, v247, v95\n"
    "v_ldexp_f32 v248, v248, v94\n"
    "v_ldexp_f32 v249, v249, v94\n"
    "v_ldexp_f32 v250, v250, v94\n"
    "v_ldexp_f32 v251, v251, v94\n"
    "v_ldexp_f32 v252, v252, v95\n"
    "v_ldexp_f32 v253, v253, v95\n"
    "v_ldexp_f32 v254, v254, v95\n"
    "v_ldexp_f32 v255, v255, v95\n"
    "s_nop 1\n"
    "s_branch LB8_%=\n"
    "LR9_%=:\n"
    "s_nop 8\n"
    "v_max3_f32 v82, v32, v33, v34\n"
    "v_max3_f32 v83, v40, v41, v42\n"
    "v_max3_f32 v82, v82, v35, v36\n"
    "v_max3_f32 v83, v83, v43, v44\n"
    "v_max3_f32 v82, v82, v37, v38\n"
    "v_max3_f32 v83, v83, v45, v46\n"
    "v_max_f32 v82, v82, v39\n"
    "v_max_f32 v83, v83, v47\n"
    "s_nop 1\n"
    "v_mov_b32 v92, v82\n"
    "v_mov_b32 v93, v83\n"
    "s_nop 1\n"
    "v_permlane16_swap_b32 v82, v92\n"
    "v_permlane16_swap_b32 v83, v93\n"
    "s_nop 1\n"
    "v_max_f32 v82, v82, v92\n"
    "v_max_f32 v83, v83, v93\n"
    "v_mov_b32 v92, v82\n"
    "v_mov_b32 v93, v83\n"
    "s_nop 1\n"
    "v_permlane32_swap_b32 v82, v92\n"
    "v_permlane32_swap_b32 v83, v93\n"
    "s_nop 1\n"
    "v_max_f32 v92, v82, v92\n"
    "v_max_f32 v93, v83, v93\n"
    "v_add_f32 v92, 0x42c80000, v92\n"
    "v_ceil_f32 v92, v92\n"
    "v_max_f32 v92, 0, v92\n"
    "v_add_f32 v93, 0x42c80000, v93\n"
    "v_ceil_f32 v93, v93\n"
    "v_max_f32 v93, 0, v93\n"
    "v_sub_f32 v32, v32, v92\n"
    "v_sub_f32 v33, v33, v92\n"
    "v_sub_f32 v34, v34, v92\n"
    "v_sub_f32 v35, v35, v92\n"
    "v_sub_f32 v36, v36, v92\n"
    "v_sub_f32 v37, v37, v92\n"
    "v_sub_f32 v38, v38, v92\n"
    "v_sub_f32 v39, v39, v92\n"
    "v_sub_f32 v40, v40, v93\n"
    "v_sub_f32 v41, v41, v93\n"
    "v_sub_f32 v42, v42, v93\n"
    "v_sub_f32 v43, v43, v93\n"
    "v_sub_f32 v44, v44, v93\n"
    "v_sub_f32 v45, v45, v93\n"
    "v_sub_f32 v46, v46, v93\n"
    "v_sub_f32 v47, v47, v93\n"
    "v_sub_f32 v64, v64, v92\n"
    "v_sub_f32 v65, v65, v92\n"
    "v_sub_f32 v66, v66, v92\n"
    "v_sub_f32 v67, v67, v92\n"
    "v_sub_f32 v68, v68, v93\n"
    "v_sub_f32 v69, v69, v93\n"
    "v_sub_f32 v70, v70, v93\n"
    "v_sub_f32 v71, v71, v93\n"
    "v_sub_f32 v94, 0, v92\n"
    "v_sub_f32 v95, 0, v93\n"
    "v_cvt_i32_f32 v94, v94\n"
    "v_cvt_i32_f32 v95, v95\n"
    "v_ldexp_f32 v80, v80, v94\n"
    "v_ldexp_f32 v81, v81, v95\n"
    "v_ldexp_f32 v128, v128, v94\n"
    "v_ldexp_f32 v129, v129, v94\n"
    "v_ldexp_f32 v130, v130, v94\n"
    "v_ldexp_f32 v131, v131, v94\n"
    "v_ldexp_f32 v132, v132, v95\n"
    "v_ldexp_f32 v133, v133, v95\n"
    "v_ldexp_f32 v134, v134, v95\n"
    "v_ldexp_f32 v135, v135, v95\n"
    "v_ldexp_f32 v136, v136, v94\n"
    "v_ldexp_f32 v137, v137, v94\n"
    "v_ldexp_f32 v138, v138, v94\n"
    "v_ldexp_f32 v139, v139, v94\n"
    "v_ldexp_f32 v140, v140, v95\n"
    "v_ldexp_f32 v141, v141, v95\n"
    "v_ldexp_f32 v142, v142, v95\n"
    "v_ldexp_f32 v143, v143, v95\n"
    "v_ldexp_f32 v144, v144, v94\n"
    "v_ldexp_f32 v145, v145, v94\n"
    "v_ldexp_f32 v146, v146, v94\n"
    "v_ldexp_f32 v147, v147, v94\n"
    "v_ldexp_f32 v148, v148, v95\n"
    "v_ldexp_f32 v149, v149, v95\n"
    "v_ldexp_f32 v150, v150, v95\n"
    "v_ldexp_f32 v151, v151, v95\n"
    "v_ldexp_f32 v152, v152, v94\n"
    "v_ldexp_f32 v153, v153, v94\n"
    "v_ldexp_f32 v154, v154, v94\n"
    "v_ldexp_f32 v155, v155, v94\n"
    "v_ldexp_f32 v156, v156, v95\n"
    "v_ldexp_f32 v157, v157, v95\n"
    "v_ldexp_f32 v158, v158, v95\n"
    "v_ldexp_f32 v159, v159, v95\n"
    "v_ldexp_f32 v160, v160, v94\n"
    "v_ldexp_f32 v161, v161, v94\n"
    "v_ldexp_f32 v162, v162, v94\n"
    "v_ldexp_f32 v163, v163, v94\n"
    "v_ldexp_f32 v164, v164, v95\n"
    "v_ldexp_f32 v165, v165, v95\n"
    "v_ldexp_f32 v166, v166, v95\n"
    "v_ldexp_f32 v167, v167, v95\n"
    "v_ldexp_f32 v168, v168, v94\n"
    "v_ldexp_f32 v169, v169, v94\n"
    "v_ldexp_f32 v170, v170, v94\n"
    "v_ldexp_f32 v171, v171, v94\n"
    "v_ldexp_f32 v172, v172, v95\n"
    "v_ldexp_f32 v173, v173, v95\n"
    "v_ldexp_f32 v174, v174, v95\n"
    "v_ldexp_f32 v175, v175, v95\n"
    "v_ldexp_f32 v176, v176, v94\n"
    "v_ldexp_f32 v177, v177, v94\n"
    "v_ldexp_f32 v178, v178, v94\n"
    "v_ldexp_f32 v179, v179, v94\n"
    "v_ldexp_f32 v180, v180, v95\n"
    "v_ldexp_f32 v181, v181, v95\n"
    "v_ldexp_f32 v182, v182, v95\n"
    "v_ldexp_f32 v183, v183, v95\n"
    "v_ldexp_f32 v184, v184, v94\n"
    "v_ldexp_f32 v185, v185, v94\n"
    "v_ldexp_f32 v186, v186, v94\n"
    "v_ldexp_f32 v187, v187, v94\n"
    "v_ldexp_f32 v188, v188, v95\n"
    "v_ldexp_f32 v189, v189, v95\n"
    "v_ldexp_f32 v190, v190, v95\n"
    "v_ldexp_f32 v191, v191, v95\n"
    "v_ldexp_f32 v192, v192, v94\n"
    "v_ldexp_f32 v193, v193, v94\n"
    "v_ldexp_f32 v194, v194, v94\n"
    "v_ldexp_f32 v195, v195, v94\n"
    "v_ldexp_f32 v196, v196, v95\n"
    "v_ldexp_f32 v197, v197, v95\n"
    "v_ldexp_f32 v198, v198, v95\n"
    "v_ldexp_f32 v199, v199, v95\n"
    "v_ldexp_f32 v200, v200, v94\n"
    "v_ldexp_f32 v201, v201, v94\n"
    "v_ldexp_f32 v202, v202, v94\n"
    "v_ldexp_f32 v203, v203, v94\n"
    "v_ldexp_f32 v204, v204, v95\n"
    "v_ldexp_f32 v205, v205, v95\n"
    "v_ldexp_f32 v206, v206, v95\n"
    "v_ldexp_f32 v207, v207, v95\n"
    "v_ldexp_f32 v208, v208, v94\n"
    "v_ldexp_f32 v209, v209, v94\n"
    "v_ldexp_f32 v210, v210, v94\n"
    "v_ldexp_f32 v211, v211, v94\n"
    "v_ldexp_f32 v212, v212, v95\n"
    "v_ldexp_f32 v213, v213, v95\n"
    "v_ldexp_f32 v214, v214, v95\n"
    "v_ldexp_f32 v215, v215, v95\n"
    "v_ldexp_f32 v216, v216, v94\n"
    "v_ldexp_f32 v217, v217, v94\n"
    "v_ldexp_f32 v218, v218, v94\n"
    "v_ldexp_f32 v219, v219, v94\n"
    "v_ldexp_f32 v220, v220, v95\n"
    "v_ldexp_f32 v221, v221, v95\n"
    "v_ldexp_f32 v222, v222, v95\n"
    "v_ldexp_f32 v223, v223, v95\n"
    "v_ldexp_f32 v224, v224, v94\n"
    "v_ldexp_f32 v225, v225, v94\n"
    "v_ldexp_f32 v226, v226, v94\n"
    "v_ldexp_f32 v227, v227, v94\n"
    "v_ldexp_f32 v228, v228, v95\n"
    "v_ldexp_f32 v229, v229, v95\n"
    "v_ldexp_f32 v230, v230, v95\n"
    "v_ldexp_f32 v231, v231, v95\n"
    "v_ldexp_f32 v232, v232, v94\n"
    "v_ldexp_f32 v233, v233, v94\n"
    "v_ldexp_f32 v234, v234, v94\n"
    "v_ldexp_f32 v235, v235, v94\n"
    "v_ldexp_f32 v236, v236, v95\n"
    "v_ldexp_f32 v237, v237, v95\n"
    "v_ldexp_f32 v238, v238, v95\n"
    "v_ldexp_f32 v239, v239, v95\n"
    "v_ldexp_f32 v240, v240, v94\n"
    "v_ldexp_f32 v241, v241, v94\n"
    "v_ldexp_f32 v242, v242, v94\n"
    "v_ldexp_f32 v243, v243, v94\n"
    "v_ldexp_f32 v244, v244, v95\n"
    "v_ldexp_f32 v245, v245, v95\n"
    "v_ldexp_f32 v246, v246, v95\n"
    "v_ldexp_f32 v247, v247, v95\n"
    "v_ldexp_f32 v248, v248, v94\n"
    "v_ldexp_f32 v249, v249, v94\n"
    "v_ldexp_f32 v250, v250, v94\n"
    "v_ldexp_f32 v251, v251, v94\n"
    "v_ldexp_f32 v252, v252, v95\n"
    "v_ldexp_f32 v253, v253, v95\n"
    "v_ldexp_f32 v254, v254, v95\n"
    "v_ldexp_f32 v255, v255, v95\n"
    "s_nop 1\n"
    "s_branch LB9_%=\n"
    "LR10_%=:\n"
    "s_nop 8\n"
    "v_max3_f32 v82, v48, v49, v50\n"
    "v_max3_f32 v83, v56, v57, v58\n"
    "v_max3_f32 v82, v82, v51, v52\n"
    "v_max3_f32 v83, v83, v59, v60\n"
    "v_max3_f32 v82, v82, v53, v54\n"
    "v_max3_f32 v83, v83, v61, v62\n"
    "v_max_f32 v82, v82, v55\n"
    "v_max_f32 v83, v83, v63\n"
    "s_nop 1\n"
    "v_mov_b32 v92, v82\n"
    "v_mov_b32 v93, v83\n"
    "s_nop 1\n"
    "v_permlane16_swap_b32 v82, v92\n"
    "v_permlane16_swap_b32 v83, v93\n"
    "s_nop 1\n"
    "v_max_f32 v82, v82, v92\n"
    "v_max_f32 v83, v83, v93\n"
    "v_mov_b32 v92, v82\n"
    "v_mov_b32 v93, v83\n"
    "s_nop 1\n"
    "v_permlane32_swap_b32 v82, v92\n"
    "v_permlane32_swap_b32 v83, v93\n"
    "s_nop 1\n"
    "v_max_f32 v92, v82, v92\n"
    "v_max_f32 v93, v83, v93\n"
    "v_add_f32 v92, 0x42c80000, v92\n"
    "v_ceil_f32 v92, v92\n"
    "v_max_f32 v92, 0, v92\n"
    "v_add_f32 v93, 0x42c80000, v93\n"
    "v_ceil_f32 v93, v93\n"
    "v_max_f32 v93, 0, v93\n"
    "v_sub_f32 v48, v48, v92\n"
    "v_sub_f32 v49, v49, v92\n"
    "v_sub_f32 v50, v50, v92\n"
    "v_sub_f32 v51, v51, v92\n"
    "v_sub_f32 v52, v52, v92\n"
    "v_sub_f32 v53, v53, v92\n"
    "v_sub_f32 v54, v54, v92\n"
    "v_sub_f32 v55, v55, v92\n"
    "v_sub_f32 v56, v56, v93\n"
    "v_sub_f32 v57, v57, v93\n"
    "v_sub_f32 v58, v58, v93\n"
    "v_sub_f32 v59, v59, v93\n"
    "v_sub_f32 v60, v60, v93\n"
    "v_sub_f32 v61, v61, v93\n"
    "v_sub_f32 v62, v62, v93\n"
    "v_sub_f32 v63, v63, v93\n"
    "v_sub_f32 v64, v64, v92\n"
    "v_sub_f32 v65, v65, v92\n"
    "v_sub_f32 v66, v66, v92\n"
    "v_sub_f32 v67, v67, v92\n"
    "v_sub_f32 v68, v68, v93\n"
    "v_sub_f32 v69, v69, v93\n"
    "v_sub_f32 v70, v70, v93\n"
    "v_sub_f32 v71, v71, v93\n"
    "v_sub_f32 v94, 0, v92\n"
    "v_sub_f32 v95, 0, v93\n"
    "v_cvt_i32_f32 v94, v94\n"
    "v_cvt_i32_f32 v95, v95\n"
    "v_ldexp_f32 v80, v80, v94\n"
    "v_ldexp_f32 v81, v81, v95\n"
    "v_ldexp_f32 v128, v128, v94\n"
    "v_ldexp_f32 v129, v129, v94\n"
    "v_ldexp_f32 v130, v130, v94\n"
    "v_ldexp_f32 v131, v131, v94\n"
    "v_ldexp_f32 v132, v132, v95\n"
    "v_ldexp_f32 v133, v133, v95\n"
    "v_ldexp_f32 v134, v134, v95\n"
    "v_ldexp_f32 v135, v135, v95\n"
    "v_ldexp_f32 v136, v136, v94\n"
    "v_ldexp_f32 v137, v137, v94\n"
    "v_ldexp_f32 v138, v138, v94\n"
    "v_ldexp_f32 v139, v139, v94\n"
    "v_ldexp_f32 v140, v140, v95\n"
    "v_ldexp_f32 v141, v141, v95\n"
    "v_ldexp_f32 v142, v142, v95\n"
    "v_ldexp_f32 v143, v143, v95\n"
    "v_ldexp_f32 v144, v144, v94\n"
    "v_ldexp_f32 v145, v145, v94\n"
    "v_ldexp_f32 v146, v146, v94\n"
    "v_ldexp_f32 v147, v147, v94\n"
    "v_ldexp_f32 v148, v148, v95\n"
    "v_ldexp_f32 v149, v149, v95\n"
    "v_ldexp_f32 v150, v150, v95\n"
    "v_ldexp_f32 v151, v151, v95\n"
    "v_ldexp_f32 v152, v152, v94\n"
    "v_ldexp_f32 v153, v153, v94\n"
    "v_ldexp_f32 v154, v154, v94\n"
    "v_ldexp_f32 v155, v155, v94\n"
    "v_ldexp_f32 v156, v156, v95\n"
    "v_ldexp_f32 v157, v157, v95\n"
    "v_ldexp_f32 v158, v158, v95\n"
    "v_ldexp_f32 v159, v159, v95\n"
    "v_ldexp_f32 v160, v160, v94\n"
    "v_ldexp_f32 v161, v161, v94\n"
    "v_ldexp_f32 v162, v162, v94\n"
    "v_ldexp_f32 v163, v163, v94\n"
    "v_ldexp_f32 v164, v164, v95\n"
    "v_ldexp_f32 v165, v165, v95\n"
    "v_ldexp_f32 v166, v166, v95\n"
    "v_ldexp_f32 v167, v167, v95\n"
    "v_ldexp_f32 v168, v168, v94\n"
    "v_ldexp_f32 v169, v169, v94\n"
    "v_ldexp_f32 v170, v170, v94\n"
    "v_ldexp_f32 v171, v171, v94\n"
    "v_ldexp_f32 v172, v172, v95\n"
    "v_ldexp_f32 v173, v173, v95\n"
    "v_ldexp_f32 v174, v174, v95\n"
    "v_ldexp_f32 v175, v175, v95\n"
    "v_ldexp_f32 v176, v176, v94\n"
    "v_ldexp_f32 v177, v177, v94\n"
    "v_ldexp_f32 v178, v178, v94\n"
    "v_ldexp_f32 v179, v179, v94\n"
    "v_ldexp_f32 v180, v180, v95\n"
    "v_ldexp_f32 v181, v181, v95\n"
    "v_ldexp_f32 v182, v182, v95\n"
    "v_ldexp_f32 v183, v183, v95\n"
    "v_ldexp_f32 v184, v184, v94\n"
    "v_ldexp_f32 v185, v185, v94\n"
    "v_ldexp_f32 v186, v186, v94\n"
    "v_ldexp_f32 v187, v187, v94\n"
    "v_ldexp_f32 v188, v188, v95\n"
    "v_ldexp_f32 v189, v189, v95\n"
    "v_ldexp_f32 v190, v190, v95\n"
    "v_ldexp_f32 v191, v191, v95\n"
    "v_ldexp_f32 v192, v192, v94\n"
    "v_ldexp_f32 v193, v193, v94\n"
    "v_ldexp_f32 v194, v194, v94\n"
    "v_ldexp_f32 v195, v195, v94\n"
    "v_ldexp_f32 v196, v196, v95\n"
    "v_ldexp_f32 v197, v197, v95\n"
    "v_ldexp_f32 v198, v198, v95\n"
    "v_ldexp_f32 v199, v199, v95\n"
    "v_ldexp_f32 v200, v200, v94\n"
    "v_ldexp_f32 v201, v201, v94\n"
    "v_ldexp_f32 v202, v202, v94\n"
    "v_ldexp_f32 v203, v203, v94\n"
    "v_ldexp_f32 v204, v204, v95\n"
    "v_ldexp_f32 v205, v205, v95\n"
    "v_ldexp_f32 v206, v206, v95\n"
    "v_ldexp_f32 v207, v207, v95\n"
    "v_ldexp_f32 v208, v208, v94\n"
    "v_ldexp_f32 v209, v209, v94\n"
    "v_ldexp_f32 v210, v210, v94\n"
    "v_ldexp_f32 v211, v211, v94\n"
    "v_ldexp_f32 v212, v212, v95\n"
    "v_ldexp_f32 v213, v213, v95\n"
    "v_ldexp_f32 v214, v214, v95\n"
    "v_ldexp_f32 v215, v215, v95\n"
    "v_ldexp_f32 v216, v216, v94\n"
    "v_ldexp_f32 v217, v217, v94\n"
    "v_ldexp_f32 v218, v218, v94\n"
    "v_ldexp_f32 v219, v219, v94\n"
    "v_ldexp_f32 v220, v220, v95\n"
    "v_ldexp_f32 v221, v221, v95\n"
    "v_ldexp_f32 v222, v222, v95\n"
    "v_ldexp_f32 v223, v223, v95\n"
    "v_ldexp_f32 v224, v224, v94\n"
    "v_ldexp_f32 v225, v225, v94\n"
    "v_ldexp_f32 v226, v226, v94\n"
    "v_ldexp_f32 v227, v227, v94\n"
    "v_ldexp_f32 v228, v228, v95\n"
    "v_ldexp_f32 v229, v229, v95\n"
    "v_ldexp_f32 v230, v230, v95\n"
    "v_ldexp_f32 v231, v231, v95\n"
    "v_ldexp_f32 v232, v232, v94\n"
    "v_ldexp_f32 v233, v233, v94\n"
    "v_ldexp_f32 v234, v234, v94\n"
    "v_ldexp_f32 v235, v235, v94\n"
    "v_ldexp_f32 v236, v236, v95\n"
    "v_ldexp_f32 v237, v237, v95\n"
    "v_ldexp_f32 v238, v238, v95\n"
    "v_ldexp_f32 v239, v239, v95\n"
    "v_ldexp_f32 v240, v240, v94\n"
    "v_ldexp_f32 v241, v241, v94\n"
    "v_ldexp_f32 v242, v242, v94\n"
    "v_ldexp_f32 v243, v243, v94\n"
    "v_ldexp_f32 v244, v244, v95\n"
    "v_ldexp_f32 v245, v245, v95\n"
    "v_ldexp_f32 v246, v246, v95\n"
    "v_ldexp_f32 v247, v247, v95\n"
    "v_ldexp_f32 v248, v248, v94\n"
    "v_ldexp_f32 v249, v249, v94\n"
    "v_ldexp_f32 v250, v250, v94\n"
    "v_ldexp_f32 v251, v251, v94\n"
    "v_ldexp_f32 v252, v252, v95\n"
    "v_ldexp_f32 v253, v253, v95\n"
    "v_ldexp_f32 v254, v254, v95\n"
    "v_ldexp_f32 v255, v255, v95\n"
    "s_nop 1\n"
    "s_branch LB10_%=\n"
    "LR11_%=:\n"
    "s_nop 8\n"
    "v_max3_f32 v82, v32, v33, v34\n"
    "v_max3_f32 v83, v40, v41, v42\n"
    "v_max3_f32 v82, v82, v35, v36\n"
    "v_max3_f32 v83, v83, v43, v44\n"
    "v_max3_f32 v82, v82, v37, v38\n"
    "v_max3_f32 v83, v83, v45, v46\n"
    "v_max_f32 v82, v82, v39\n"
    "v_max_f32 v83, v83, v47\n"
    "s_nop 1\n"
    "v_mov_b32 v92, v82\n"
    "v_mov_b32 v93, v83\n"
    "s_nop 1\n"
    "v_permlane16_swap_b32 v82, v92\n"
    "v_permlane16_swap_b32 v83, v93\n"
    "s_nop 1\n"
    "v_max_f32 v82, v82, v92\n"
    "v_max_f32 v83, v83, v93\n"
    "v_mov_b32 v92, v82\n"
    "v_mov_b32 v93, v83\n"
    "s_nop 1\n"
    "v_permlane32_swap_b32 v82, v92\n"
    "v_permlane32_swap_b32 v83, v93\n"
    "s_nop 1\n"
    "v_max_f32 v92, v82, v92\n"
    "v_max_f32 v93, v83, v93\n"
    "v_add_f32 v92, 0x42c80000, v92\n"
    "v_ceil_f32 v92, v92\n"
    "v_max_f32 v92, 0, v92\n"
    "v_add_f32 v93, 0x42c80000, v93\n"
    "v_ceil_f32 v93, v93\n"
    "v_max_f32 v93, 0, v93\n"
    "v_sub_f32 v32, v32, v92\n"
    "v_sub_f32 v33, v33, v92\n"
    "v_sub_f32 v34, v34, v92\n"
    "v_sub_f32 v35, v35, v92\n"
    "v_sub_f32 v36, v36, v92\n"
    "v_sub_f32 v37, v37, v92\n"
    "v_sub_f32 v38, v38, v92\n"
    "v_sub_f32 v39, v39, v92\n"
    "v_sub_f32 v40, v40, v93\n"
    "v_sub_f32 v41, v41, v93\n"
    "v_sub_f32 v42, v42, v93\n"
    "v_sub_f32 v43, v43, v93\n"
    "v_sub_f32 v44, v44, v93\n"
    "v_sub_f32 v45, v45, v93\n"
    "v_sub_f32 v46, v46, v93\n"
    "v_sub_f32 v47, v47, v93\n"
    "v_sub_f32 v64, v64, v92\n"
    "v_sub_f32 v65, v65, v92\n"
    "v_sub_f32 v66, v66, v92\n"
    "v_sub_f32 v67, v67, v92\n"
    "v_sub_f32 v68, v68, v93\n"
    "v_sub_f32 v69, v69, v93\n"
    "v_sub_f32 v70, v70, v93\n"
    "v_sub_f32 v71, v71, v93\n"
    "v_sub_f32 v94, 0, v92\n"
    "v_sub_f32 v95, 0, v93\n"
    "v_cvt_i32_f32 v94, v94\n"
    "v_cvt_i32_f32 v95, v95\n"
    "v_ldexp_f32 v80, v80, v94\n"
    "v_ldexp_f32 v81, v81, v95\n"
    "v_ldexp_f32 v128, v128, v94\n"
    "v_ldexp_f32 v129, v129, v94\n"
    "v_ldexp_f32 v130, v130, v94\n"
    "v_ldexp_f32 v131, v131, v94\n"
    "v_ldexp_f32 v132, v132, v95\n"
    "v_ldexp_f32 v133, v133, v95\n"
    "v_ldexp_f32 v134, v134, v95\n"
    "v_ldexp_f32 v135, v135, v95\n"
    "v_ldexp_f32 v136, v136, v94\n"
    "v_ldexp_f32 v137, v137, v94\n"
    "v_ldexp_f32 v138, v138, v94\n"
    "v_ldexp_f32 v139, v139, v94\n"
    "v_ldexp_f32 v140, v140, v95\n"
    "v_ldexp_f32 v141, v141, v95\n"
    "v_ldexp_f32 v142, v142, v95\n"
    "v_ldexp_f32 v143, v143, v95\n"
    "v_ldexp_f32 v144, v144, v94\n"
    "v_ldexp_f32 v145, v145, v94\n"
    "v_ldexp_f32 v146, v146, v94\n"
    "v_ldexp_f32 v147, v147, v94\n"
    "v_ldexp_f32 v148, v148, v95\n"
    "v_ldexp_f32 v149, v149, v95\n"
    "v_ldexp_f32 v150, v150, v95\n"
    "v_ldexp_f32 v151, v151, v95\n"
    "v_ldexp_f32 v152, v152, v94\n"
    "v_ldexp_f32 v153, v153, v94\n"
    "v_ldexp_f32 v154, v154, v94\n"
    "v_ldexp_f32 v155, v155, v94\n"
    "v_ldexp_f32 v156, v156, v95\n"
    "v_ldexp_f32 v157, v157, v95\n"
    "v_ldexp_f32 v158, v158, v95\n"
    "v_ldexp_f32 v159, v159, v95\n"
    "v_ldexp_f32 v160, v160, v94\n"
    "v_ldexp_f32 v161, v161, v94\n"
    "v_ldexp_f32 v162, v162, v94\n"
    "v_ldexp_f32 v163, v163, v94\n"
    "v_ldexp_f32 v164, v164, v95\n"
    "v_ldexp_f32 v165, v165, v95\n"
    "v_ldexp_f32 v166, v166, v95\n"
    "v_ldexp_f32 v167, v167, v95\n"
    "v_ldexp_f32 v168, v168, v94\n"
    "v_ldexp_f32 v169, v169, v94\n"
    "v_ldexp_f32 v170, v170, v94\n"
    "v_ldexp_f32 v171, v171, v94\n"
    "v_ldexp_f32 v172, v172, v95\n"
    "v_ldexp_f32 v173, v173, v95\n"
    "v_ldexp_f32 v174, v174, v95\n"
    "v_ldexp_f32 v175, v175, v95\n"
    "v_ldexp_f32 v176, v176, v94\n"
    "v_ldexp_f32 v177, v177, v94\n"
    "v_ldexp_f32 v178, v178, v94\n"
    "v_ldexp_f32 v179, v179, v94\n"
    "v_ldexp_f32 v180, v180, v95\n"
    "v_ldexp_f32 v181, v181, v95\n"
    "v_ldexp_f32 v182, v182, v95\n"
    "v_ldexp_f32 v183, v183, v95\n"
    "v_ldexp_f32 v184, v184, v94\n"
    "v_ldexp_f32 v185, v185, v94\n"
    "v_ldexp_f32 v186, v186, v94\n"
    "v_ldexp_f32 v187, v187, v94\n"
    "v_ldexp_f32 v188, v188, v95\n"
    "v_ldexp_f32 v189, v189, v95\n"
    "v_ldexp_f32 v190, v190, v95\n"
    "v_ldexp_f32 v191, v191, v95\n"
    "v_ldexp_f32 v192, v192, v94\n"
    "v_ldexp_f32 v193, v193, v94\n"
    "v_ldexp_f32 v194, v194, v94\n"
    "v_ldexp_f32 v195, v195, v94\n"
    "v_ldexp_f32 v196, v196, v95\n"
    "v_ldexp_f32 v197, v197, v95\n"
    "v_ldexp_f32 v198, v198, v95\n"
    "v_ldexp_f32 v199, v199, v95\n"
    "v_ldexp_f32 v200, v200, v94\n"
    "v_ldexp_f32 v201, v201, v94\n"
    "v_ldexp_f32 v202, v202, v94\n"
    "v_ldexp_f32 v203, v203, v94\n"
    "v_ldexp_f32 v204, v204, v95\n"
    "v_ldexp_f32 v205, v205, v95\n"
    "v_ldexp_f32 v206, v206, v95\n"
    "v_ldexp_f32 v207, v207, v95\n"
    "v_ldexp_f32 v208, v208, v94\n"
    "v_ldexp_f32 v209, v209, v94\n"
    "v_ldexp_f32 v210, v210, v94\n"
    "v_ldexp_f32 v211, v211, v94\n"
    "v_ldexp_f32 v212, v212, v95\n"
    "v_ldexp_f32 v213, v213, v95\n"
    "v_ldexp_f32 v214, v214, v95\n"
    "v_ldexp_f32 v215, v215, v95\n"
    "v_ldexp_f32 v216, v216, v94\n"
    "v_ldexp_f32 v217, v217, v94\n"
    "v_ldexp_f32 v218, v218, v94\n"
    "v_ldexp_f32 v219, v219, v94\n"
    "v_ldexp_f32 v220, v220, v95\n"
    "v_ldexp_f32 v221, v221, v95\n"
    "v_ldexp_f32 v222, v222, v95\n"
    "v_ldexp_f32 v223, v223, v95\n"
    "v_ldexp_f32 v224, v224, v94\n"
    "v_ldexp_f32 v225, v225, v94\n"
    "v_ldexp_f32 v226, v226, v94\n"
    "v_ldexp_f32 v227, v227, v94\n"
    "v_ldexp_f32 v228, v228, v95\n"
    "v_ldexp_f32 v229, v229, v95\n"
    "v_ldexp_f32 v230, v230, v95\n"
    "v_ldexp_f32 v231, v231, v95\n"
    "v_ldexp_f32 v232, v232, v94\n"
    "v_ldexp_f32 v233, v233, v94\n"
    "v_ldexp_f32 v234, v234, v94\n"
    "v_ldexp_f32 v235, v235, v94\n"
    "v_ldexp_f32 v236, v236, v95\n"
    "v_ldexp_f32 v237, v237, v95\n"
    "v_ldexp_f32 v238, v238, v95\n"
    "v_ldexp_f32 v239, v239, v95\n"
    "v_ldexp_f32 v240, v240, v94\n"
    "v_ldexp_f32 v241, v241, v94\n"
    "v_ldexp_f32 v242, v242, v94\n"
    "v_ldexp_f32 v243, v243, v94\n"
    "v_ldexp_f32 v244, v244, v95\n"
    "v_ldexp_f32 v245, v245, v95\n"
    "v_ldexp_f32 v246, v246, v95\n"
    "v_ldexp_f32 v247, v247, v95\n"
    "v_ldexp_f32 v248, v248, v94\n"
    "v_ldexp_f32 v249, v249, v94\n"
    "v_ldexp_f32 v250, v250, v94\n"
    "v_ldexp_f32 v251, v251, v94\n"
    "v_ldexp_f32 v252, v252, v95\n"
    "v_ldexp_f32 v253, v253, v95\n"
    "v_ldexp_f32 v254, v254, v95\n"
    "v_ldexp_f32 v255, v255, v95\n"
    "s_nop 1\n"
    "s_branch LB11_%=\n"
    "LR12_%=:\n"
    "s_nop 8\n"
    "v_max3_f32 v82, v48, v49, v50\n"
    "v_max3_f32 v83, v56, v57, v58\n"
    "v_max3_f32 v82, v82, v51, v52\n"
    "v_max3_f32 v83, v83, v59, v60\n"
    "v_max3_f32 v82, v82, v53, v54\n"
    "v_max3_f32 v83, v83, v61, v62\n"
    "v_max_f32 v82, v82, v55\n"
    "v_max_f32 v83, v83, v63\n"
    "s_nop 1\n"
    "v_mov_b32 v92, v82\n"
    "v_mov_b32 v93, v83\n"
    "s_nop 1\n"
    "v_permlane16_swap_b32 v82, v92\n"
    "v_permlane16_swap_b32 v83, v93\n"
    "s_nop 1\n"
    "v_max_f32 v82, v82, v92\n"
    "v_max_f32 v83, v83, v93\n"
    "v_mov_b32 v92, v82\n"
    "v_mov_b32 v93, v83\n"
    "s_nop 1\n"
    "v_permlane32_swap_b32 v82, v92\n"
    "v_permlane32_swap_b32 v83, v93\n"
    "s_nop 1\n"
    "v_max_f32 v92, v82, v92\n"
    "v_max_f32 v93, v83, v93\n"
    "v_add_f32 v92, 0x42c80000, v92\n"
    "v_ceil_f32 v92, v92\n"
    "v_max_f32 v92, 0, v92\n"
    "v_add_f32 v93, 0x42c80000, v93\n"
    "v_ceil_f32 v93, v93\n"
    "v_max_f32 v93, 0, v93\n"
    "v_sub_f32 v48, v48, v92\n"
    "v_sub_f32 v49, v49, v92\n"
    "v_sub_f32 v50, v50, v92\n"
    "v_sub_f32 v51, v51, v92\n"
    "v_sub_f32 v52, v52, v92\n"
    "v_sub_f32 v53, v53, v92\n"
    "v_sub_f32 v54, v54, v92\n"
    "v_sub_f32 v55, v55, v92\n"
    "v_sub_f32 v56, v56, v93\n"
    "v_sub_f32 v57, v57, v93\n"
    "v_sub_f32 v58, v58, v93\n"
    "v_sub_f32 v59, v59, v93\n"
    "v_sub_f32 v60, v60, v93\n"
    "v_sub_f32 v61, v61, v93\n"
    "v_sub_f32 v62, v62, v93\n"
    "v_sub_f32 v63, v63, v93\n"
    "v_sub_f32 v64, v64, v92\n"
    "v_sub_f32 v65, v65, v92\n"
    "v_sub_f32 v66, v66, v92\n"
    "v_sub_f32 v67, v67, v92\n"
    "v_sub_f32 v68, v68, v93\n"
    "v_sub_f32 v69, v69, v93\n"
    "v_sub_f32 v70, v70, v93\n"
    "v_sub_f32 v71, v71, v93\n"
    "v_sub_f32 v94, 0, v92\n"
    "v_sub_f32 v95, 0, v93\n"
    "v_cvt_i32_f32 v94, v94\n"
    "v_cvt_i32_f32 v95, v95\n"
    "v_ldexp_f32 v80, v80, v94\n"
    "v_ldexp_f32 v81, v81, v95\n"
    "v_ldexp_f32 v128, v128, v94\n"
    "v_ldexp_f32 v129, v129, v94\n"
    "v_ldexp_f32 v130, v130, v94\n"
    "v_ldexp_f32 v131, v131, v94\n"
    "v_ldexp_f32 v132, v132, v95\n"
    "v_ldexp_f32 v133, v133, v95\n"
    "v_ldexp_f32 v134, v134, v95\n"
    "v_ldexp_f32 v135, v135, v95\n"
    "v_ldexp_f32 v136, v136, v94\n"
    "v_ldexp_f32 v137, v137, v94\n"
    "v_ldexp_f32 v138, v138, v94\n"
    "v_ldexp_f32 v139, v139, v94\n"
    "v_ldexp_f32 v140, v140, v95\n"
    "v_ldexp_f32 v141, v141, v95\n"
    "v_ldexp_f32 v142, v142, v95\n"
    "v_ldexp_f32 v143, v143, v95\n"
    "v_ldexp_f32 v144, v144, v94\n"
    "v_ldexp_f32 v145, v145, v94\n"
    "v_ldexp_f32 v146, v146, v94\n"
    "v_ldexp_f32 v147, v147, v94\n"
    "v_ldexp_f32 v148, v148, v95\n"
    "v_ldexp_f32 v149, v149, v95\n"
    "v_ldexp_f32 v150, v150, v95\n"
    "v_ldexp_f32 v151, v151, v95\n"
    "v_ldexp_f32 v152, v152, v94\n"
    "v_ldexp_f32 v153, v153, v94\n"
    "v_ldexp_f32 v154, v154, v94\n"
    "v_ldexp_f32 v155, v155, v94\n"
    "v_ldexp_f32 v156, v156, v95\n"
    "v_ldexp_f32 v157, v157, v95\n"
    "v_ldexp_f32 v158, v158, v95\n"
    "v_ldexp_f32 v159, v159, v95\n"
    "v_ldexp_f32 v160, v160, v94\n"
    "v_ldexp_f32 v161, v161, v94\n"
    "v_ldexp_f32 v162, v162, v94\n"
    "v_ldexp_f32 v163, v163, v94\n"
    "v_ldexp_f32 v164, v164, v95\n"
    "v_ldexp_f32 v165, v165, v95\n"
    "v_ldexp_f32 v166, v166, v95\n"
    "v_ldexp_f32 v167, v167, v95\n"
    "v_ldexp_f32 v168, v168, v94\n"
    "v_ldexp_f32 v169, v169, v94\n"
    "v_ldexp_f32 v170, v170, v94\n"
    "v_ldexp_f32 v171, v171, v94\n"
    "v_ldexp_f32 v172, v172, v95\n"
    "v_ldexp_f32 v173, v173, v95\n"
    "v_ldexp_f32 v174, v174, v95\n"
    "v_ldexp_f32 v175, v175, v95\n"
    "v_ldexp_f32 v176, v176, v94\n"
    "v_ldexp_f32 v177, v177, v94\n"
    "v_ldexp_f32 v178, v178, v94\n"
    "v_ldexp_f32 v179, v179, v94\n"
    "v_ldexp_f32 v180, v180, v95\n"
    "v_ldexp_f32 v181, v181, v95\n"
    "v_ldexp_f32 v182, v182, v95\n"
    "v_ldexp_f32 v183, v183, v95\n"
    "v_ldexp_f32 v184, v184, v94\n"
    "v_ldexp_f32 v185, v185, v94\n"
    "v_ldexp_f32 v186, v186, v94\n"
    "v_ldexp_f32 v187, v187, v94\n"
    "v_ldexp_f32 v188, v188, v95\n"
    "v_ldexp_f32 v189, v189, v95\n"
    "v_ldexp_f32 v190, v190, v95\n"
    "v_ldexp_f32 v191, v191, v95\n"
    "v_ldexp_f32 v192, v192, v94\n"
    "v_ldexp_f32 v193, v193, v94\n"
    "v_ldexp_f32 v194, v194, v94\n"
    "v_ldexp_f32 v195, v195, v94\n"
    "v_ldexp_f32 v196, v196, v95\n"
    "v_ldexp_f32 v197, v197, v95\n"
    "v_ldexp_f32 v198, v198, v95\n"
    "v_ldexp_f32 v199, v199, v95\n"
    "v_ldexp_f32 v200, v200, v94\n"
    "v_ldexp_f32 v201, v201, v94\n"
    "v_ldexp_f32 v202, v202, v94\n"
    "v_ldexp_f32 v203, v203, v94\n"
    "v_ldexp_f32 v204, v204, v95\n"
    "v_ldexp_f32 v205, v205, v95\n"
    "v_ldexp_f32 v206, v206, v95\n"
    "v_ldexp_f32 v207, v207, v95\n"
    "v_ldexp_f32 v208, v208, v94\n"
    "v_ldexp_f32 v209, v209, v94\n"
    "v_ldexp_f32 v210, v210, v94\n"
    "v_ldexp_f32 v211, v211, v94\n"
    "v_ldexp_f32 v212, v212, v95\n"
    "v_ldexp_f32 v213, v213, v95\n"
    "v_ldexp_f32 v214, v214, v95\n"
    "v_ldexp_f32 v215, v215, v95\n"
    "v_ldexp_f32 v216, v216, v94\n"
    "v_ldexp_f32 v217, v217, v94\n"
    "v_ldexp_f32 v218, v218, v94\n"
    "v_ldexp_f32 v219, v219, v94\n"
    "v_ldexp_f32 v220, v220, v95\n"
    "v_ldexp_f32 v221, v221, v95\n"
    "v_ldexp_f32 v222, v222, v95\n"
    "v_ldexp_f32 v223, v223, v95\n"
    "v_ldexp_f32 v224, v224, v94\n"
    "v_ldexp_f32 v225, v225, v94\n"
    "v_ldexp_f32 v226, v226, v94\n"
    "v_ldexp_f32 v227, v227, v94\n"
    "v_ldexp_f32 v228, v228, v95\n"
    "v_ldexp_f32 v229, v229, v95\n"
    "v_ldexp_f32 v230, v230, v95\n"
    "v_ldexp_f32 v231, v231, v95\n"
    "v_ldexp_f32 v232, v232, v94\n"
    "v_ldexp_f32 v233, v233, v94\n"
    "v_ldexp_f32 v234, v234, v94\n"
    "v_ldexp_f32 v235, v235, v94\n"
    "v_ldexp_f32 v236, v236, v95\n"
    "v_ldexp_f32 v237, v237, v95\n"
    "v_ldexp_f32 v238, v238, v95\n"
    "v_ldexp_f32 v239, v239, v95\n"
    "v_ldexp_f32 v240, v240, v94\n"
    "v_ldexp_f32 v241, v241, v94\n"
    "v_ldexp_f32 v242, v242, v94\n"
    "v_ldexp_f32 v243, v243, v94\n"
    "v_ldexp_f32 v244, v244, v95\n"
    "v_ldexp_f32 v245, v245, v95\n"
    "v_ldexp_f32 v246, v246, v95\n"
    "v_ldexp_f32 v247, v247, v95\n"
    "v_ldexp_f32 v248, v248, v94\n"
    "v_ldexp_f32 v249, v249, v94\n"
    "v_ldexp_f32 v250, v250, v94\n"
    "v_ldexp_f32 v251, v251, v94\n"
    "v_ldexp_f32 v252, v252, v95\n"
    "v_ldexp_f32 v253, v253, v95\n"
    "v_ldexp_f32 v254, v254, v95\n"
    "v_ldexp_f32 v255, v255, v95\n"
    "s_nop 1\n"
    "s_branch LB12_%=\n"
    "LR65_%=:\n"
    "s_nop 8\n"
    "v_max3_f32 v82, v32, v33, v34\n"
    "v_max3_f32 v83, v40, v41, v42\n"
    "v_max3_f32 v82, v82, v35, v36\n"
    "v_max3_f32 v83, v83, v43, v44\n"
    "v_max3_f32 v82, v82, v37, v38\n"
    "v_max3_f32 v83, v83, v45, v46\n"
    "v_max_f32 v82, v82, v39\n"
    "v_max_f32 v83, v83, v47\n"
    "s_nop 1\n"
    "v_mov_b32 v92, v82\n"
    "v_mov_b32 v93, v83\n"
    "s_nop 1\n"
    "v_permlane16_swap_b32 v82, v92\n"
    "v_permlane16_swap_b32 v83, v93\n"
    "s_nop 1\n"
    "v_max_f32 v82, v82, v92\n"
    "v_max_f32 v83, v83, v93\n"
    "v_mov_b32 v92, v82\n"
    "v_mov_b32 v93, v83\n"
    "s_nop 1\n"
    "v_permlane32_swap_b32 v82, v92\n"
    "v_permlane32_swap_b32 v83, v93\n"
    "s_nop 1\n"
    "v_max_f32 v92, v82, v92\n"
    "v_max_f32 v93, v83, v93\n"
    "v_add_f32 v92, 0x42c80000, v92\n"
    "v_ceil_f32 v92, v92\n"
    "v_max_f32 v92, 0, v92\n"
    "v_add_f32 v93, 0x42c80000, v93\n"
    "v_ceil_f32 v93, v93\n"
    "v_max_f32 v93, 0, v93\n"
    "v_sub_f32 v32, v32, v92\n"
    "v_sub_f32 v33, v33, v92\n"
    "v_sub_f32 v34, v34, v92\n"
    "v_sub_f32 v35, v35, v92\n"
    "v_sub_f32 v36, v36, v92\n"
    "v_sub_f32 v37, v37, v92\n"
    "v_sub_f32 v38, v38, v92\n"
    "v_sub_f32 v39, v39, v92\n"
    "v_sub_f32 v40, v40, v93\n"
    "v_sub_f32 v41, v41, v93\n"
    "v_sub_f32 v42, v42, v93\n"
    "v_sub_f32 v43, v43, v93\n"
    "v_sub_f32 v44, v44, v93\n"
    "v_sub_f32 v45, v45, v93\n"
    "v_sub_f32 v46, v46, v93\n"
    "v_sub_f32 v47, v47, v93\n"
    "v_sub_f32 v64, v64, v92\n"
    "v_sub_f32 v65, v65, v92\n"
    "v_sub_f32 v66, v66, v92\n"
    "v_sub_f32 v67, v67, v92\n"
    "v_sub_f32 v68, v68, v93\n"
    "v_sub_f32 v69, v69, v93\n"
    "v_sub_f32 v70, v70, v93\n"
    "v_sub_f32 v71, v71, v93\n"
    "v_sub_f32 v94, 0, v92\n"
    "v_sub_f32 v95, 0, v93\n"
    "v_cvt_i32_f32 v94, v94\n"
    "v_cvt_i32_f32 v95, v95\n"
    "v_ldexp_f32 v80, v80, v94\n"
    "v_ldexp_f32 v81, v81, v95\n"
    "v_ldexp_f32 v128, v128, v94\n"
    "v_ldexp_f32 v129, v129, v94\n"
    "v_ldexp_f32 v130, v130, v94\n"
    "v_ldexp_f32 v131, v131, v94\n"
    "v_ldexp_f32 v132, v132, v95\n"
    "v_ldexp_f32 v133, v133, v95\n"
    "v_ldexp_f32 v134, v134, v95\n"
    "v_ldexp_f32 v135, v135, v95\n"
    "v_ldexp_f32 v136, v136, v94\n"
    "v_ldexp_f32 v137, v137, v94\n"
    "v_ldexp_f32 v138, v138, v94\n"
    "v_ldexp_f32 v139, v139, v94\n"
    "v_ldexp_f32 v140, v140, v95\n"
    "v_ldexp_f32 v141, v141, v95\n"
    "v_ldexp_f32 v142, v142, v95\n"
    "v_ldexp_f32 v143, v143, v95\n"
    "v_ldexp_f32 v144, v144, v94\n"
    "v_ldexp_f32 v145, v145, v94\n"
    "v_ldexp_f32 v146, v146, v94\n"
    "v_ldexp_f32 v147, v147, v94\n"
    "v_ldexp_f32 v148, v148, v95\n"
    "v_ldexp_f32 v149, v149, v95\n"
    "v_ldexp_f32 v150, v150, v95\n"
    "v_ldexp_f32 v151, v151, v95\n"
    "v_ldexp_f32 v152, v152, v94\n"
    "v_ldexp_f32 v153, v153, v94\n"
    "v_ldexp_f32 v154, v154, v94\n"
    "v_ldexp_f32 v155, v155, v94\n"
    "v_ldexp_f32 v156, v156, v95\n"
    "v_ldexp_f32 v157, v157, v95\n"
    "v_ldexp_f32 v158, v158, v95\n"
    "v_ldexp_f32 v159, v159, v95\n"
    "v_ldexp_f32 v160, v160, v94\n"
    "v_ldexp_f32 v161, v161, v94\n"
    "v_ldexp_f32 v162, v162, v94\n"
    "v_ldexp_f32 v163, v163, v94\n"
    "v_ldexp_f32 v164, v164, v95\n"
    "v_ldexp_f32 v165, v165, v95\n"
    "v_ldexp_f32 v166, v166, v95\n"
    "v_ldexp_f32 v167, v167, v95\n"
    "v_ldexp_f32 v168, v168, v94\n"
    "v_ldexp_f32 v169, v169, v94\n"
    "v_ldexp_f32 v170, v170, v94\n"
    "v_ldexp_f32 v171, v171, v94\n"
    "v_ldexp_f32 v172, v172, v95\n"
    "v_ldexp_f32 v173, v173, v95\n"
    "v_ldexp_f32 v174, v174, v95\n"
    "v_ldexp_f32 v175, v175, v95\n"
    "v_ldexp_f32 v176, v176, v94\n"
    "v_ldexp_f32 v177, v177, v94\n"
    "v_ldexp_f32 v178, v178, v94\n"
    "v_ldexp_f32 v179, v179, v94\n"
    "v_ldexp_f32 v180, v180, v95\n"
    "v_ldexp_f32 v181, v181, v95\n"
    "v_ldexp_f32 v182, v182, v95\n"
    "v_ldexp_f32 v183, v183, v95\n"
    "v_ldexp_f32 v184, v184, v94\n"
    "v_ldexp_f32 v185, v185, v94\n"
    "v_ldexp_f32 v186, v186, v94\n"
    "v_ldexp_f32 v187, v187, v94\n"
    "v_ldexp_f32 v188, v188, v95\n"
    "v_ldexp_f32 v189, v189, v95\n"
    "v_ldexp_f32 v190, v190, v95\n"
    "v_ldexp_f32 v191, v191, v95\n"
    "v_ldexp_f32 v192, v192, v94\n"
    "v_ldexp_f32 v193, v193, v94\n"
    "v_ldexp_f32 v194, v194, v94\n"
    "v_ldexp_f32 v195, v195, v94\n"
    "v_ldexp_f32 v196, v196, v95\n"
    "v_ldexp_f32 v197, v197, v95\n"
    "v_ldexp_f32 v198, v198, v95\n"
    "v_ldexp_f32 v199, v199, v95\n"
    "v_ldexp_f32 v200, v200, v94\n"
    "v_ldexp_f32 v201, v201, v94\n"
    "v_ldexp_f32 v202, v202, v94\n"
    "v_ldexp_f32 v203, v203, v94\n"
    "v_ldexp_f32 v204, v204, v95\n"
    "v_ldexp_f32 v205, v205, v95\n"
    "v_ldexp_f32 v206, v206, v95\n"
    "v_ldexp_f32 v207, v207, v95\n"
    "v_ldexp_f32 v208, v208, v94\n"
    "v_ldexp_f32 v209, v209, v94\n"
    "v_ldexp_f32 v210, v210, v94\n"
    "v_ldexp_f32 v211, v211, v94\n"
    "v_ldexp_f32 v212, v212, v95\n"
    "v_ldexp_f32 v213, v213, v95\n"
    "v_ldexp_f32 v214, v214, v95\n"
    "v_ldexp_f32 v215, v215, v95\n"
    "v_ldexp_f32 v216, v216, v94\n"
    "v_ldexp_f32 v217, v217, v94\n"
    "v_ldexp_f32 v218, v218, v94\n"
    "v_ldexp_f32 v219, v219, v94\n"
    "v_ldexp_f32 v220, v220, v95\n"
    "v_ldexp_f32 v221, v221, v95\n"
    "v_ldexp_f32 v222, v222, v95\n"
    "v_ldexp_f32 v223, v223, v95\n"
    "v_ldexp_f32 v224, v224, v94\n"
    "v_ldexp_f32 v225, v225, v94\n"
    "v_ldexp_f32 v226, v226, v94\n"
    "v_ldexp_f32 v227, v227, v94\n"
    "v_ldexp_f32 v228, v228, v95\n"
    "v_ldexp_f32 v229, v229, v95\n"
    "v_ldexp_f32 v230, v230, v95\n"
    "v_ldexp_f32 v231, v231, v95\n"
    "v_ldexp_f32 v232, v232, v94\n"
    "v_ldexp_f32 v233, v233, v94\n"
    "v_ldexp_f32 v234, v234, v94\n"
    "v_ldexp_f32 v235, v235, v94\n"
    "v_ldexp_f32 v236, v236, v95\n"
    "v_ldexp_f32 v237, v237, v95\n"
    "v_ldexp_f32 v238, v238, v95\n"
    "v_ldexp_f32 v239, v239, v95\n"
    "v_ldexp_f32 v240, v240, v94\n"
    "v_ldexp_f32 v241, v241, v94\n"
    "v_ldexp_f32 v242, v242, v94\n"
    "v_ldexp_f32 v243, v243, v94\n"
    "v_ldexp_f32 v244, v244, v95\n"
    "v_ldexp_f32 v245, v245, v95\n"
    "v_ldexp_f32 v246, v246, v95\n"
    "v_ldexp_f32 v247, v247, v95\n"
    "v_ldexp_f32 v248, v248, v94\n"
    "v_ldexp_f32 v249, v249, v94\n"
    "v_ldexp_f32 v250, v250, v94\n"
    "v_ldexp_f32 v251, v251, v94\n"
    "v_ldexp_f32 v252, v252, v95\n"
    "v_ldexp_f32 v253, v253, v95\n"
    "v_ldexp_f32 v254, v254, v95\n"
    "v_ldexp_f32 v255, v255, v95\n"
    "s_nop 1\n"
    "s_branch LB65_%=\n"
    "LR66_%=:\n"
    "s_nop 8\n"
    "v_max3_f32 v82, v48, v49, v50\n"
    "v_max3_f32 v83, v56, v57, v58\n"
    "v_max3_f32 v82, v82, v51, v52\n"
    "v_max3_f32 v83, v83, v59, v60\n"
    "v_max3_f32 v82, v82, v53, v54\n"
    "v_max3_f32 v83, v83, v61, v62\n"
    "v_max_f32 v82, v82, v55\n"
    "v_max_f32 v83, v83, v63\n"
    "s_nop 1\n"
    "v_mov_b32 v92, v82\n"
    "v_mov_b32 v93, v83\n"
    "s_nop 1\n"
    "v_permlane16_swap_b32 v82, v92\n"
    "v_permlane16_swap_b32 v83, v93\n"
    "s_nop 1\n"
    "v_max_f32 v82, v82, v92\n"
    "v_max_f32 v83, v83, v93\n"
    "v_mov_b32 v92, v82\n"
    "v_mov_b32 v93, v83\n"
    "s_nop 1\n"
    "v_permlane32_swap_b32 v82, v92\n"
    "v_permlane32_swap_b32 v83, v93\n"
    "s_nop 1\n"
    "v_max_f32 v92, v82, v92\n"
    "v_max_f32 v93, v83, v93\n"
    "v_add_f32 v92, 0x42c80000, v92\n"
    "v_ceil_f32 v92, v92\n"
    "v_max_f32 v92, 0, v92\n"
    "v_add_f32 v93, 0x42c80000, v93\n"
    "v_ceil_f32 v93, v93\n"
    "v_max_f32 v93, 0, v93\n"
    "v_sub_f32 v48, v48, v92\n"
    "v_sub_f32 v49, v49, v92\n"
    "v_sub_f32 v50, v50, v92\n"
    "v_sub_f32 v51, v51, v92\n"
    "v_sub_f32 v52, v52, v92\n"
    "v_sub_f32 v53, v53, v92\n"
    "v_sub_f32 v54, v54, v92\n"
    "v_sub_f32 v55, v55, v92\n"
    "v_sub_f32 v56, v56, v93\n"
    "v_sub_f32 v57, v57, v93\n"
    "v_sub_f32 v58, v58, v93\n"
    "v_sub_f32 v59, v59, v93\n"
    "v_sub_f32 v60, v60, v93\n"
    "v_sub_f32 v61, v61, v93\n"
    "v_sub_f32 v62, v62, v93\n"
    "v_sub_f32 v63, v63, v93\n"
    "v_sub_f32 v64, v64, v92\n"
    "v_sub_f32 v65, v65, v92\n"
    "v_sub_f32 v66, v66, v92\n"
    "v_sub_f32 v67, v67, v92\n"
    "v_sub_f32 v68, v68, v93\n"
    "v_sub_f32 v69, v69, v93\n"
    "v_sub_f32 v70, v70, v93\n"
    "v_sub_f32 v71, v71, v93\n"
    "v_sub_f32 v94, 0, v92\n"
    "v_sub_f32 v95, 0, v93\n"
    "v_cvt_i32_f32 v94, v94\n"
    "v_cvt_i32_f32 v95, v95\n"
    "v_ldexp_f32 v80, v80, v94\n"
    "v_ldexp_f32 v81, v81, v95\n"
    "v_ldexp_f32 v128, v128, v94\n"
    "v_ldexp_f32 v129, v129, v94\n"
    "v_ldexp_f32 v130, v130, v94\n"
    "v_ldexp_f32 v131, v131, v94\n"
    "v_ldexp_f32 v132, v132, v95\n"
    "v_ldexp_f32 v133, v133, v95\n"
    "v_ldexp_f32 v134, v134, v95\n"
    "v_ldexp_f32 v135, v135, v95\n"
    "v_ldexp_f32 v136, v136, v94\n"
    "v_ldexp_f32 v137, v137, v94\n"
    "v_ldexp_f32 v138, v138, v94\n"
    "v_ldexp_f32 v139, v139, v94\n"
    "v_ldexp_f32 v140, v140, v95\n"
    "v_ldexp_f32 v141, v141, v95\n"
    "v_ldexp_f32 v142, v142, v95\n"
    "v_ldexp_f32 v143, v143, v95\n"
    "v_ldexp_f32 v144, v144, v94\n"
    "v_ldexp_f32 v145, v145, v94\n"
    "v_ldexp_f32 v146, v146, v94\n"
    "v_ldexp_f32 v147, v147, v94\n"
    "v_ldexp_f32 v148, v148, v95\n"
    "v_ldexp_f32 v149, v149, v95\n"
    "v_ldexp_f32 v150, v150, v95\n"
    "v_ldexp_f32 v151, v151, v95\n"
    "v_ldexp_f32 v152, v152, v94\n"
    "v_ldexp_f32 v153, v153, v94\n"
    "v_ldexp_f32 v154, v154, v94\n"
    "v_ldexp_f32 v155, v155, v94\n"
    "v_ldexp_f32 v156, v156, v95\n"
    "v_ldexp_f32 v157, v157, v95\n"
    "v_ldexp_f32 v158, v158, v95\n"
    "v_ldexp_f32 v159, v159, v95\n"
    "v_ldexp_f32 v160, v160, v94\n"
    "v_ldexp_f32 v161, v161, v94\n"
    "v_ldexp_f32 v162, v162, v94\n"
    "v_ldexp_f32 v163, v163, v94\n"
    "v_ldexp_f32 v164, v164, v95\n"
    "v_ldexp_f32 v165, v165, v95\n"
    "v_ldexp_f32 v166, v166, v95\n"
    "v_ldexp_f32 v167, v167, v95\n"
    "v_ldexp_f32 v168, v168, v94\n"
    "v_ldexp_f32 v169, v169, v94\n"
    "v_ldexp_f32 v170, v170, v94\n"
    "v_ldexp_f32 v171, v171, v94\n"
    "v_ldexp_f32 v172, v172, v95\n"
    "v_ldexp_f32 v173, v173, v95\n"
    "v_ldexp_f32 v174, v174, v95\n"
    "v_ldexp_f32 v175, v175, v95\n"
    "v_ldexp_f32 v176, v176, v94\n"
    "v_ldexp_f32 v177, v177, v94\n"
    "v_ldexp_f32 v178, v178, v94\n"
    "v_ldexp_f32 v179, v179, v94\n"
    "v_ldexp_f32 v180, v180, v95\n"
    "v_ldexp_f32 v181, v181, v95\n"
    "v_ldexp_f32 v182, v182, v95\n"
    "v_ldexp_f32 v183, v183, v95\n"
    "v_ldexp_f32 v184, v184, v94\n"
    "v_ldexp_f32 v185, v185, v94\n"
    "v_ldexp_f32 v186, v186, v94\n"
    "v_ldexp_f32 v187, v187, v94\n"
    "v_ldexp_f32 v188, v188, v95\n"
    "v_ldexp_f32 v189, v189, v95\n"
    "v_ldexp_f32 v190, v190, v95\n"
    "v_ldexp_f32 v191, v191, v95\n"
    "v_ldexp_f32 v192, v192, v94\n"
    "v_ldexp_f32 v193, v193, v94\n"
    "v_ldexp_f32 v194, v194, v94\n"
    "v_ldexp_f32 v195, v195, v94\n"
    "v_ldexp_f32 v196, v196, v95\n"
    "v_ldexp_f32 v197, v197, v95\n"
    "v_ldexp_f32 v198, v198, v95\n"
    "v_ldexp_f32 v199, v199, v95\n"
    "v_ldexp_f32 v200, v200, v94\n"
    "v_ldexp_f32 v201, v201, v94\n"
    "v_ldexp_f32 v202, v202, v94\n"
    "v_ldexp_f32 v203, v203, v94\n"
    "v_ldexp_f32 v204, v204, v95\n"
    "v_ldexp_f32 v205, v205, v95\n"
    "v_ldexp_f32 v206, v206, v95\n"
    "v_ldexp_f32 v207, v207, v95\n"
    "v_ldexp_f32 v208, v208, v94\n"
    "v_ldexp_f32 v209, v209, v94\n"
    "v_ldexp_f32 v210, v210, v94\n"
    "v_ldexp_f32 v211, v211, v94\n"
    "v_ldexp_f32 v212, v212, v95\n"
    "v_ldexp_f32 v213, v213, v95\n"
    "v_ldexp_f32 v214, v214, v95\n"
    "v_ldexp_f32 v215, v215, v95\n"
    "v_ldexp_f32 v216, v216, v94\n"
    "v_ldexp_f32 v217, v217, v94\n"
    "v_ldexp_f32 v218, v218, v94\n"
    "v_ldexp_f32 v219, v219, v94\n"
    "v_ldexp_f32 v220, v220, v95\n"
    "v_ldexp_f32 v221, v221, v95\n"
    "v_ldexp_f32 v222, v222, v95\n"
    "v_ldexp_f32 v223, v223, v95\n"
    "v_ldexp_f32 v224, v224, v94\n"
    "v_ldexp_f32 v225, v225, v94\n"
    "v_ldexp_f32 v226, v226, v94\n"
    "v_ldexp_f32 v227, v227, v94\n"
    "v_ldexp_f32 v228, v228, v95\n"
    "v_ldexp_f32 v229, v229, v95\n"
    "v_ldexp_f32 v230, v230, v95\n"
    "v_ldexp_f32 v231, v231, v95\n"
    "v_ldexp_f32 v232, v232, v94\n"
    "v_ldexp_f32 v233, v233, v94\n"
    "v_ldexp_f32 v234, v234, v94\n"
    "v_ldexp_f32 v235, v235, v94\n"
    "v_ldexp_f32 v236, v236, v95\n"
    "v_ldexp_f32 v237, v237, v95\n"
    "v_ldexp_f32 v238, v238, v95\n"
    "v_ldexp_f32 v239, v239, v95\n"
    "v_ldexp_f32 v240, v240, v94\n"
    "v_ldexp_f32 v241, v241, v94\n"
    "v_ldexp_f32 v242, v242, v94\n"
    "v_ldexp_f32 v243, v243, v94\n"
    "v_ldexp_f32 v244, v244, v95\n"
    "v_ldexp_f32 v245, v245, v95\n"
    "v_ldexp_f32 v246, v246, v95\n"
    "v_ldexp_f32 v247, v247, v95\n"
    "v_ldexp_f32 v248, v248, v94\n"
    "v_ldexp_f32 v249, v249, v94\n"
    "v_ldexp_f32 v250, v250, v94\n"
    "v_ldexp_f32 v251, v251, v94\n"
    "v_ldexp_f32 v252, v252, v95\n"
    "v_ldexp_f32 v253, v253, v95\n"
    "v_ldexp_f32 v254, v254, v95\n"
    "v_ldexp_f32 v255, v255, v95\n"
    "s_nop 1\n"
    "s_branch LB66_%=\n"
    "LR67_%=:\n"
    "s_nop 8\n"
    "v_max3_f32 v82, v32, v33, v34\n"
    "v_max3_f32 v83, v40, v41, v42\n"
    "v_max3_f32 v82, v82, v35, v36\n"
    "v_max3_f32 v83, v83, v43, v44\n"
    "v_max3_f32 v82, v82, v37, v38\n"
    "v_max3_f32 v83, v83, v45, v46\n"
    "v_max_f32 v82, v82, v39\n"
    "v_max_f32 v83, v83, v47\n"
    "s_nop 1\n"
    "v_mov_b32 v92, v82\n"
    "v_mov_b32 v93, v83\n"
    "s_nop 1\n"
    "v_permlane16_swap_b32 v82, v92\n"
    "v_permlane16_swap_b32 v83, v93\n"
    "s_nop 1\n"
    "v_max_f32 v82, v82, v92\n"
    "v_max_f32 v83, v83, v93\n"
    "v_mov_b32 v92, v82\n"
    "v_mov_b32 v93, v83\n"
    "s_nop 1\n"
    "v_permlane32_swap_b32 v82, v92\n"
    "v_permlane32_swap_b32 v83, v93\n"
    "s_nop 1\n"
    "v_max_f32 v92, v82, v92\n"
    "v_max_f32 v93, v83, v93\n"
    "v_add_f32 v92, 0x42c80000, v92\n"
    "v_ceil_f32 v92, v92\n"
    "v_max_f32 v92, 0, v92\n"
    "v_add_f32 v93, 0x42c80000, v93\n"
    "v_ceil_f32 v93, v93\n"
    "v_max_f32 v93, 0, v93\n"
    "v_sub_f32 v32, v32, v92\n"
    "v_sub_f32 v33, v33, v92\n"
    "v_sub_f32 v34, v34, v92\n"
    "v_sub_f32 v35, v35, v92\n"
    "v_sub_f32 v36, v36, v92\n"
    "v_sub_f32 v37, v37, v92\n"
    "v_sub_f32 v38, v38, v92\n"
    "v_sub_f32 v39, v39, v92\n"
    "v_sub_f32 v40, v40, v93\n"
    "v_sub_f32 v41, v41, v93\n"
    "v_sub_f32 v42, v42, v93\n"
    "v_sub_f32 v43, v43, v93\n"
    "v_sub_f32 v44, v44, v93\n"
    "v_sub_f32 v45, v45, v93\n"
    "v_sub_f32 v46, v46, v93\n"
    "v_sub_f32 v47, v47, v93\n"
    "v_sub_f32 v64, v64, v92\n"
    "v_sub_f32 v65, v65, v92\n"
    "v_sub_f32 v66, v66, v92\n"
    "v_sub_f32 v67, v67, v92\n"
    "v_sub_f32 v68, v68, v93\n"
    "v_sub_f32 v69, v69, v93\n"
    "v_sub_f32 v70, v70, v93\n"
    "v_sub_f32 v71, v71, v93\n"
    "v_sub_f32 v94, 0, v92\n"
    "v_sub_f32 v95, 0, v93\n"
    "v_cvt_i32_f32 v94, v94\n"
    "v_cvt_i32_f32 v95, v95\n"
    "v_ldexp_f32 v80, v80, v94\n"
    "v_ldexp_f32 v81, v81, v95\n"
    "v_ldexp_f32 v128, v128, v94\n"
    "v_ldexp_f32 v129, v129, v94\n"
    "v_ldexp_f32 v130, v130, v94\n"
    "v_ldexp_f32 v131, v131, v94\n"
    "v_ldexp_f32 v132, v132, v95\n"
    "v_ldexp_f32 v133, v133, v95\n"
    "v_ldexp_f32 v134, v134, v95\n"
    "v_ldexp_f32 v135, v135, v95\n"
    "v_ldexp_f32 v136, v136, v94\n"
    "v_ldexp_f32 v137, v137, v94\n"
    "v_ldexp_f32 v138, v138, v94\n"
    "v_ldexp_f32 v139, v139, v94\n"
    "v_ldexp_f32 v140, v140, v95\n"
    "v_ldexp_f32 v141, v141, v95\n"
    "v_ldexp_f32 v142, v142, v95\n"
    "v_ldexp_f32 v143, v143, v95\n"
    "v_ldexp_f32 v144, v144, v94\n"
    "v_ldexp_f32 v145, v145, v94\n"
    "v_ldexp_f32 v146, v146, v94\n"
    "v_ldexp_f32 v147, v147, v94\n"
    "v_ldexp_f32 v148, v148, v95\n"
    "v_ldexp_f32 v149, v149, v95\n"
    "v_ldexp_f32 v150, v150, v95\n"
    "v_ldexp_f32 v151, v151, v95\n"
    "v_ldexp_f32 v152, v152, v94\n"
    "v_ldexp_f32 v153, v153, v94\n"
    "v_ldexp_f32 v154, v154, v94\n"
    "v_ldexp_f32 v155, v155, v94\n"
    "v_ldexp_f32 v156, v156, v95\n"
    "v_ldexp_f32 v157, v157, v95\n"
    "v_ldexp_f32 v158, v158, v95\n"
    "v_ldexp_f32 v159, v159, v95\n"
    "v_ldexp_f32 v160, v160, v94\n"
    "v_ldexp_f32 v161, v161, v94\n"
    "v_ldexp_f32 v162, v162, v94\n"
    "v_ldexp_f32 v163, v163, v94\n"
    "v_ldexp_f32 v164, v164, v95\n"
    "v_ldexp_f32 v165, v165, v95\n"
    "v_ldexp_f32 v166, v166, v95\n"
    "v_ldexp_f32 v167, v167, v95\n"
    "v_ldexp_f32 v168, v168, v94\n"
    "v_ldexp_f32 v169, v169, v94\n"
    "v_ldexp_f32 v170, v170, v94\n"
    "v_ldexp_f32 v171, v171, v94\n"
    "v_ldexp_f32 v172, v172, v95\n"
    "v_ldexp_f32 v173, v173, v95\n"
    "v_ldexp_f32 v174, v174, v95\n"
    "v_ldexp_f32 v175, v175, v95\n"
    "v_ldexp_f32 v176, v176, v94\n"
    "v_ldexp_f32 v177, v177, v94\n"
    "v_ldexp_f32 v178, v178, v94\n"
    "v_ldexp_f32 v179, v179, v94\n"
    "v_ldexp_f32 v180, v180, v95\n"
    "v_ldexp_f32 v181, v181, v95\n"
    "v_ldexp_f32 v182, v182, v95\n"
    "v_ldexp_f32 v183, v183, v95\n"
    "v_ldexp_f32 v184, v184, v94\n"
    "v_ldexp_f32 v185, v185, v94\n"
    "v_ldexp_f32 v186, v186, v94\n"
    "v_ldexp_f32 v187, v187, v94\n"
    "v_ldexp_f32 v188, v188, v95\n"
    "v_ldexp_f32 v189, v189, v95\n"
    "v_ldexp_f32 v190, v190, v95\n"
    "v_ldexp_f32 v191, v191, v95\n"
    "v_ldexp_f32 v192, v192, v94\n"
    "v_ldexp_f32 v193, v193, v94\n"
    "v_ldexp_f32 v194, v194, v94\n"
    "v_ldexp_f32 v195, v195, v94\n"
    "v_ldexp_f32 v196, v196, v95\n"
    "v_ldexp_f32 v197, v197, v95\n"
    "v_ldexp_f32 v198, v198, v95\n"
    "v_ldexp_f32 v199, v199, v95\n"
    "v_ldexp_f32 v200, v200, v94\n"
    "v_ldexp_f32 v201, v201, v94\n"
    "v_ldexp_f32 v202, v202, v94\n"
    "v_ldexp_f32 v203, v203, v94\n"
    "v_ldexp_f32 v204, v204, v95\n"
    "v_ldexp_f32 v205, v205, v95\n"
    "v_ldexp_f32 v206, v206, v95\n"
    "v_ldexp_f32 v207, v207, v95\n"
    "v_ldexp_f32 v208, v208, v94\n"
    "v_ldexp_f32 v209, v209, v94\n"
    "v_ldexp_f32 v210, v210, v94\n"
    "v_ldexp_f32 v211, v211, v94\n"
    "v_ldexp_f32 v212, v212, v95\n"
    "v_ldexp_f32 v213, v213, v95\n"
    "v_ldexp_f32 v214, v214, v95\n"
    "v_ldexp_f32 v215, v215, v95\n"
    "v_ldexp_f32 v216, v216, v94\n"
    "v_ldexp_f32 v217, v217, v94\n"
    "v_ldexp_f32 v218, v218, v94\n"
    "v_ldexp_f32 v219, v219, v94\n"
    "v_ldexp_f32 v220, v220, v95\n"
    "v_ldexp_f32 v221, v221, v95\n"
    "v_ldexp_f32 v222, v222, v95\n"
    "v_ldexp_f32 v223, v223, v95\n"
    "v_ldexp_f32 v224, v224, v94\n"
    "v_ldexp_f32 v225, v225, v94\n"
    "v_ldexp_f32 v226, v226, v94\n"
    "v_ldexp_f32 v227, v227, v94\n"
    "v_ldexp_f32 v228, v228, v95\n"
    "v_ldexp_f32 v229, v229, v95\n"
    "v_ldexp_f32 v230, v230, v95\n"
    "v_ldexp_f32 v231, v231, v95\n"
    "v_ldexp_f32 v232, v232, v94\n"
    "v_ldexp_f32 v233, v233, v94\n"
    "v_ldexp_f32 v234, v234, v94\n"
    "v_ldexp_f32 v235, v235, v94\n"
    "v_ldexp_f32 v236, v236, v95\n"
    "v_ldexp_f32 v237, v237, v95\n"
    "v_ldexp_f32 v238, v238, v95\n"
    "v_ldexp_f32 v239, v239, v95\n"
    "v_ldexp_f32 v240, v240, v94\n"
    "v_ldexp_f32 v241, v241, v94\n"
    "v_ldexp_f32 v242, v242, v94\n"
    "v_ldexp_f32 v243, v243, v94\n"
    "v_ldexp_f32 v244, v244, v95\n"
    "v_ldexp_f32 v245, v245, v95\n"
    "v_ldexp_f32 v246, v246, v95\n"
    "v_ldexp_f32 v247, v247, v95\n"
    "v_ldexp_f32 v248, v248, v94\n"
    "v_ldexp_f32 v249, v249, v94\n"
    "v_ldexp_f32 v250, v250, v94\n"
    "v_ldexp_f32 v251, v251, v94\n"
    "v_ldexp_f32 v252, v252, v95\n"
    "v_ldexp_f32 v253, v253, v95\n"
    "v_ldexp_f32 v254, v254, v95\n"
    "v_ldexp_f32 v255, v255, v95\n"
    "s_nop 1\n"
    "s_branch LB67_%=\n"
    "LR68_%=:\n"
    "s_nop 8\n"
    "v_max3_f32 v82, v48, v49, v50\n"
    "v_max3_f32 v83, v56, v57, v58\n"
    "v_max3_f32 v82, v82, v51, v52\n"
    "v_max3_f32 v83, v83, v59, v60\n"
    "v_max3_f32 v82, v82, v53, v54\n"
    "v_max3_f32 v83, v83, v61, v62\n"
    "v_max_f32 v82, v82, v55\n"
    "v_max_f32 v83, v83, v63\n"
    "s_nop 1\n"
    "v_mov_b32 v92, v82\n"
    "v_mov_b32 v93, v83\n"
    "s_nop 1\n"
    "v_permlane16_swap_b32 v82, v92\n"
    "v_permlane16_swap_b32 v83, v93\n"
    "s_nop 1\n"
    "v_max_f32 v82, v82, v92\n"
    "v_max_f32 v83, v83, v93\n"
    "v_mov_b32 v92, v82\n"
    "v_mov_b32 v93, v83\n"
    "s_nop 1\n"
    "v_permlane32_swap_b32 v82, v92\n"
    "v_permlane32_swap_b32 v83, v93\n"
    "s_nop 1\n"
    "v_max_f32 v92, v82, v92\n"
    "v_max_f32 v93, v83, v93\n"
    "v_add_f32 v92, 0x42c80000, v92\n"
    "v_ceil_f32 v92, v92\n"
    "v_max_f32 v92, 0, v92\n"
    "v_add_f32 v93, 0x42c80000, v93\n"
    "v_ceil_f32 v93, v93\n"
    "v_max_f32 v93, 0, v93\n"
    "v_sub_f32 v48, v48, v92\n"
    "v_sub_f32 v49, v49, v92\n"
    "v_sub_f32 v50, v50, v92\n"
    "v_sub_f32 v51, v51, v92\n"
    "v_sub_f32 v52, v52, v92\n"
    "v_sub_f32 v53, v53, v92\n"
    "v_sub_f32 v54, v54, v92\n"
    "v_sub_f32 v55, v55, v92\n"
    "v_sub_f32 v56, v56, v93\n"
    "v_sub_f32 v57, v57, v93\n"
    "v_sub_f32 v58, v58, v93\n"
    "v_sub_f32 v59, v59, v93\n"
    "v_sub_f32 v60, v60, v93\n"
    "v_sub_f32 v61, v61, v93\n"
    "v_sub_f32 v62, v62, v93\n"
    "v_sub_f32 v63, v63, v93\n"
    "v_sub_f32 v64, v64, v92\n"
    "v_sub_f32 v65, v65, v92\n"
    "v_sub_f32 v66, v66, v92\n"
    "v_sub_f32 v67, v67, v92\n"
    "v_sub_f32 v68, v68, v93\n"
    "v_sub_f32 v69, v69, v93\n"
    "v_sub_f32 v70, v70, v93\n"
    "v_sub_f32 v71, v71, v93\n"
    "v_sub_f32 v94, 0, v92\n"
    "v_sub_f32 v95, 0, v93\n"
    "v_cvt_i32_f32 v94, v94\n"
    "v_cvt_i32_f32 v95, v95\n"
    "v_ldexp_f32 v80, v80, v94\n"
    "v_ldexp_f32 v81, v81, v95\n"
    "v_ldexp_f32 v128, v128, v94\n"
    "v_ldexp_f32 v129, v129, v94\n"
    "v_ldexp_f32 v130, v130, v94\n"
    "v_ldexp_f32 v131, v131, v94\n"
    "v_ldexp_f32 v132, v132, v95\n"
    "v_ldexp_f32 v133, v133, v95\n"
    "v_ldexp_f32 v134, v134, v95\n"
    "v_ldexp_f32 v135, v135, v95\n"
    "v_ldexp_f32 v136, v136, v94\n"
    "v_ldexp_f32 v137, v137, v94\n"
    "v_ldexp_f32 v138, v138, v94\n"
    "v_ldexp_f32 v139, v139, v94\n"
    "v_ldexp_f32 v140, v140, v95\n"
    "v_ldexp_f32 v141, v141, v95\n"
    "v_ldexp_f32 v142, v142, v95\n"
    "v_ldexp_f32 v143, v143, v95\n"
    "v_ldexp_f32 v144, v144, v94\n"
    "v_ldexp_f32 v145, v145, v94\n"
    "v_ldexp_f32 v146, v146, v94\n"
    "v_ldexp_f32 v147, v147, v94\n"
    "v_ldexp_f32 v148, v148, v95\n"
    "v_ldexp_f32 v149, v149, v95\n"
    "v_ldexp_f32 v150, v150, v95\n"
    "v_ldexp_f32 v151, v151, v95\n"
    "v_ldexp_f32 v152, v152, v94\n"
    "v_ldexp_f32 v153, v153, v94\n"
    "v_ldexp_f32 v154, v154, v94\n"
    "v_ldexp_f32 v155, v155, v94\n"
    "v_ldexp_f32 v156, v156, v95\n"
    "v_ldexp_f32 v157, v157, v95\n"
    "v_ldexp_f32 v158, v158, v95\n"
    "v_ldexp_f32 v159, v159, v95\n"
    "v_ldexp_f32 v160, v160, v94\n"
    "v_ldexp_f32 v161, v161, v94\n"
    "v_ldexp_f32 v162, v162, v94\n"
    "v_ldexp_f32 v163, v163, v94\n"
    "v_ldexp_f32 v164, v164, v95\n"
    "v_ldexp_f32 v165, v165, v95\n"
    "v_ldexp_f32 v166, v166, v95\n"
    "v_ldexp_f32 v167, v167, v95\n"
    "v_ldexp_f32 v168, v168, v94\n"
    "v_ldexp_f32 v169, v169, v94\n"
    "v_ldexp_f32 v170, v170, v94\n"
    "v_ldexp_f32 v171, v171, v94\n"
    "v_ldexp_f32 v172, v172, v95\n"
    "v_ldexp_f32 v173, v173, v95\n"
    "v_ldexp_f32 v174, v174, v95\n"
    "v_ldexp_f32 v175, v175, v95\n"
    "v_ldexp_f32 v176, v176, v94\n"
    "v_ldexp_f32 v177, v177, v94\n"
    "v_ldexp_f32 v178, v178, v94\n"
    "v_ldexp_f32 v179, v179, v94\n"
    "v_ldexp_f32 v180, v180, v95\n"
    "v_ldexp_f32 v181, v181, v95\n"
    "v_ldexp_f32 v182, v182, v95\n"
    "v_ldexp_f32 v183, v183, v95\n"
    "v_ldexp_f32 v184, v184, v94\n"
    "v_ldexp_f32 v185, v185, v94\n"
    "v_ldexp_f32 v186, v186, v94\n"
    "v_ldexp_f32 v187, v187, v94\n"
    "v_ldexp_f32 v188, v188, v95\n"
    "v_ldexp_f32 v189, v189, v95\n"
    "v_ldexp_f32 v190, v190, v95\n"
    "v_ldexp_f32 v191, v191, v95\n"
    "v_ldexp_f32 v192, v192, v94\n"
    "v_ldexp_f32 v193, v193, v94\n"
    "v_ldexp_f32 v194, v194, v94\n"
    "v_ldexp_f32 v195, v195, v94\n"
    "v_ldexp_f32 v196, v196, v95\n"
    "v_ldexp_f32 v197, v197, v95\n"
    "v_ldexp_f32 v198, v198, v95\n"
    "v_ldexp_f32 v199, v199, v95\n"
    "v_ldexp_f32 v200, v200, v94\n"
    "v_ldexp_f32 v201, v201, v94\n"
    "v_ldexp_f32 v202, v202, v94\n"
    "v_ldexp_f32 v203, v203, v94\n"
    "v_ldexp_f32 v204, v204, v95\n"
    "v_ldexp_f32 v205, v205, v95\n"
    "v_ldexp_f32 v206, v206, v95\n"
    "v_ldexp_f32 v207, v207, v95\n"
    "v_ldexp_f32 v208, v208, v94\n"
    "v_ldexp_f32 v209, v209, v94\n"
    "v_ldexp_f32 v210, v210, v94\n"
    "v_ldexp_f32 v211, v211, v94\n"
    "v_ldexp_f32 v212, v212, v95\n"
    "v_ldexp_f32 v213, v213, v95\n"
    "v_ldexp_f32 v214, v214, v95\n"
    "v_ldexp_f32 v215, v215, v95\n"
    "v_ldexp_f32 v216, v216, v94\n"
    "v_ldexp_f32 v217, v217, v94\n"
    "v_ldexp_f32 v218, v218, v94\n"
    "v_ldexp_f32 v219, v219, v94\n"
    "v_ldexp_f32 v220, v220, v95\n"
    "v_ldexp_f32 v221, v221, v95\n"
    "v_ldexp_f32 v222, v222, v95\n"
    "v_ldexp_f32 v223, v223, v95\n"
    "v_ldexp_f32 v224, v224, v94\n"
    "v_ldexp_f32 v225, v225, v94\n"
    "v_ldexp_f32 v226, v226, v94\n"
    "v_ldexp_f32 v227, v227, v94\n"
    "v_ldexp_f32 v228, v228, v95\n"
    "v_ldexp_f32 v229, v229, v95\n"
    "v_ldexp_f32 v230, v230, v95\n"
    "v_ldexp_f32 v231, v231, v95\n"
    "v_ldexp_f32 v232, v232, v94\n"
    "v_ldexp_f32 v233, v233, v94\n"
    "v_ldexp_f32 v234, v234, v94\n"
    "v_ldexp_f32 v235, v235, v94\n"
    "v_ldexp_f32 v236, v236, v95\n"
    "v_ldexp_f32 v237, v237, v95\n"
    "v_ldexp_f32 v238, v238, v95\n"
    "v_ldexp_f32 v239, v239, v95\n"
    "v_ldexp_f32 v240, v240, v94\n"
    "v_ldexp_f32 v241, v241, v94\n"
    "v_ldexp_f32 v242, v242, v94\n"
    "v_ldexp_f32 v243, v243, v94\n"
    "v_ldexp_f32 v244, v244, v95\n"
    "v_ldexp_f32 v245, v245, v95\n"
    "v_ldexp_f32 v246, v246, v95\n"
    "v_ldexp_f32 v247, v247, v95\n"
    "v_ldexp_f32 v248, v248, v94\n"
    "v_ldexp_f32 v249, v249, v94\n"
    "v_ldexp_f32 v250, v250, v94\n"
    "v_ldexp_f32 v251, v251, v94\n"
    "v_ldexp_f32 v252, v252, v95\n"
    "v_ldexp_f32 v253, v253, v95\n"
    "v_ldexp_f32 v254, v254, v95\n"
    "v_ldexp_f32 v255, v255, v95\n"
    "s_nop 1\n"
    "s_branch LB68_%=\n"
    "LR69_%=:\n"
    "s_nop 8\n"
    "v_max3_f32 v82, v32, v33, v34\n"
    "v_max3_f32 v83, v40, v41, v42\n"
    "v_max3_f32 v82, v82, v35, v36\n"
    "v_max3_f32 v83, v83, v43, v44\n"
    "v_max3_f32 v82, v82, v37, v38\n"
    "v_max3_f32 v83, v83, v45, v46\n"
    "v_max_f32 v82, v82, v39\n"
    "v_max_f32 v83, v83, v47\n"
    "s_nop 1\n"
    "v_mov_b32 v92, v82\n"
    "v_mov_b32 v93, v83\n"
    "s_nop 1\n"
    "v_permlane16_swap_b32 v82, v92\n"
    "v_permlane16_swap_b32 v83, v93\n"
    "s_nop 1\n"
    "v_max_f32 v82, v82, v92\n"
    "v_max_f32 v83, v83, v93\n"
    "v_mov_b32 v92, v82\n"
    "v_mov_b32 v93, v83\n"
    "s_nop 1\n"
    "v_permlane32_swap_b32 v82, v92\n"
    "v_permlane32_swap_b32 v83, v93\n"
    "s_nop 1\n"
    "v_max_f32 v92, v82, v92\n"
    "v_max_f32 v93, v83, v93\n"
    "v_add_f32 v92, 0x42c80000, v92\n"
    "v_ceil_f32 v92, v92\n"
    "v_max_f32 v92, 0, v92\n"
    "v_add_f32 v93, 0x42c80000, v93\n"
    "v_ceil_f32 v93, v93\n"
    "v_max_f32 v93, 0, v93\n"
    "v_sub_f32 v32, v32, v92\n"
    "v_sub_f32 v33, v33, v92\n"
    "v_sub_f32 v34, v34, v92\n"
    "v_sub_f32 v35, v35, v92\n"
    "v_sub_f32 v36, v36, v92\n"
    "v_sub_f32 v37, v37, v92\n"
    "v_sub_f32 v38, v38, v92\n"
    "v_sub_f32 v39, v39, v92\n"
    "v_sub_f32 v40, v40, v93\n"
    "v_sub_f32 v41, v41, v93\n"
    "v_sub_f32 v42, v42, v93\n"
    "v_sub_f32 v43, v43, v93\n"
    "v_sub_f32 v44, v44, v93\n"
    "v_sub_f32 v45, v45, v93\n"
    "v_sub_f32 v46, v46, v93\n"
    "v_sub_f32 v47, v47, v93\n"
    "v_sub_f32 v64, v64, v92\n"
    "v_sub_f32 v65, v65, v92\n"
    "v_sub_f32 v66, v66, v92\n"
    "v_sub_f32 v67, v67, v92\n"
    "v_sub_f32 v68, v68, v93\n"
    "v_sub_f32 v69, v69, v93\n"
    "v_sub_f32 v70, v70, v93\n"
    "v_sub_f32 v71, v71, v93\n"
    "v_sub_f32 v94, 0, v92\n"
    "v_sub_f32 v95, 0, v93\n"
    "v_cvt_i32_f32 v94, v94\n"
    "v_cvt_i32_f32 v95, v95\n"
    "v_ldexp_f32 v80, v80, v94\n"
    "v_ldexp_f32 v81, v81, v95\n"
    "v_ldexp_f32 v128, v128, v94\n"
    "v_ldexp_f32 v129, v129, v94\n"
    "v_ldexp_f32 v130, v130, v94\n"
    "v_ldexp_f32 v131, v131, v94\n"
    "v_ldexp_f32 v132, v132, v95\n"
    "v_ldexp_f32 v133, v133, v95\n"
    "v_ldexp_f32 v134, v134, v95\n"
    "v_ldexp_f32 v135, v135, v95\n"
    "v_ldexp_f32 v136, v136, v94\n"
    "v_ldexp_f32 v137, v137, v94\n"
    "v_ldexp_f32 v138, v138, v94\n"
    "v_ldexp_f32 v139, v139, v94\n"
    "v_ldexp_f32 v140, v140, v95\n"
    "v_ldexp_f32 v141, v141, v95\n"
    "v_ldexp_f32 v142, v142, v95\n"
    "v_ldexp_f32 v143, v143, v95\n"
    "v_ldexp_f32 v144, v144, v94\n"
    "v_ldexp_f32 v145, v145, v94\n"
    "v_ldexp_f32 v146, v146, v94\n"
    "v_ldexp_f32 v147, v147, v94\n"
    "v_ldexp_f32 v148, v148, v95\n"
    "v_ldexp_f32 v149, v149, v95\n"
    "v_ldexp_f32 v150, v150, v95\n"
    "v_ldexp_f32 v151, v151, v95\n"
    "v_ldexp_f32 v152, v152, v94\n"
    "v_ldexp_f32 v153, v153, v94\n"
    "v_ldexp_f32 v154, v154, v94\n"
    "v_ldexp_f32 v155, v155, v94\n"
    "v_ldexp_f32 v156, v156, v95\n"
    "v_ldexp_f32 v157, v157, v95\n"
    "v_ldexp_f32 v158, v158, v95\n"
    "v_ldexp_f32 v159, v159, v95\n"
    "v_ldexp_f32 v160, v160, v94\n"
    "v_ldexp_f32 v161, v161, v94\n"
    "v_ldexp_f32 v162, v162, v94\n"
    "v_ldexp_f32 v163, v163, v94\n"
    "v_ldexp_f32 v164, v164, v95\n"
    "v_ldexp_f32 v165, v165, v95\n"
    "v_ldexp_f32 v166, v166, v95\n"
    "v_ldexp_f32 v167, v167, v95\n"
    "v_ldexp_f32 v168, v168, v94\n"
    "v_ldexp_f32 v169, v169, v94\n"
    "v_ldexp_f32 v170, v170, v94\n"
    "v_ldexp_f32 v171, v171, v94\n"
    "v_ldexp_f32 v172, v172, v95\n"
    "v_ldexp_f32 v173, v173, v95\n"
    "v_ldexp_f32 v174, v174, v95\n"
    "v_ldexp_f32 v175, v175, v95\n"
    "v_ldexp_f32 v176, v176, v94\n"
    "v_ldexp_f32 v177, v177, v94\n"
    "v_ldexp_f32 v178, v178, v94\n"
    "v_ldexp_f32 v179, v179, v94\n"
    "v_ldexp_f32 v180, v180, v95\n"
    "v_ldexp_f32 v181, v181, v95\n"
    "v_ldexp_f32 v182, v182, v95\n"
    "v_ldexp_f32 v183, v183, v95\n"
    "v_ldexp_f32 v184, v184, v94\n"
    "v_ldexp_f32 v185, v185, v94\n"
    "v_ldexp_f32 v186, v186, v94\n"
    "v_ldexp_f32 v187, v187, v94\n"
    "v_ldexp_f32 v188, v188, v95\n"
    "v_ldexp_f32 v189, v189, v95\n"
    "v_ldexp_f32 v190, v190, v95\n"
    "v_ldexp_f32 v191, v191, v95\n"
    "v_ldexp_f32 v192, v192, v94\n"
    "v_ldexp_f32 v193, v193, v94\n"
    "v_ldexp_f32 v194, v194, v94\n"
    "v_ldexp_f32 v195, v195, v94\n"
    "v_ldexp_f32 v196, v196, v95\n"
    "v_ldexp_f32 v197, v197, v95\n"
    "v_ldexp_f32 v198, v198, v95\n"
    "v_ldexp_f32 v199, v199, v95\n"
    "v_ldexp_f32 v200, v200, v94\n"
    "v_ldexp_f32 v201, v201, v94\n"
    "v_ldexp_f32 v202, v202, v94\n"
    "v_ldexp_f32 v203, v203, v94\n"
    "v_ldexp_f32 v204, v204, v95\n"
    "v_ldexp_f32 v205, v205, v95\n"
    "v_ldexp_f32 v206, v206, v95\n"
    "v_ldexp_f32 v207, v207, v95\n"
    "v_ldexp_f32 v208, v208, v94\n"
    "v_ldexp_f32 v209, v209, v94\n"
    "v_ldexp_f32 v210, v210, v94\n"
    "v_ldexp_f32 v211, v211, v94\n"
    "v_ldexp_f32 v212, v212, v95\n"
    "v_ldexp_f32 v213, v213, v95\n"
    "v_ldexp_f32 v214, v214, v95\n"
    "v_ldexp_f32 v215, v215, v95\n"
    "v_ldexp_f32 v216, v216, v94\n"
    "v_ldexp_f32 v217, v217, v94\n"
    "v_ldexp_f32 v218, v218, v94\n"
    "v_ldexp_f32 v219, v219, v94\n"
    "v_ldexp_f32 v220, v220, v95\n"
    "v_ldexp_f32 v221, v221, v95\n"
    "v_ldexp_f32 v222, v222, v95\n"
    "v_ldexp_f32 v223, v223, v95\n"
    "v_ldexp_f32 v224, v224, v94\n"
    "v_ldexp_f32 v225, v225, v94\n"
    "v_ldexp_f32 v226, v226, v94\n"
    "v_ldexp_f32 v227, v227, v94\n"
    "v_ldexp_f32 v228, v228, v95\n"
    "v_ldexp_f32 v229, v229, v95\n"
    "v_ldexp_f32 v230, v230, v95\n"
    "v_ldexp_f32 v231, v231, v95\n"
    "v_ldexp_f32 v232, v232, v94\n"
    "v_ldexp_f32 v233, v233, v94\n"
    "v_ldexp_f32 v234, v234, v94\n"
    "v_ldexp_f32 v235, v235, v94\n"
    "v_ldexp_f32 v236, v236, v95\n"
    "v_ldexp_f32 v237, v237, v95\n"
    "v_ldexp_f32 v238, v238, v95\n"
    "v_ldexp_f32 v239, v239, v95\n"
    "v_ldexp_f32 v240, v240, v94\n"
    "v_ldexp_f32 v241, v241, v94\n"
    "v_ldexp_f32 v242, v242, v94\n"
    "v_ldexp_f32 v243, v243, v94\n"
    "v_ldexp_f32 v244, v244, v95\n"
    "v_ldexp_f32 v245, v245, v95\n"
    "v_ldexp_f32 v246, v246, v95\n"
    "v_ldexp_f32 v247, v247, v95\n"
    "v_ldexp_f32 v248, v248, v94\n"
    "v_ldexp_f32 v249, v249, v94\n"
    "v_ldexp_f32 v250, v250, v94\n"
    "v_ldexp_f32 v251, v251, v94\n"
    "v_ldexp_f32 v252, v252, v95\n"
    "v_ldexp_f32 v253, v253, v95\n"
    "v_ldexp_f32 v254, v254, v95\n"
    "v_ldexp_f32 v255, v255, v95\n"
    "s_nop 1\n"
    "s_branch LB69_%=\n"
    "LR70_%=:\n"
    "s_nop 8\n"
    "v_max3_f32 v82, v48, v49, v50\n"
    "v_max3_f32 v83, v56, v57, v58\n"
    "v_max3_f32 v82, v82, v51, v52\n"
    "v_max3_f32 v83, v83, v59, v60\n"
    "v_max3_f32 v82, v82, v53, v54\n"
    "v_max3_f32 v83, v83, v61, v62\n"
    "v_max_f32 v82, v82, v55\n"
    "v_max_f32 v83, v83, v63\n"
    "s_nop 1\n"
    "v_mov_b32 v92, v82\n"
    "v_mov_b32 v93, v83\n"
    "s_nop 1\n"
    "v_permlane16_swap_b32 v82, v92\n"
    "v_permlane16_swap_b32 v83, v93\n"
    "s_nop 1\n"
    "v_max_f32 v82, v82, v92\n"
    "v_max_f32 v83, v83, v93\n"
    "v_mov_b32 v92, v82\n"
    "v_mov_b32 v93, v83\n"
    "s_nop 1\n"
    "v_permlane32_swap_b32 v82, v92\n"
    "v_permlane32_swap_b32 v83, v93\n"
    "s_nop 1\n"
    "v_max_f32 v92, v82, v92\n"
    "v_max_f32 v93, v83, v93\n"
    "v_add_f32 v92, 0x42c80000, v92\n"
    "v_ceil_f32 v92, v92\n"
    "v_max_f32 v92, 0, v92\n"
    "v_add_f32 v93, 0x42c80000, v93\n"
    "v_ceil_f32 v93, v93\n"
    "v_max_f32 v93, 0, v93\n"
    "v_sub_f32 v48, v48, v92\n"
    "v_sub_f32 v49, v49, v92\n"
    "v_sub_f32 v50, v50, v92\n"
    "v_sub_f32 v51, v51, v92\n"
    "v_sub_f32 v52, v52, v92\n"
    "v_sub_f32 v53, v53, v92\n"
    "v_sub_f32 v54, v54, v92\n"
    "v_sub_f32 v55, v55, v92\n"
    "v_sub_f32 v56, v56, v93\n"
    "v_sub_f32 v57, v57, v93\n"
    "v_sub_f32 v58, v58, v93\n"
    "v_sub_f32 v59, v59, v93\n"
    "v_sub_f32 v60, v60, v93\n"
    "v_sub_f32 v61, v61, v93\n"
    "v_sub_f32 v62, v62, v93\n"
    "v_sub_f32 v63, v63, v93\n"
    "v_sub_f32 v64, v64, v92\n"
    "v_sub_f32 v65, v65, v92\n"
    "v_sub_f32 v66, v66, v92\n"
    "v_sub_f32 v67, v67, v92\n"
    "v_sub_f32 v68, v68, v93\n"
    "v_sub_f32 v69, v69, v93\n"
    "v_sub_f32 v70, v70, v93\n"
    "v_sub_f32 v71, v71, v93\n"
    "v_sub_f32 v94, 0, v92\n"
    "v_sub_f32 v95, 0, v93\n"
    "v_cvt_i32_f32 v94, v94\n"
    "v_cvt_i32_f32 v95, v95\n"
    "v_ldexp_f32 v80, v80, v94\n"
    "v_ldexp_f32 v81, v81, v95\n"
    "v_ldexp_f32 v128, v128, v94\n"
    "v_ldexp_f32 v129, v129, v94\n"
    "v_ldexp_f32 v130, v130, v94\n"
    "v_ldexp_f32 v131, v131, v94\n"
    "v_ldexp_f32 v132, v132, v95\n"
    "v_ldexp_f32 v133, v133, v95\n"
    "v_ldexp_f32 v134, v134, v95\n"
    "v_ldexp_f32 v135, v135, v95\n"
    "v_ldexp_f32 v136, v136, v94\n"
    "v_ldexp_f32 v137, v137, v94\n"
    "v_ldexp_f32 v138, v138, v94\n"
    "v_ldexp_f32 v139, v139, v94\n"
    "v_ldexp_f32 v140, v140, v95\n"
    "v_ldexp_f32 v141, v141, v95\n"
    "v_ldexp_f32 v142, v142, v95\n"
    "v_ldexp_f32 v143, v143, v95\n"
    "v_ldexp_f32 v144, v144, v94\n"
    "v_ldexp_f32 v145, v145, v94\n"
    "v_ldexp_f32 v146, v146, v94\n"
    "v_ldexp_f32 v147, v147, v94\n"
    "v_ldexp_f32 v148, v148, v95\n"
    "v_ldexp_f32 v149, v149, v95\n"
    "v_ldexp_f32 v150, v150, v95\n"
    "v_ldexp_f32 v151, v151, v95\n"
    "v_ldexp_f32 v152, v152, v94\n"
    "v_ldexp_f32 v153, v153, v94\n"
    "v_ldexp_f32 v154, v154, v94\n"
    "v_ldexp_f32 v155, v155, v94\n"
    "v_ldexp_f32 v156, v156, v95\n"
    "v_ldexp_f32 v157, v157, v95\n"
    "v_ldexp_f32 v158, v158, v95\n"
    "v_ldexp_f32 v159, v159, v95\n"
    "v_ldexp_f32 v160, v160, v94\n"
    "v_ldexp_f32 v161, v161, v94\n"
    "v_ldexp_f32 v162, v162, v94\n"
    "v_ldexp_f32 v163, v163, v94\n"
    "v_ldexp_f32 v164, v164, v95\n"
    "v_ldexp_f32 v165, v165, v95\n"
    "v_ldexp_f32 v166, v166, v95\n"
    "v_ldexp_f32 v167, v167, v95\n"
    "v_ldexp_f32 v168, v168, v94\n"
    "v_ldexp_f32 v169, v169, v94\n"
    "v_ldexp_f32 v170, v170, v94\n"
    "v_ldexp_f32 v171, v171, v94\n"
    "v_ldexp_f32 v172, v172, v95\n"
    "v_ldexp_f32 v173, v173, v95\n"
    "v_ldexp_f32 v174, v174, v95\n"
    "v_ldexp_f32 v175, v175, v95\n"
    "v_ldexp_f32 v176, v176, v94\n"
    "v_ldexp_f32 v177, v177, v94\n"
    "v_ldexp_f32 v178, v178, v94\n"
    "v_ldexp_f32 v179, v179, v94\n"
    "v_ldexp_f32 v180, v180, v95\n"
    "v_ldexp_f32 v181, v181, v95\n"
    "v_ldexp_f32 v182, v182, v95\n"
    "v_ldexp_f32 v183, v183, v95\n"
    "v_ldexp_f32 v184, v184, v94\n"
    "v_ldexp_f32 v185, v185, v94\n"
    "v_ldexp_f32 v186, v186, v94\n"
    "v_ldexp_f32 v187, v187, v94\n"
    "v_ldexp_f32 v188, v188, v95\n"
    "v_ldexp_f32 v189, v189, v95\n"
    "v_ldexp_f32 v190, v190, v95\n"
    "v_ldexp_f32 v191, v191, v95\n"
    "v_ldexp_f32 v192, v192, v94\n"
    "v_ldexp_f32 v193, v193, v94\n"
    "v_ldexp_f32 v194, v194, v94\n"
    "v_ldexp_f32 v195, v195, v94\n"
    "v_ldexp_f32 v196, v196, v95\n"
    "v_ldexp_f32 v197, v197, v95\n"
    "v_ldexp_f32 v198, v198, v95\n"
    "v_ldexp_f32 v199, v199, v95\n"
    "v_ldexp_f32 v200, v200, v94\n"
    "v_ldexp_f32 v201, v201, v94\n"
    "v_ldexp_f32 v202, v202, v94\n"
    "v_ldexp_f32 v203, v203, v94\n"
    "v_ldexp_f32 v204, v204, v95\n"
    "v_ldexp_f32 v205, v205, v95\n"
    "v_ldexp_f32 v206, v206, v95\n"
    "v_ldexp_f32 v207, v207, v95\n"
    "v_ldexp_f32 v208, v208, v94\n"
    "v_ldexp_f32 v209, v209, v94\n"
    "v_ldexp_f32 v210, v210, v94\n"
    "v_ldexp_f32 v211, v211, v94\n"
    "v_ldexp_f32 v212, v212, v95\n"
    "v_ldexp_f32 v213, v213, v95\n"
    "v_ldexp_f32 v214, v214, v95\n"
    "v_ldexp_f32 v215, v215, v95\n"
    "v_ldexp_f32 v216, v216, v94\n"
    "v_ldexp_f32 v217, v217, v94\n"
    "v_ldexp_f32 v218, v218, v94\n"
    "v_ldexp_f32 v219, v219, v94\n"
    "v_ldexp_f32 v220, v220, v95\n"
    "v_ldexp_f32 v221, v221, v95\n"
    "v_ldexp_f32 v222, v222, v95\n"
    "v_ldexp_f32 v223, v223, v95\n"
    "v_ldexp_f32 v224, v224, v94\n"
    "v_ldexp_f32 v225, v225, v94\n"
    "v_ldexp_f32 v226, v226, v94\n"
    "v_ldexp_f32 v227, v227, v94\n"
    "v_ldexp_f32 v228, v228, v95\n"
    "v_ldexp_f32 v229, v229, v95\n"
    "v_ldexp_f32 v230, v230, v95\n"
    "v_ldexp_f32 v231, v231, v95\n"
    "v_ldexp_f32 v232, v232, v94\n"
    "v_ldexp_f32 v233, v233, v94\n"
    "v_ldexp_f32 v234, v234, v94\n"
    "v_ldexp_f32 v235, v235, v94\n"
    "v_ldexp_f32 v236, v236, v95\n"
    "v_ldexp_f32 v237, v237, v95\n"
    "v_ldexp_f32 v238, v238, v95\n"
    "v_ldexp_f32 v239, v239, v95\n"
    "v_ldexp_f32 v240, v240, v94\n"
    "v_ldexp_f32 v241, v241, v94\n"
    "v_ldexp_f32 v242, v242, v94\n"
    "v_ldexp_f32 v243, v243, v94\n"
    "v_ldexp_f32 v244, v244, v95\n"
    "v_ldexp_f32 v245, v245, v95\n"
    "v_ldexp_f32 v246, v246, v95\n"
    "v_ldexp_f32 v247, v247, v95\n"
    "v_ldexp_f32 v248, v248, v94\n"
    "v_ldexp_f32 v249, v249, v94\n"
    "v_ldexp_f32 v250, v250, v94\n"
    "v_ldexp_f32 v251, v251, v94\n"
    "v_ldexp_f32 v252, v252, v95\n"
    "v_ldexp_f32 v253, v253, v95\n"
    "v_ldexp_f32 v254, v254, v95\n"
    "v_ldexp_f32 v255, v255, v95\n"
    "s_nop 1\n"
    "s_branch LB70_%=\n"
    "LEND_%=:\n"
    :
    : [hf] "s"(hfp), [wq] "s"(wqp), [k] "s"(kp), [v] "s"(vp), [wo] "s"(wop), [yp] "s"(ypp), [ab] "s"(ab), [pf] "s"(qblk * 14336 + w * 3584), [w] "s"(w), [lb] "s"(lb), [wqn] "s"(wqn)
    : "v0", "v1", "v2", "v3", "v4", "v5", "v6", "v7", "v8", "v9", "v10", "v11", "v12", "v13", "v14", "v15", "v16", "v17", "v18", "v19", "v20", "v21", "v22", "v23", "v24", "v25", "v26", "v27", "v28", "v29", "v30", "v31", "v32", "v33", "v34", "v35", "v36", "v37", "v38", "v39", "v40", "v41", "v42", "v43", "v44", "v45", "v46", "v47", "v48", "v49", "v50", "v51", "v52", "v53", "v54", "v55", "v56", "v57", "v58", "v59", "v60", "v61", "v62", "v63", "v64", "v65", "v66", "v67", "v68", "v69", "v70", "v71", "v72", "v73", "v74", "v75", "v76", "v77", "v78", "v79", "v80", "v81", "v82", "v83", "v84", "v85", "v86", "v87", "v88", "v89", "v90", "v91", "v92", "v93", "v94", "v95", "v96", "v97", "v98", "v99", "v100", "v101", "v102", "v103", "v104", "v105", "v106", "v107", "v108", "v109", "v110", "v111", "v112", "v113", "v114", "v115", "v116", "v117", "v118", "v119", "v120", "v121", "v122", "v123", "v124", "v125", "v126", "v127", "v128", "v129", "v130", "v131", "v132", "v133", "v134", "v135", "v136", "v137", "v138", "v139", "v140", "v141", "v142", "v143", "v144", "v145", "v146", "v147", "v148", "v149", "v150", "v151", "v152", "v153", "v154", "v155", "v156", "v157", "v158", "v159", "v160", "v161", "v162", "v163", "v164", "v165", "v166", "v167", "v168", "v169", "v170", "v171", "v172", "v173", "v174", "v175", "v176", "v177", "v178", "v179", "v180", "v181", "v182", "v183", "v184", "v185", "v186", "v187", "v188", "v189", "v190", "v191", "v192", "v193", "v194", "v195", "v196", "v197", "v198", "v199", "v200", "v201", "v202", "v203", "v204", "v205", "v206", "v207", "v208", "v209", "v210", "v211", "v212", "v213", "v214", "v215", "v216", "v217", "v218", "v219", "v220", "v221", "v222", "v223", "v224", "v225", "v226", "v227", "v228", "v229", "v230", "v231", "v232", "v233", "v234", "v235", "v236", "v237", "v238", "v239", "v240", "v241", "v242", "v243", "v244", "v245", "v246", "v247", "v248", "v249", "v250", "v251", "v252", "v253", "v254", "v255", "a0", "a1", "a2", "a3", "a4", "a5", "a6", "a7", "a8", "a9", "a10", "a11", "a12", "a13", "a14", "a15", "a16", "a17", "a18", "a19", "a20", "a21", "a22", "a23", "a24", "a25", "a26", "a27", "a28", "a29", "a30", "a31", "a32", "a33", "a34", "a35", "a36", "a37", "a38", "a39", "a40", "a41", "a42", "a43", "a44", "a45", "a46", "a47", "a48", "a49", "a50", "a51", "a52", "a53", "a54", "a55", "a56", "a57", "a58", "a59", "a60", "a61", "a62", "a63", "a64", "a65", "a66", "a67", "a68", "a69", "a70", "a71", "a72", "a73", "a74", "a75", "a76", "a77", "a78", "a79", "a80", "a81", "a82", "a83", "a84", "a85", "a86", "a87", "a88", "a89", "a90", "a91", "a92", "a93", "a94", "a95", "a96", "a97", "a98", "a99", "a100", "a101", "a102", "a103", "a104", "a105", "a106", "a107", "a108", "a109", "a110", "a111", "a112", "a113", "a114", "a115", "a116", "a117", "a118", "a119", "a120", "a121", "a122", "a123", "a124", "a125", "a126", "a127", "a128", "a129", "a130", "a131", "a132", "a133", "a134", "a135", "a136", "a137", "a138", "a139", "a140", "a141", "a142", "a143", "a144", "a145", "a146", "a147", "a148", "a149", "a150", "a151", "a152", "a153", "a154", "a155", "a156", "a157", "a158", "a159", "a160", "a161", "a162", "a163", "a164", "a165", "a166", "a167", "a168", "a169", "a170", "a171", "a172", "a173", "a174", "a175", "a176", "a177", "a178", "a179", "a180", "a181", "a182", "a183", "a184", "a185", "a186", "a187", "a188", "a189", "a190", "a191", "a192", "a193", "a194", "a195", "a196", "a197", "a198", "a199", "a200", "a201", "a202", "a203", "a204", "a205", "a206", "a207", "a208", "a209", "a210", "a211", "a212", "a213", "a214", "a215", "a216", "a217", "a218", "a219", "a220", "a221", "a222", "a223", "a224", "a225", "a226", "a227", "a228", "a229", "a230", "a231", "a232", "a233", "a234", "a235", "a236", "a237", "a238", "a239", "a240", "a241", "a242", "a243", "a244", "a245", "a246", "a247", "a248", "a249", "a250", "a251", "a252", "a253", "a254", "a255", "s36", "s37", "s38", "s39", "s40", "s41", "s42", "s43", "s44", "s45", "s46", "s47", "s48", "s49", "s50", "s51", "s52", "s53", "s54", "s55", "s56", "s57", "s58", "s59", "s60", "s61", "s62", "s63", "s64", "s65", "s66", "s67", "s68", "s69", "s70", "s71", "s72", "s73", "s74", "s75", "s76", "s77", "s78", "s79", "s80", "s81", "s82", "s83", "s84", "s85", "s86", "s87", "s88", "s89", "s90", "s91", "s92", "s93", "s94", "s95", "s96", "s97", "s98", "s99", "vcc", "scc", "m0", "memory");

  const int t = w * 64 + (int)__builtin_amdgcn_mbcnt_hi(~0u, __builtin_amdgcn_mbcnt_lo(~0u, 0u));
  const char* ka = (const char*)__builtin_amdgcn_kernarg_segment_ptr();
  typedef __attribute__((address_space(1))) unsigned gu32;
  const char *x, *ws;
  asm volatile("s_load_dwordx2 %0, %2, 0x38\n s_load_dwordx2 %1, %2, 0x40\n s_waitcnt lgkmcnt(0)\n" : "=&s"(x), "=&s"(ws) : "s"(ka));
  __syncthreads();
  if (t == 0) __hip_atomic_fetch_add((gu32*)(ws + WS_SYNC) + qblk * 64, 1u << head, __ATOMIC_RELAXED, __HIP_MEMORY_SCOPE_AGENT);
  float* red = (float*)(lds + 131072);
  const int r8 = t & 7, cg = t >> 3, row = qblk * 128 + head * 8 + r8, rq = row >> 5, r = row & 31;
  const bool two = cg < 24;
  const int ch1 = two ? cg + 32 : cg;
  const int xp = kvh >> 1, idx = (kvh & 1) * 28 + (j < 28 ? j : 0), fc = (xp >> 1) * 7 + (idx >> 3), gq = (xp & 1) * 8 + (idx & 7);
  const int fa = ((rq * NS * 64) + cg * 32 + r) * 8, fb = ((rq * NS * 64) + ch1 * 32 + r) * 8;
  f32x4 s0, s1, s2, s3, g[4], A[4], B[4]; u32x4 xa, xb;
  asm volatile(
    "s_mov_b64 s[52:53], %[ws]\n"
    "s_mov_b32 s54, 10752\n"
    "s_mov_b32 s55, 0x00020000\n"
    "s_mov_b64 s[56:57], %[x]\n"
    "s_mov_b32 s58, 1835008\n"
    "s_mov_b32 s59, 0x00020000\n"
    "s_add_u32 s40, s52, 7356416\n"
    "s_addc_u32 s41, s53, 0\n"
    "s_mov_b32 s42, 29360128\n"
    "s_mov_b32 s43, 0x00020000\n"
    "s_mov_b32 s44, 0\n"
    "s_mov_b32 s45, 0\n"
    "v_mov_b32 v126, %[qo]\n"
    "s_cmp_ge_u32 %[j], 28\n"
    "s_cselect_b32 s49, 1, 0\n"
    "global_load_dword v127, v126, %[ws] sc1\n"
    "buffer_load_dwordx4 v[54:57], %[xa], s[56:59], 0 offen\n"
    "buffer_load_dwordx4 v[58:61], %[xb], s[56:59], 0 offen\n"
    "s_mov_b32 s48, 3584\n"
    "buffer_load_dwordx4 v[62:65], %[ma], s[52:55], s48 offen offset:0\n"
    "buffer_load_dwordx4 v[66:69], %[ma], s[52:55], s48 offen offset:16\n"
    "buffer_load_dwordx4 v[70:73], %[mb], s[52:55], s48 offen offset:0\n"
    "buffer_load_dwordx4 v[74:77], %[mb], s[52:55], s48 offen offset:16\n"
    "s_mov_b32 s48, 5376\n"
    "buffer_load_dwordx4 v[78:81], %[ma], s[52:55], s48 offen offset:0\n"
    "buffer_load_dwordx4 v[82:85], %[ma], s[52:55], s48 offen offset:16\n"
    "buffer_load_dwordx4 v[86:89], %[mb], s[52:55], s48 offen offset:0\n"
    "buffer_load_dwordx4 v[90:93], %[mb], s[52:55], s48 offen offset:16\n"
    "s_mov_b32 s48, 7168\n"
    "buffer_load_dwordx4 v[94:97], %[ma], s[52:55], s48 offen offset:0\n"
    "buffer_load_dwordx4 v[98:101], %[ma], s[52:55], s48 offen offset:16\n"
    "buffer_load_dwordx4 v[102:105], %[mb], s[52:55], s48 offen offset:0\n"
    "buffer_load_dwordx4 v[106:109], %[mb], s[52:55], s48 offen offset:16\n"
    "s_add_u32 s56, s52, %[wo]\n"
    "s_addc_u32 s57, s53, 0\n"
    "s_nop 0\n"
    "s_mov_b32 s58, 229376\n"
    "s_waitcnt vmcnt(14)\n"
    "s_branch LGQ_%=\n"
    "LGP_%=:\n"
    "global_load_dword v127, v126, %[ws] sc1\n"
    "s_waitcnt vmcnt(0)\n"
    "LGQ_%=:\n"
    "v_readfirstlane_b32 s46, v127\n"
    "s_nop 3\n"
    "LGR_%=:\n"
    "s_andn2_b32 s47, s46, s44\n"
    "s_bitcmp1_b32 s47, 0\n"
    "s_cbranch_scc0 LGS0_%=\n"
    "s_mov_b32 s48, 0\n"
    "buffer_load_dwordx4 v[128:131], %[fa], s[40:43], s48 offen sc1\n"
    "buffer_load_dwordx4 v[132:135], %[fb], s[40:43], s48 offen sc1\n"
    "LGS0_%=:\n"
    "s_bitcmp1_b32 s47, 1\n"
    "s_cbranch_scc0 LGS1_%=\n"
    "s_mov_b32 s48, 1835008\n"
    "buffer_load_dwordx4 v[136:139], %[fa], s[40:43], s48 offen sc1\n"
    "buffer_load_dwordx4 v[140:143], %[fb], s[40:43], s48 offen sc1\n"
    "LGS1_%=:\n"
    "s_bitcmp1_b32 s47, 2\n"
    "s_cbranch_scc0 LGS2_%=\n"
    "s_mov_b32 s48, 3670016\n"
    "buffer_load_dwordx4 v[144:147], %[fa], s[40:43], s48 offen sc1\n"
    "buffer_load_dwordx4 v[148:151], %[fb], s[40:43], s48 offen sc1\n"
    "LGS2_%=:\n"
    "s_bitcmp1_b32 s47, 3\n"
    "s_cbranch_scc0 LGS3_%=\n"
    "s_mov_b32 s48, 5505024\n"
    "buffer_load_dwordx4 v[152:155], %[fa], s[40:43], s48 offen sc1\n"
    "buffer_load_dwordx4 v[156:159], %[fb], s[40:43], s48 offen sc1\n"
    "LGS3_%=:\n"
    "s_bitcmp1_b32 s47, 4\n"
    "s_cbranch_scc0 LGS4_%=\n"
    "s_mov_b32 s48, 7340032\n"
    "buffer_load_dwordx4 v[160:163], %[fa], s[40:43], s48 offen sc1\n"
    "buffer_load_dwordx4 v[164:167], %[fb], s[40:43], s48 offen sc1\n"
    "LGS4_%=:\n"
    "s_bitcmp1_b32 s47, 5\n"
    "s_cbranch_scc0 LGS5_%=\n"
    "s_mov_b32 s48, 9175040\n"
    "buffer_load_dwordx4 v[168:171], %[fa], s[40:43], s48 offen sc1\n"
    "buffer_load_dwordx4 v[172:175], %[fb], s[40:43], s48 offen sc1\n"
    "LGS5_%=:\n"
    "s_bitcmp1_b32 s47, 6\n"
    "s_cbranch_scc0 LGS6_%=\n"
    "s_mov_b32 s48, 11010048\n"
    "buffer_load_dwordx4 v[176:179], %[fa], s[40:43], s48 offen sc1\n"
    "buffer_load_dwordx4 v[180:183], %[fb], s[40:43], s48 offen sc1\n"
    "LGS6_%=:\n"
    "s_bitcmp1_b32 s47, 7\n"
    "s_cbranch_scc0 LGS7_%=\n"
    "s_mov_b32 s48, 12845056\n"
    "buffer_load_dwordx4 v[184:187], %[fa], s[40:43], s48 offen sc1\n"
    "buffer_load_dwordx4 v[188:191], %[fb], s[40:43], s48 offen sc1\n"
    "LGS7_%=:\n"
    "s_bitcmp1_b32 s47, 8\n"
    "s_cbranch_scc0 LGS8_%=\n"
    "s_mov_b32 s48, 14680064\n"
    "buffer_load_dwordx4 v[192:195], %[fa], s[40:43], s48 offen sc1\n"
    "buffer_load_dwordx4 v[196:199], %[fb], s[40:43], s48 offen sc1\n"
    "LGS8_%=:\n"
    "s_bitcmp1_b32 s47, 9\n"
    "s_cbranch_scc0 LGS9_%=\n"
    "s_mov_b32 s48, 16515072\n"
    "buffer_load_dwordx4 v[200:203], %[fa], s[40:43], s48 offen sc1\n"
    "buffer_load_dwordx4 v[204:207], %[fb], s[40:43], s48 offen sc1\n"
    "LGS9_%=:\n"
    "s_bitcmp1_b32 s47, 10\n"
    "s_cbranch_scc0 LGS10_%=\n"
    "s_mov_b32 s48, 18350080\n"
    "buffer_load_dwordx4 v[208:211], %[fa], s[40:43], s48 offen sc1\n"
    "buffer_load_dwordx4 v[212:215], %[fb], s[40:43], s48 offen sc1\n"
    "LGS10_%=:\n"
    "s_bitcmp1_b32 s47, 11\n"
    "s_cbranch_scc0 LGS11_%=\n"
    "s_mov_b32 s48, 20185088\n"
    "buffer_load_dwordx4 v[216:219], %[fa], s[40:43], s48 offen sc1\n"
    "buffer_load_dwordx4 v[220:223], %[fb], s[40:43], s48 offen sc1\n"
    "LGS11_%=:\n"
    "s_bitcmp1_b32 s47, 12\n"
    "s_cbranch_scc0 LGS12_%=\n"
    "s_mov_b32 s48, 22020096\n"
    "buffer_load_dwordx4 v[224:227], %[fa], s[40:43], s48 offen sc1\n"
    "buffer_load_dwordx4 v[228:231], %[fb], s[40:43], s48 offen sc1\n"
    "LGS12_%=:\n"
    "s_bitcmp1_b32 s47, 13\n"
    "s_cbranch_scc0 LGS13_%=\n"
    "s_mov_b32 s48, 23855104\n"
    "buffer_load_dwordx4 v[232:235], %[fa], s[40:43], s48 offen sc1\n"
    "buffer_load_dwordx4 v[236:239], %[fb], s[40:43], s48 offen sc1\n"
    "LGS13_%=:\n"
    "s_bitcmp1_b32 s47, 14\n"
    "s_cbranch_scc0 LGS14_%=\n"
    "s_mov_b32 s48, 25690112\n"
    "buffer_load_dwordx4 v[240:243], %[fa], s[40:43], s48 offen sc1\n"
    "buffer_load_dwordx4 v[244:247], %[fb], s[40:43], s48 offen sc1\n"
    "LGS14_%=:\n"
    "s_bitcmp1_b32 s47, 15\n"
    "s_cbranch_scc0 LGS15_%=\n"
    "s_mov_b32 s48, 27525120\n"
    "buffer_load_dwordx4 v[248:251], %[fa], s[40:43], s48 offen sc1\n"
    "buffer_load_dwordx4 v[252:255], %[fb], s[40:43], s48 offen sc1\n"
    "LGS15_%=:\n"
    "s_or_b32 s44, s44, s47\n"
    "s_cmp_eq_u32 s44, 0xffff\n"
    "s_cbranch_scc1 LGD_%=\n"
    "s_cmp_eq_u32 s49, 0\n"
    "s_cbranch_scc0 LGF_%=\n"
    "s_mov_b32 s49, 1\n"
    "s_waitcnt vmcnt(30)\n"
    "s_add_u32 m0, %[m0b], 0\n"
    "s_mov_b32 s48, 0\n"
    "buffer_load_dwordx4 %[t16], s[56:59], s48 offen offset:0 lds\n"
    "buffer_load_dwordx4 %[t16], s[56:59], s48 offen offset:1024 lds\n"
    "buffer_load_dwordx4 %[t16], s[56:59], s48 offen offset:2048 lds\n"
    "buffer_load_dwordx4 %[t16], s[56:59], s48 offen offset:3072 lds\n"
    "s_add_u32 m0, %[m0b], 65536\n"
    "s_mov_b32 s48, 16384\n"
    "buffer_load_dwordx4 %[t16], s[56:59], s48 offen offset:0 lds\n"
    "buffer_load_dwordx4 %[t16], s[56:59], s48 offen offset:1024 lds\n"
    "buffer_load_dwordx4 %[t16], s[56:59], s48 offen offset:2048 lds\n"
    "buffer_load_dwordx4 %[t16], s[56:59], s48 offen offset:3072 lds\n"
    "s_add_u32 m0, %[m0b], 16384\n"
    "s_mov_b32 s48, 32768\n"
    "buffer_load_dwordx4 %[t16], s[56:59], s48 offen offset:0 lds\n"
    "buffer_load_dwordx4 %[t16], s[56:59], s48 offen offset:1024 lds\n"
    "buffer_load_dwordx4 %[t16], s[56:59], s48 offen offset:2048 lds\n"
    "buffer_load_dwordx4 %[t16], s[56:59], s48 offen offset:3072 lds\n"
    "s_add_u32 m0, %[m0b], 81920\n"
    "s_mov_b32 s48, 49152\n"
    "buffer_load_dwordx4 %[t16], s[56:59], s48 offen offset:0 lds\n"
    "buffer_load_dwordx4 %[t16], s[56:59], s48 offen offset:1024 lds\n"
    "buffer_load_dwordx4 %[t16], s[56:59], s48 offen offset:2048 lds\n"
    "buffer_load_dwordx4 %[t16], s[56:59], s48 offen offset:3072 lds\n"
    "s_add_u32 m0, %[m0b], 32768\n"
    "s_mov_b32 s48, 65536\n"
    "buffer_load_dwordx4 %[t16], s[56:59], s48 offen offset:0 lds\n"
    "buffer_load_dwordx4 %[t16], s[56:59], s48 offen offset:1024 lds\n"
    "buffer_load_dwordx4 %[t16], s[56:59], s48 offen offset:2048 lds\n"
    "buffer_load_dwordx4 %[t16], s[56:59], s48 offen offset:3072 lds\n"
    "s_add_u32 m0, %[m0b], 98304\n"
    "s_mov_b32 s48, 81920\n"
    "buffer_load_dwordx4 %[t16], s[56:59], s48 offen offset:0 lds\n"
    "buffer_load_dwordx4 %[t16], s[56:59], s48 offen offset:1024 lds\n"
    "buffer_load_dwordx4 %[t16], s[56:59], s48 offen offset:2048 lds\n"
    "buffer_load_dwordx4 %[t16], s[56:59], s48 offen offset:3072 lds\n"
    "s_add_u32 m0, %[m0b], 49152\n"
    "s_mov_b32 s48, 98304\n"
    "buffer_load_dwordx4 %[t16], s[56:59], s48 offen offset:0 lds\n"
    "buffer_load_dwordx4 %[t16], s[56:59], s48 offen offset:1024 lds\n"
    "buffer_load_dwordx4 %[t16], s[56:59], s48 offen offset:2048 lds\n"
    "buffer_load_dwordx4 %[t16], s[56:59], s48 offen offset:3072 lds\n"
    "s_add_u32 m0, %[m0b], 114688\n"
    "s_mov_b32 s48, 114688\n"
    "buffer_load_dwordx4 %[t16], s[56:59], s48 offen offset:0 lds\n"
    "buffer_load_dwordx4 %[t16], s[56:59], s48 offen offset:1024 lds\n"
    "buffer_load_dwordx4 %[t16], s[56:59], s48 offen offset:2048 lds\n"
    "buffer_load_dwordx4 %[t16], s[56:59], s48 offen offset:3072 lds\n"
    "LGF_%=:\n"
    "s_sleep 2\n"
    "s_add_u32 s45, s45, 1\n"
    "s_cmp_lt_u32 s45, 0x40000\n"
    "s_cbranch_scc1 LGP_%=\n"
    "LGD_%=:\n"
    "s_waitcnt vmcnt(0)\n"
    "s_cmp_eq_u32 s49, 0\n"
    "s_cbranch_scc0 LGU0_%=\n"
    "s_add_u32 m0, %[m0b], 0\n"
    "s_mov_b32 s48, 0\n"
    "buffer_load_dwordx4 %[t16], s[56:59], s48 offen offset:0 lds\n"
    "buffer_load_dwordx4 %[t16], s[56:59], s48 offen offset:1024 lds\n"
    "buffer_load_dwordx4 %[t16], s[56:59], s48 offen offset:2048 lds\n"
    "buffer_load_dwordx4 %[t16], s[56:59], s48 offen offset:3072 lds\n"
    "LGU0_%=:\n"
    "v_lshlrev_b32 v110, 16, v128\n"
    "v_and_b32 v111, 0xffff0000, v128\n"
    "v_lshlrev_b32 v112, 16, v129\n"
    "v_and_b32 v113, 0xffff0000, v129\n"
    "v_lshlrev_b32 v114, 16, v130\n"
    "v_and_b32 v115, 0xffff0000, v130\n"
    "v_lshlrev_b32 v116, 16, v131\n"
    "v_and_b32 v117, 0xffff0000, v131\n"
    "v_lshlrev_b32 v118, 16, v132\n"
    "v_and_b32 v119, 0xffff0000, v132\n"
    "v_lshlrev_b32 v120, 16, v133\n"
    "v_and_b32 v121, 0xffff0000, v133\n"
    "v_lshlrev_b32 v122, 16, v134\n"
    "v_and_b32 v123, 0xffff0000, v134\n"
    "v_lshlrev_b32 v124, 16, v135\n"
    "v_and_b32 v125, 0xffff0000, v135\n"
    "v_lshlrev_b32 v126, 16, v136\n"
    "v_and_b32 v127, 0xffff0000, v136\n"
    "v_pk_add_f32 v[110:111], v[110:111], v[126:127]\n"
    "v_lshlrev_b32 v128, 16, v137\n"
    "v_and_b32 v129, 0xffff0000, v137\n"
    "v_pk_add_f32 v[112:113], v[112:113], v[128:129]\n"
    "v_lshlrev_b32 v126, 16, v138\n"
    "v_and_b32 v127, 0xffff0000, v138\n"
    "v_pk_add_f32 v[114:115], v[114:115], v[126:127]\n"
    "v_lshlrev_b32 v128, 16, v139\n"
    "v_and_b32 v129, 0xffff0000, v139\n"
    "v_pk_add_f32 v[116:117], v[116:117], v[128:129]\n"
    "v_lshlrev_b32 v126, 16, v140\n"
    "v_and_b32 v127, 0xffff0000, v140\n"
    "v_pk_add_f32 v[118:119], v[118:119], v[126:127]\n"
    "v_lshlrev_b32 v128, 16, v141\n"
    "v_and_b32 v129, 0xffff0000, v141\n"
    "v_pk_add_f32 v[120:121], v[120:121], v[128:129]\n"
    "v_lshlrev_b32 v126, 16, v142\n"
    "v_and_b32 v127, 0xffff0000, v142\n"
    "v_pk_add_f32 v[122:123], v[122:123], v[126:127]\n"
    "v_lshlrev_b32 v128, 16, v143\n"
    "v_and_b32 v129, 0xffff0000, v143\n"
    "v_pk_add_f32 v[124:125], v[124:125], v[128:129]\n"
    "s_cmp_eq_u32 s49, 0\n"
    "s_cbranch_scc0 LGU2_%=\n"
    "s_add_u32 m0, %[m0b], 65536\n"
    "s_mov_b32 s48, 16384\n"
    "buffer_load_dwordx4 %[t16], s[56:59], s48 offen offset:0 lds\n"
    "buffer_load_dwordx4 %[t16], s[56:59], s48 offen offset:1024 lds\n"
    "buffer_load_dwordx4 %[t16], s[56:59], s48 offen offset:2048 lds\n"
    "buffer_load_dwordx4 %[t16], s[56:59], s48 offen offset:3072 lds\n"
    "LGU2_%=:\n"
    "v_lshlrev_b32 v126, 16, v144\n"
    "v_and_b32 v127, 0xffff0000, v144\n"
    "v_pk_add_f32 v[110:111], v[110:111], v[126:127]\n"
    "v_lshlrev_b32 v128, 16, v145\n"
    "v_and_b32 v129, 0xffff0000, v145\n"
    "v_pk_add_f32 v[112:113], v[112:113], v[128:129]\n"
    "v_lshlrev_b32 v126, 16, v146\n"
    "v_and_b32 v127, 0xffff0000, v146\n"
    "v_pk_add_f32 v[114:115], v[114:115], v[126:127]\n"
    "v_lshlrev_b32 v128, 16, v147\n"
    "v_and_b32 v129, 0xffff0000, v147\n"
    "v_pk_add_f32 v[116:117], v[116:117], v[128:129]\n"
    "v_lshlrev_b32 v126, 16, v148\n"
    "v_and_b32 v127, 0xffff0000, v148\n"
    "v_pk_add_f32 v[118:119], v[118:119], v[126:127]\n"
    "v_lshlrev_b32 v128, 16, v149\n"
    "v_and_b32 v129, 0xffff0000, v149\n"
    "v_pk_add_f32 v[120:121], v[120:121], v[128:129]\n"
    "v_lshlrev_b32 v126, 16, v150\n"
    "v_and_b32 v127, 0xffff0000, v150\n"
    "v_pk_add_f32 v[122:123], v[122:123], v[126:127]\n"
    "v_lshlrev_b32 v128, 16, v151\n"
    "v_and_b32 v129, 0xffff0000, v151\n"
    "v_pk_add_f32 v[124:125], v[124:125], v[128:129]\n"
    "v_lshlrev_b32 v126, 16, v152\n"
    "v_and_b32 v127, 0xffff0000, v152\n"
    "v_pk_add_f32 v[110:111], v[110:111], v[126:127]\n"
    "v_lshlrev_b32 v128, 16, v153\n"
    "v_and_b32 v129, 0xffff0000, v153\n"
    "v_pk_add_f32 v[112:113], v[112:113], v[128:129]\n"
    "v_lshlrev_b32 v126, 16, v154\n"
    "v_and_b32 v127, 0xffff0000, v154\n"
    "v_pk_add_f32 v[114:115], v[114:115], v[126:127]\n"
    "v_lshlrev_b32 v128, 16, v155\n"
    "v_and_b32 v129, 0xffff0000, v155\n"
    "v_pk_add_f32 v[116:117], v[116:117], v[128:129]\n"
    "v_lshlrev_b32 v126, 16, v156\n"
    "v_and_b32 v127, 0xffff0000, v156\n"
    "v_pk_add_f32 v[118:119], v[118:119], v[126:127]\n"
    "v_lshlrev_b32 v128, 16, v157\n"
    "v_and_b32 v129, 0xffff0000, v157\n"
    "v_pk_add_f32 v[120:121], v[120:121], v[128:129]\n"
    "v_lshlrev_b32 v126, 16, v158\n"
    "v_and_b32 v127, 0xffff0000, v158\n"
    "v_pk_add_f32 v[122:123], v[122:123], v[126:127]\n"
    "v_lshlrev_b32 v128, 16, v159\n"
    "v_and_b32 v129, 0xffff0000, v159\n"
    "v_pk_add_f32 v[124:125], v[124:125], v[128:129]\n"
    "s_cmp_eq_u32 s49, 0\n"
    "s_cbranch_scc0 LGU4_%=\n"
    "s_add_u32 m0, %[m0b], 16384\n"
    "s_mov_b32 s48, 32768\n"
    "buffer_load_dwordx4 %[t16], s[56:59], s48 offen offset:0 lds\n"
    "buffer_load_dwordx4 %[t16], s[56:59], s48 offen offset:1024 lds\n"
    "buffer_load_dwordx4 %[t16], s[56:59], s48 offen offset:2048 lds\n"
    "buffer_load_dwordx4 %[t16], s[56:59], s48 offen offset:3072 lds\n"
    "LGU4_%=:\n"
    "v_lshlrev_b32 v126, 16, v160\n"
    "v_and_b32 v127, 0xffff0000, v160\n"
    "v_pk_add_f32 v[110:111], v[110:111], v[126:127]\n"
    "v_lshlrev_b32 v128, 16, v161\n"
    "v_and_b32 v129, 0xffff0000, v161\n"
    "v_pk_add_f32 v[112:113], v[112:113], v[128:129]\n"
    "v_lshlrev_b32 v126, 16, v162\n"
    "v_and_b32 v127, 0xffff0000, v162\n"
    "v_pk_add_f32 v[114:115], v[114:115], v[126:127]\n"
    "v_lshlrev_b32 v128, 16, v163\n"
    "v_and_b32 v129, 0xffff0000, v163\n"
    "v_pk_add_f32 v[116:117], v[116:117], v[128:129]\n"
    "v_lshlrev_b32 v126, 16, v164\n"
    "v_and_b32 v127, 0xffff0000, v164\n"
    "v_pk_add_f32 v[118:119], v[118:119], v[126:127]\n"
    "v_lshlrev_b32 v128, 16, v165\n"
    "v_and_b32 v129, 0xffff0000, v165\n"
    "v_pk_add_f32 v[120:121], v[120:121], v[128:129]\n"
    "v_lshlrev_b32 v126, 16, v166\n"
    "v_and_b32 v127, 0xffff0000, v166\n"
    "v_pk_add_f32 v[122:123], v[122:123], v[126:127]\n"
    "v_lshlrev_b32 v128, 16, v167\n"
    "v_and_b32 v129, 0xffff0000, v167\n"
    "v_pk_add_f32 v[124:125], v[124:125], v[128:129]\n"
    "v_lshlrev_b32 v126, 16, v168\n"
    "v_and_b32 v127, 0xffff0000, v168\n"
    "v_pk_add_f32 v[110:111], v[110:111], v[126:127]\n"
    "v_lshlrev_b32 v128, 16, v169\n"
    "v_and_b32 v129, 0xffff0000, v169\n"
    "v_pk_add_f32 v[112:113], v[112:113], v[128:129]\n"
    "v_lshlrev_b32 v126, 16, v170\n"
    "v_and_b32 v127, 0xffff0000, v170\n"
    "v_pk_add_f32 v[114:115], v[114:115], v[126:127]\n"
    "v_lshlrev_b32 v128, 16, v171\n"
    "v_and_b32 v129, 0xffff0000, v171\n"
    "v_pk_add_f32 v[116:117], v[116:117], v[128:129]\n"
    "v_lshlrev_b32 v126, 16, v172\n"
    "v_and_b32 v127, 0xffff0000, v172\n"
    "v_pk_add_f32 v[118:119], v[118:119], v[126:127]\n"
    "v_lshlrev_b32 v128, 16, v173\n"
    "v_and_b32 v129, 0xffff0000, v173\n"
    "v_pk_add_f32 v[120:121], v[120:121], v[128:129]\n"
    "v_lshlrev_b32 v126, 16, v174\n"
    "v_and_b32 v127, 0xffff0000, v174\n"
    "v_pk_add_f32 v[122:123], v[122:123], v[126:127]\n"
    "v_lshlrev_b32 v128, 16, v175\n"
    "v_and_b32 v129, 0xffff0000, v175\n"
    "v_pk_add_f32 v[124:125], v[124:125], v[128:129]\n"
    "s_cmp_eq_u32 s49, 0\n"
    "s_cbranch_scc0 LGU6_%=\n"
    "s_add_u32 m0, %[m0b], 81920\n"
    "s_mov_b32 s48, 49152\n"
    "buffer_load_dwordx4 %[t16], s[56:59], s48 offen offset:0 lds\n"
    "buffer_load_dwordx4 %[t16], s[56:59], s48 offen offset:1024 lds\n"
    "buffer_load_dwordx4 %[t16], s[56:59], s48 offen offset:2048 lds\n"
    "buffer_load_dwordx4 %[t16], s[56:59], s48 offen offset:3072 lds\n"
    "LGU6_%=:\n"
    "v_lshlrev_b32 v126, 16, v176\n"
    "v_and_b32 v127, 0xffff0000, v176\n"
    "v_pk_add_f32 v[110:111], v[110:111], v[126:127]\n"
    "v_lshlrev_b32 v128, 16, v177\n"
    "v_and_b32 v129, 0xffff0000, v177\n"
    "v_pk_add_f32 v[112:113], v[112:113], v[128:129]\n"
    "v_lshlrev_b32 v126, 16, v178\n"
    "v_and_b32 v127, 0xffff0000, v178\n"
    "v_pk_add_f32 v[114:115], v[114:115], v[126:127]\n"
    "v_lshlrev_b32 v128, 16, v179\n"
    "v_and_b32 v129, 0xffff0000, v179\n"
    "v_pk_add_f32 v[116:117], v[116:117], v[128:129]\n"
    "v_lshlrev_b32 v126, 16, v180\n"
    "v_and_b32 v127, 0xffff0000, v180\n"
    "v_pk_add_f32 v[118:119], v[118:119], v[126:127]\n"
    "v_lshlrev_b32 v128, 16, v181\n"
    "v_and_b32 v129, 0xffff0000, v181\n"
    "v_pk_add_f32 v[120:121], v[120:121], v[128:129]\n"
    "v_lshlrev_b32 v126, 16, v182\n"
    "v_and_b32 v127, 0xffff0000, v182\n"
    "v_pk_add_f32 v[122:123], v[122:123], v[126:127]\n"
    "v_lshlrev_b32 v128, 16, v183\n"
    "v_and_b32 v129, 0xffff0000, v183\n"
    "v_pk_add_f32 v[124:125], v[124:125], v[128:129]\n"
    "v_lshlrev_b32 v126, 16, v184\n"
    "v_and_b32 v127, 0xffff0000, v184\n"
    "v_pk_add_f32 v[110:111], v[110:111], v[126:127]\n"
    "v_lshlrev_b32 v128, 16, v185\n"
    "v_and_b32 v129, 0xffff0000, v185\n"
    "v_pk_add_f32 v[112:113], v[112:113], v[128:129]\n"
    "v_lshlrev_b32 v126, 16, v186\n"
    "v_and_b32 v127, 0xffff0000, v186\n"
    "v_pk_add_f32 v[114:115], v[114:115], v[126:127]\n"
    "v_lshlrev_b32 v128, 16, v187\n"
    "v_and_b32 v129, 0xffff0000, v187\n"
    "v_pk_add_f32 v[116:117], v[116:117], v[128:129]\n"
    "v_lshlrev_b32 v126, 16, v188\n"
    "v_and_b32 v127, 0xffff0000, v188\n"
    "v_pk_add_f32 v[118:119], v[118:119], v[126:127]\n"
    "v_lshlrev_b32 v128, 16, v189\n"
    "v_and_b32 v129, 0xffff0000, v189\n"
    "v_pk_add_f32 v[120:121], v[120:121], v[128:129]\n"
    "v_lshlrev_b32 v126, 16, v190\n"
    "v_and_b32 v127, 0xffff0000, v190\n"
    "v_pk_add_f32 v[122:123], v[122:123], v[126:127]\n"
    "v_lshlrev_b32 v128, 16, v191\n"
    "v_and_b32 v129, 0xffff0000, v191\n"
    "v_pk_add_f32 v[124:125], v[124:125], v[128:129]\n"
    "s_cmp_eq_u32 s49, 0\n"
    "s_cbranch_scc0 LGU8_%=\n"
    "s_add_u32 m0, %[m0b], 32768\n"
    "s_mov_b32 s48, 65536\n"
    "buffer_load_dwordx4 %[t16], s[56:59], s48 offen offset:0 lds\n"
    "buffer_load_dwordx4 %[t16], s[56:59], s48 offen offset:1024 lds\n"
    "buffer_load_dwordx4 %[t16], s[56:59], s48 offen offset:2048 lds\n"
    "buffer_load_dwordx4 %[t16], s[56:59], s48 offen offset:3072 lds\n"
    "LGU8_%=:\n"
    "v_lshlrev_b32 v126, 16, v192\n"
    "v_and_b32 v127, 0xffff0000, v192\n"
    "v_pk_add_f32 v[110:111], v[110:111], v[126:127]\n"
    "v_lshlrev_b32 v128, 16, v193\n"
    "v_and_b32 v129, 0xffff0000, v193\n"
    "v_pk_add_f32 v[112:113], v[112:113], v[128:129]\n"
    "v_lshlrev_b32 v126, 16, v194\n"
    "v_and_b32 v127, 0xffff0000, v194\n"
    "v_pk_add_f32 v[114:115], v[114:115], v[126:127]\n"
    "v_lshlrev_b32 v128, 16, v195\n"
    "v_and_b32 v129, 0xffff0000, v195\n"
    "v_pk_add_f32 v[116:117], v[116:117], v[128:129]\n"
    "v_lshlrev_b32 v126, 16, v196\n"
    "v_and_b32 v127, 0xffff0000, v196\n"
    "v_pk_add_f32 v[118:119], v[118:119], v[126:127]\n"
    "v_lshlrev_b32 v128, 16, v197\n"
    "v_and_b32 v129, 0xffff0000, v197\n"
    "v_pk_add_f32 v[120:121], v[120:121], v[128:129]\n"
    "v_lshlrev_b32 v126, 16, v198\n"
    "v_and_b32 v127, 0xffff0000, v198\n"
    "v_pk_add_f32 v[122:123], v[122:123], v[126:127]\n"
    "v_lshlrev_b32 v128, 16, v199\n"
    "v_and_b32 v129, 0xffff0000, v199\n"
    "v_pk_add_f32 v[124:125], v[124:125], v[128:129]\n"
    "v_lshlrev_b32 v126, 16, v200\n"
    "v_and_b32 v127, 0xffff0000, v200\n"
    "v_pk_add_f32 v[110:111], v[110:111], v[126:127]\n"
    "v_lshlrev_b32 v128, 16, v201\n"
    "v_and_b32 v129, 0xffff0000, v201\n"
    "v_pk_add_f32 v[112:113], v[112:113], v[128:129]\n"
    "v_lshlrev_b32 v126, 16, v202\n"
    "v_and_b32 v127, 0xffff0000, v202\n"
    "v_pk_add_f32 v[114:115], v[114:115], v[126:127]\n"
    "v_lshlrev_b32 v128, 16, v203\n"
    "v_and_b32 v129, 0xffff0000, v203\n"
    "v_pk_add_f32 v[116:117], v[116:117], v[128:129]\n"
    "v_lshlrev_b32 v126, 16, v204\n"
    "v_and_b32 v127, 0xffff0000, v204\n"
    "v_pk_add_f32 v[118:119], v[118:119], v[126:127]\n"
    "v_lshlrev_b32 v128, 16, v205\n"
    "v_and_b32 v129, 0xffff0000, v205\n"
    "v_pk_add_f32 v[120:121], v[120:121], v[128:129]\n"
    "v_lshlrev_b32 v126, 16, v206\n"
    "v_and_b32 v127, 0xffff0000, v206\n"
    "v_pk_add_f32 v[122:123], v[122:123], v[126:127]\n"
    "v_lshlrev_b32 v128, 16, v207\n"
    "v_and_b32 v129, 0xffff0000, v207\n"
    "v_pk_add_f32 v[124:125], v[124:125], v[128:129]\n"
    "s_cmp_eq_u32 s49, 0\n"
    "s_cbranch_scc0 LGU10_%=\n"
    "s_add_u32 m0, %[m0b], 98304\n"
    "s_mov_b32 s48, 81920\n"
    "buffer_load_dwordx4 %[t16], s[56:59], s48 offen offset:0 lds\n"
    "buffer_load_dwordx4 %[t16], s[56:59], s48 offen offset:1024 lds\n"
    "buffer_load_dwordx4 %[t16], s[56:59], s48 offen offset:2048 lds\n"
    "buffer_load_dwordx4 %[t16], s[56:59], s48 offen offset:3072 lds\n"
    "LGU10_%=:\n"
    "v_lshlrev_b32 v126, 16, v208\n"
    "v_and_b32 v127, 0xffff0000, v208\n"
    "v_pk_add_f32 v[110:111], v[110:111], v[126:127]\n"
    "v_lshlrev_b32 v128, 16, v209\n"
    "v_and_b32 v129, 0xffff0000, v209\n"
    "v_pk_add_f32 v[112:113], v[112:113], v[128:129]\n"
    "v_lshlrev_b32 v126, 16, v210\n"
    "v_and_b32 v127, 0xffff0000, v210\n"
    "v_pk_add_f32 v[114:115], v[114:115], v[126:127]\n"
    "v_lshlrev_b32 v128, 16, v211\n"
    "v_and_b32 v129, 0xffff0000, v211\n"
    "v_pk_add_f32 v[116:117], v[116:117], v[128:129]\n"
    "v_lshlrev_b32 v126, 16, v212\n"
    "v_and_b32 v127, 0xffff0000, v212\n"
    "v_pk_add_f32 v[118:119], v[118:119], v[126:127]\n"
    "v_lshlrev_b32 v128, 16, v213\n"
    "v_and_b32 v129, 0xffff0000, v213\n"
    "v_pk_add_f32 v[120:121], v[120:121], v[128:129]\n"
    "v_lshlrev_b32 v126, 16, v214\n"
    "v_and_b32 v127, 0xffff0000, v214\n"
    "v_pk_add_f32 v[122:123], v[122:123], v[126:127]\n"
    "v_lshlrev_b32 v128, 16, v215\n"
    "v_and_b32 v129, 0xffff0000, v215\n"
    "v_pk_add_f32 v[124:125], v[124:125], v[128:129]\n"
    "v_lshlrev_b32 v126, 16, v216\n"
    "v_and_b32 v127, 0xffff0000, v216\n"
    "v_pk_add_f32 v[110:111], v[110:111], v[126:127]\n"
    "v_lshlrev_b32 v128, 16, v217\n"
    "v_and_b32 v129, 0xffff0000, v217\n"
    "v_pk_add_f32 v[112:113], v[112:113], v[128:129]\n"
    "v_lshlrev_b32 v126, 16, v218\n"
    "v_and_b32 v127, 0xffff0000, v218\n"
    "v_pk_add_f32 v[114:115], v[114:115], v[126:127]\n"
    "v_lshlrev_b32 v128, 16, v219\n"
    "v_and_b32 v129, 0xffff0000, v219\n"
    "v_pk_add_f32 v[116:117], v[116:117], v[128:129]\n"
    "v_lshlrev_b32 v126, 16, v220\n"
    "v_and_b32 v127, 0xffff0000, v220\n"
    "v_pk_add_f32 v[118:119], v[118:119], v[126:127]\n"
    "v_lshlrev_b32 v128, 16, v221\n"
    "v_and_b32 v129, 0xffff0000, v221\n"
    "v_pk_add_f32 v[120:121], v[120:121], v[128:129]\n"
    "v_lshlrev_b32 v126, 16, v222\n"
    "v_and_b32 v127, 0xffff0000, v222\n"
    "v_pk_add_f32 v[122:123], v[122:123], v[126:127]\n"
    "v_lshlrev_b32 v128, 16, v223\n"
    "v_and_b32 v129, 0xffff0000, v223\n"
    "v_pk_add_f32 v[124:125], v[124:125], v[128:129]\n"
    "s_cmp_eq_u32 s49, 0\n"
    "s_cbranch_scc0 LGU12_%=\n"
    "s_add_u32 m0, %[m0b], 49152\n"
    "s_mov_b32 s48, 98304\n"
    "buffer_load_dwordx4 %[t16], s[56:59], s48 offen offset:0 lds\n"
    "buffer_load_dwordx4 %[t16], s[56:59], s48 offen offset:1024 lds\n"
    "buffer_load_dwordx4 %[t16], s[56:59], s48 offen offset:2048 lds\n"
    "buffer_load_dwordx4 %[t16], s[56:59], s48 offen offset:3072 lds\n"
    "LGU12_%=:\n"
    "v_lshlrev_b32 v126, 16, v224\n"
    "v_and_b32 v127, 0xffff0000, v224\n"
    "v_pk_add_f32 v[110:111], v[110:111], v[126:127]\n"
    "v_lshlrev_b32 v128, 16, v225\n"
    "v_and_b32 v129, 0xffff0000, v225\n"
    "v_pk_add_f32 v[112:113], v[112:113], v[128:129]\n"
    "v_lshlrev_b32 v126, 16, v226\n"
    "v_and_b32 v127, 0xffff0000, v226\n"
    "v_pk_add_f32 v[114:115], v[114:115], v[126:127]\n"
    "v_lshlrev_b32 v128, 16, v227\n"
    "v_and_b32 v129, 0xffff0000, v227\n"
    "v_pk_add_f32 v[116:117], v[116:117], v[128:129]\n"
    "v_lshlrev_b32 v126, 16, v228\n"
    "v_and_b32 v127, 0xffff0000, v228\n"
    "v_pk_add_f32 v[118:119], v[118:119], v[126:127]\n"
    "v_lshlrev_b32 v128, 16, v229\n"
    "v_and_b32 v129, 0xffff0000, v229\n"
    "v_pk_add_f32 v[120:121], v[120:121], v[128:129]\n"
    "v_lshlrev_b32 v126, 16, v230\n"
    "v_and_b32 v127, 0xffff0000, v230\n"
    "v_pk_add_f32 v[122:123], v[122:123], v[126:127]\n"
    "v_lshlrev_b32 v128, 16, v231\n"
    "v_and_b32 v129, 0xffff0000, v231\n"
    "v_pk_add_f32 v[124:125], v[124:125], v[128:129]\n"
    "v_lshlrev_b32 v126, 16, v232\n"
    "v_and_b32 v127, 0xffff0000, v232\n"
    "v_pk_add_f32 v[110:111], v[110:111], v[126:127]\n"
    "v_lshlrev_b32 v128, 16, v233\n"
    "v_and_b32 v129, 0xffff0000, v233\n"
    "v_pk_add_f32 v[112:113], v[112:113], v[128:129]\n"
    "v_lshlrev_b32 v126, 16, v234\n"
    "v_and_b32 v127, 0xffff0000, v234\n"
    "v_pk_add_f32 v[114:115], v[114:115], v[126:127]\n"
    "v_lshlrev_b32 v128, 16, v235\n"
    "v_and_b32 v129, 0xffff0000, v235\n"
    "v_pk_add_f32 v[116:117], v[116:117], v[128:129]\n"
    "v_lshlrev_b32 v126, 16, v236\n"
    "v_and_b32 v127, 0xffff0000, v236\n"
    "v_pk_add_f32 v[118:119], v[118:119], v[126:127]\n"
    "v_lshlrev_b32 v128, 16, v237\n"
    "v_and_b32 v129, 0xffff0000, v237\n"
    "v_pk_add_f32 v[120:121], v[120:121], v[128:129]\n"
    "v_lshlrev_b32 v126, 16, v238\n"
    "v_and_b32 v127, 0xffff0000, v238\n"
    "v_pk_add_f32 v[122:123], v[122:123], v[126:127]\n"
    "v_lshlrev_b32 v128, 16, v239\n"
    "v_and_b32 v129, 0xffff0000, v239\n"
    "v_pk_add_f32 v[124:125], v[124:125], v[128:129]\n"
    "s_cmp_eq_u32 s49, 0\n"
    "s_cbranch_scc0 LGU14_%=\n"
    "s_add_u32 m0, %[m0b], 114688\n"
    "s_mov_b32 s48, 114688\n"
    "buffer_load_dwordx4 %[t16], s[56:59], s48 offen offset:0 lds\n"
    "buffer_load_dwordx4 %[t16], s[56:59], s48 offen offset:1024 lds\n"
    "buffer_load_dwordx4 %[t16], s[56:59], s48 offen offset:2048 lds\n"
    "buffer_load_dwordx4 %[t16], s[56:59], s48 offen offset:3072 lds\n"
    "LGU14_%=:\n"
    "v_lshlrev_b32 v126, 16, v240\n"
    "v_and_b32 v127, 0xffff0000, v240\n"
    "v_pk_add_f32 v[110:111], v[110:111], v[126:127]\n"
    "v_lshlrev_b32 v128, 16, v241\n"
    "v_and_b32 v129, 0xffff0000, v241\n"
    "v_pk_add_f32 v[112:113], v[112:113], v[128:129]\n"
    "v_lshlrev_b32 v126, 16, v242\n"
    "v_and_b32 v127, 0xffff0000, v242\n"
    "v_pk_add_f32 v[114:115], v[114:115], v[126:127]\n"
    "v_lshlrev_b32 v128, 16, v243\n"
    "v_and_b32 v129, 0xffff0000, v243\n"
    "v_pk_add_f32 v[116:117], v[116:117], v[128:129]\n"
    "v_lshlrev_b32 v126, 16, v244\n"
    "v_and_b32 v127, 0xffff0000, v244\n"
    "v_pk_add_f32 v[118:119], v[118:119], v[126:127]\n"
    "v_lshlrev_b32 v128, 16, v245\n"
    "v_and_b32 v129, 0xffff0000, v245\n"
    "v_pk_add_f32 v[120:121], v[120:121], v[128:129]\n"
    "v_lshlrev_b32 v126, 16, v246\n"
    "v_and_b32 v127, 0xffff0000, v246\n"
    "v_pk_add_f32 v[122:123], v[122:123], v[126:127]\n"
    "v_lshlrev_b32 v128, 16, v247\n"
    "v_and_b32 v129, 0xffff0000, v247\n"
    "v_pk_add_f32 v[124:125], v[124:125], v[128:129]\n"
    "v_lshlrev_b32 v126, 16, v248\n"
    "v_and_b32 v127, 0xffff0000, v248\n"
    "v_pk_add_f32 v[110:111], v[110:111], v[126:127]\n"
    "v_lshlrev_b32 v128, 16, v249\n"
    "v_and_b32 v129, 0xffff0000, v249\n"
    "v_pk_add_f32 v[112:113], v[112:113], v[128:129]\n"
    "v_lshlrev_b32 v126, 16, v250\n"
    "v_and_b32 v127, 0xffff0000, v250\n"
    "v_pk_add_f32 v[114:115], v[114:115], v[126:127]\n"
    "v_lshlrev_b32 v128, 16, v251\n"
    "v_and_b32 v129, 0xffff0000, v251\n"
    "v_pk_add_f32 v[116:117], v[116:117], v[128:129]\n"
    "v_lshlrev_b32 v126, 16, v252\n"
    "v_and_b32 v127, 0xffff0000, v252\n"
    "v_pk_add_f32 v[118:119], v[118:119], v[126:127]\n"
    "v_lshlrev_b32 v128, 16, v253\n"
    "v_and_b32 v129, 0xffff0000, v253\n"
    "v_pk_add_f32 v[120:121], v[120:121], v[128:129]\n"
    "v_lshlrev_b32 v126, 16, v254\n"
    "v_and_b32 v127, 0xffff0000, v254\n"
    "v_pk_add_f32 v[122:123], v[122:123], v[126:127]\n"
    "v_lshlrev_b32 v128, 16, v255\n"
    "v_and_b32 v129, 0xffff0000, v255\n"
    "v_pk_add_f32 v[124:125], v[124:125], v[128:129]\n"
    : "=&{v[110:113]}"(s0), "=&{v[114:117]}"(s1), "=&{v[118:121]}"(s2), "=&{v[122:125]}"(s3), "=&{v[54:57]}"(xa), "=&{v[58:61]}"(xb),
      "=&{v[62:65]}"(g[0]), "=&{v[66:69]}"(g[1]), "=&{v[70:73]}"(g[2]), "=&{v[74:77]}"(g[3]),
      "=&{v[78:81]}"(A[0]), "=&{v[82:85]}"(A[1]), "=&{v[86:89]}"(A[2]), "=&{v[90:93]}"(A[3]),
      "=&{v[94:97]}"(B[0]), "=&{v[98:101]}"(B[1]), "=&{v[102:105]}"(B[2]), "=&{v[106:109]}"(B[3])
    : [fa] "v"(fa * 2), [fb] "v"(fb * 2), [xa] "v"(row * (D * 2) + cg * 16), [xb] "v"(row * (D * 2) + ch1 * 16), [ma] "v"(cg * 32), [mb] "v"(ch1 * 32), [t16] "v"(t * 16 + w * 3072),
      [x] "s"(x), [ws] "s"(ws), [qo] "s"((int)WS_SYNC + qblk * 256), [j] "s"(j), [wo] "s"((int)WS_WGU + fc * 229376), [m0b] "s"(lb + w * 4096)
    : "v126", "v127", "v128", "v129", "v130", "v131", "v132", "v133", "v134", "v135", "v136", "v137", "v138", "v139", "v140", "v141", "v142", "v143", "v144", "v145", "v146", "v147", "v148", "v149", "v150", "v151", "v152", "v153", "v154", "v155", "v156", "v157", "v158", "v159", "v160", "v161", "v162", "v163", "v164", "v165", "v166", "v167", "v168", "v169", "v170", "v171", "v172", "v173", "v174", "v175", "v176", "v177", "v178", "v179", "v180", "v181", "v182", "v183", "v184", "v185", "v186", "v187", "v188", "v189", "v190", "v191", "v192", "v193", "v194", "v195", "v196", "v197", "v198", "v199", "v200", "v201", "v202", "v203", "v204", "v205", "v206", "v207", "v208", "v209", "v210", "v211", "v212", "v213", "v214", "v215", "v216", "v217", "v218", "v219", "v220", "v221", "v222", "v223", "v224", "v225", "v226", "v227", "v228", "v229", "v230", "v231", "v232", "v233", "v234", "v235", "v236", "v237", "v238", "v239", "v240", "v241", "v242", "v243", "v244", "v245", "v246", "v247", "v248", "v249", "v250", "v251", "v252", "v253", "v254", "v255", "s40", "s41", "s42", "s43", "s44", "s45", "s46", "s47", "s48", "s49", "s50", "s51", "s52", "s53", "s54", "s55", "s56", "s57", "s58", "s59", "scc", "m0", "memory");
  float va[8] = {s0[0], s0[1], s0[2], s0[3], s1[0], s1[1], s1[2], s1[3]}, vb[8] = {s2[0], s2[1], s2[2], s2[3], s3[0], s3[1], s3[2], s3[3]};
  float sum = 0;
#pragma unroll
  for (int e = 0; e < 4; ++e) {
    va[2 * e] = lo16(xa[e]) + g[e >> 1][(2 * e) & 3] * va[2 * e]; va[2 * e + 1] = hi16(xa[e]) + g[e >> 1][(2 * e + 1) & 3] * va[2 * e + 1];
    vb[2 * e] = lo16(xb[e]) + g[2 + (e >> 1)][(2 * e) & 3] * vb[2 * e]; vb[2 * e + 1] = hi16(xb[e]) + g[2 + (e >> 1)][(2 * e + 1) & 3] * vb[2 * e + 1];
  }
  const __amdgpu_buffer_rsrc_t rw = __builtin_amdgcn_make_buffer_rsrc((void*)ws, 0, (int)WS_YP, 0x00020000);
#define ST16(v, off, so) __builtin_amdgcn_raw_buffer_store_b128(__builtin_bit_cast(u32x4, v), rw, off, so, 16)
  ST16((f32x4{va[0], va[1], va[2], va[3]}), fa * 4 - r * 16, (int)WS_X1); ST16((f32x4{va[4], va[5], va[6], va[7]}), fa * 4 - r * 16 + 512, (int)WS_X1);
  if (two) { ST16((f32x4{vb[0], vb[1], vb[2], vb[3]}), fb * 4 - r * 16, (int)WS_X1); ST16((f32x4{vb[4], vb[5], vb[6], vb[7]}), fb * 4 - r * 16 + 512, (int)WS_X1); }
  float sb = 0;
#pragma unroll
  for (int e = 0; e < 8; ++e) { sum += va[e]; sb += vb[e]; }
  if (two) sum += sb;
  sum += __shfl_xor(sum, 8); sum += __shfl_xor(sum, 16); sum += __shfl_xor(sum, 32);
  if ((t & 63) < 8) red[w * 8 + r8] = sum;
  asm volatile("s_waitcnt lgkmcnt(0)\n s_barrier\n" ::: "memory");
  const float mu = ((red[r8] + red[8 + r8]) + (red[16 + r8] + red[24 + r8])) * (1.f / D);
  float sq = 0, sq2 = 0;
#pragma unroll
  for (int e = 0; e < 8; ++e) { va[e] -= mu; sq += va[e] * va[e]; vb[e] -= mu; sq2 += vb[e] * vb[e]; }
  if (two) sq += sq2;
  sq += __shfl_xor(sq, 8); sq += __shfl_xor(sq, 16); sq += __shfl_xor(sq, 32);
  if ((t & 63) < 8) red[32 + w * 8 + r8] = sq;
  asm volatile("s_waitcnt lgkmcnt(0)\n s_barrier\n" ::: "memory");
  const float rsd = rsqrtf(((red[32 + r8] + red[40 + r8]) + (red[48 + r8] + red[56 + r8])) * (1.f / D) + 1e-5f);
  u32x4 oa, ob;
#pragma unroll
  for (int e = 0; e < 4; ++e) {
    oa[e] = cvtpk(va[2 * e] * rsd * A[e >> 1][(2 * e) & 3] + B[e >> 1][(2 * e) & 3], va[2 * e + 1] * rsd * A[e >> 1][(2 * e + 1) & 3] + B[e >> 1][(2 * e + 1) & 3]);
    ob[e] = cvtpk(vb[2 * e] * rsd * A[2 + (e >> 1)][(2 * e) & 3] + B[2 + (e >> 1)][(2 * e) & 3], vb[2 * e + 1] * rsd * A[2 + (e >> 1)][(2 * e + 1) & 3] + B[2 + (e >> 1)][(2 * e + 1) & 3]);
  }
  ST16(oa, fa * 2, (int)WS_H2F);
  if (two) ST16(ob, fb * 2, (int)WS_H2F);
  asm volatile("s_waitcnt vmcnt(0)\n s_barrier\n" ::: "memory");
  if (t == 0) __hip_atomic_fetch_add((gu32*)(ws + WS_SYNC) + (16 + qblk) * 64, 1u, __ATOMIC_RELAXED, __HIP_MEMORY_SCOPE_AGENT);
  if (j >= 28) return;
  {
    const int rb = kvh * 4 + (j & 3), nb = j >> 2, qd = rb >> 1;
    f32x4 x0, x1v, x2, x3, g0, g1, g2, g3;
    asm volatile(
    "v_mbcnt_lo_u32_b32 v0, -1, 0\n"
    "v_mbcnt_hi_u32_b32 v0, -1, v0\n"
    "s_mov_b64 s[36:37], %[hf]\n"
    "s_mov_b32 s38, 0x7000\n"
    "s_mov_b32 s39, 0x20000\n"
    "s_mov_b64 s[40:41], %[wgu]\n"
    "s_mov_b32 s42, 0x38000\n"
    "s_mov_b32 s43, 0x20000\n"
    "v_lshlrev_b32 v1, 4, v0\n"
    "s_lshl_b32 s70, %[w], 12\n"
    "s_add_u32 s60, s70, %[lb]\n"
    "v_add_u32 v2, s70, v1\n"
    "s_add_u32 s61, s60, 0x4000\n"
    "s_add_u32 s62, s60, 0x8000\n"
    "s_add_u32 s63, s60, 0xc000\n"
    "s_add_u32 s64, s60, 0x10000\n"
    "s_add_u32 s65, s60, 0x14000\n"
    "s_add_u32 s66, s60, 0x18000\n"
    "s_add_u32 s67, s60, 0x1c000\n"
    "s_mov_b64 s[44:45], %[act]\n"
    "s_mov_b32 s46, 0x2000\n"
    "s_mov_b32 s47, 0x20000\n"
    "v_add_u32 v3, %[lb], v1\n"
    "v_add_u32 v4, 0x10000, v3\n"
    "s_mov_b32 s73, 0xbfb8aa3b\n"
    "s_mov_b32 s72, 0x1000\n"
    "s_mov_b64 s[48:49], %[wd]\n"
    "s_mov_b32 s50, 0x23000\n"
    "s_mov_b32 s51, 0x20000\n"
    "ds_read_b128 a[112:115], v3 offset:0\n"
    "ds_read_b128 a[116:119], v3 offset:1024\n"
    "ds_read_b128 a[120:123], v3 offset:2048\n"
    "ds_read_b128 a[124:127], v3 offset:3072\n"
    "ds_read_b128 a[128:131], v3 offset:4096\n"
    "ds_read_b128 a[132:135], v3 offset:5120\n"
    "ds_read_b128 a[136:139], v3 offset:6144\n"
    "ds_read_b128 a[140:143], v3 offset:7168\n"
    "ds_read_b128 a[144:147], v3 offset:8192\n"
    "ds_read_b128 a[148:151], v3 offset:9216\n"
    "ds_read_b128 a[152:155], v3 offset:10240\n"
    "ds_read_b128 a[156:159], v3 offset:11264\n"
    "ds_read_b128 a[160:163], v3 offset:12288\n"
    "ds_read_b128 a[164:167], v3 offset:13312\n"
    "ds_read_b128 a[168:171], v3 offset:14336\n"
    "ds_read_b128 a[172:175], v3 offset:15360\n"
    "s_mov_b32 s96, 0\n"
    "buffer_load_dwordx4 v[32:35], v1, s[48:51], s96 offen offset:0\n"
    "s_mov_b32 s97, 0x1c000\n"
    "buffer_load_dwordx4 v[36:39], v1, s[48:51], s97 offen offset:0\n"
    "s_mov_b32 s96, 0\n"
    "buffer_load_dwordx4 v[40:43], v1, s[48:51], s96 offen offset:1024\n"
    "s_mov_b32 s97, 0x1c000\n"
    "buffer_load_dwordx4 v[44:47], v1, s[48:51], s97 offen offset:1024\n"
    "s_mov_b32 s96, 0\n"
    "buffer_load_dwordx4 v[48:51], v1, s[48:51], s96 offen offset:2048\n"
    "s_mov_b32 s97, 0x1c000\n"
    "buffer_load_dwordx4 v[52:55], v1, s[48:51], s97 offen offset:2048\n"
    "s_mov_b32 s96, 0\n"
    "buffer_load_dwordx4 v[56:59], v1, s[48:51], s96 offen offset:3072\n"
    "s_mov_b32 s97, 0x1c000\n"
    "buffer_load_dwordx4 v[60:63], v1, s[48:51], s97 offen offset:3072\n"
    "s_mov_b32 s96, 0x1000\n"
    "buffer_load_dwordx4 v[64:67], v1, s[48:51], s96 offen offset:0\n"
    "s_mov_b32 s97, 0x1d000\n"
    "buffer_load_dwordx4 v[68:71], v1, s[48:51], s97 offen offset:0\n"
    "s_mov_b32 s96, 0x1000\n"
    "buffer_load_dwordx4 v[72:75], v1, s[48:51], s96 offen offset:1024\n"
    "s_mov_b32 s97, 0x1d000\n"
    "buffer_load_dwordx4 v[76:79], v1, s[48:51], s97 offen offset:1024\n"
    "s_mov_b32 s96, 0x1000\n"
    "buffer_load_dwordx4 v[80:83], v1, s[48:51], s96 offen offset:2048\n"
    "s_mov_b32 s97, 0x1d000\n"
    "buffer_load_dwordx4 v[84:87], v1, s[48:51], s97 offen offset:2048\n"
    "s_mov_b32 s96, 0x1000\n"
    "buffer_load_dwordx4 v[88:91], v1, s[48:51], s96 offen offset:3072\n"
    "s_mov_b32 s97, 0x1d000\n"
    "buffer_load_dwordx4 v[92:95], v1, s[48:51], s97 offen offset:3072\n"
    "s_mov_b32 s96, 0x2000\n"
    "buffer_load_dwordx4 v[96:99], v1, s[48:51], s96 offen offset:0\n"
    "s_mov_b32 s97, 0x1e000\n"
    "buffer_load_dwordx4 v[100:103], v1, s[48:51], s97 offen offset:0\n"
    "s_mov_b32 s96, 0x2000\n"
    "buffer_load_dwordx4 v[104:107], v1, s[48:51], s96 offen offset:1024\n"
    "s_mov_b32 s97, 0x1e000\n"
    "buffer_load_dwordx4 v[108:111], v1, s[48:51], s97 offen offset:1024\n"
    "s_mov_b32 s96, 0x2000\n"
    "buffer_load_dwordx4 v[112:115], v1, s[48:51], s96 offen offset:2048\n"
    "s_mov_b32 s97, 0x1e000\n"
    "buffer_load_dwordx4 v[116:119], v1, s[48:51], s97 offen offset:2048\n"
    "s_mov_b32 s96, 0x2000\n"
    "buffer_load_dwordx4 v[120:123], v1, s[48:51], s96 offen offset:3072\n"
    "s_mov_b32 s97, 0x1e000\n"
    "buffer_load_dwordx4 v[124:127], v1, s[48:51], s97 offen offset:3072\n"
    "s_mov_b32 s68, 0\n"
    "v_mov_b32 v8, %[o2]\n"
    "LPA_%=:\n"
    "global_load_dword v5, v8, %[sync] sc1\n"
    "s_waitcnt vmcnt(0)\n"
    "v_readfirstlane_b32 s71, v5\n"
    "s_nop 3\n"
    "s_cmp_eq_u32 s71, 16\n"
    "s_cbranch_scc1 LPAD_%=\n"
    "s_sleep 1\n"
    "s_add_u32 s68, s68, 1\n"
    "s_cmp_lt_u32 s68, 0x40000\n"
    "s_cbranch_scc1 LPA_%=\n"
    "LPAD_%=:\n"
    "s_mov_b32 s74, 0\n"
    "buffer_load_dwordx4 a[0:3], v1, s[36:39], s74 offen offset:0 sc1\n"
    "s_mov_b32 s74, 0\n"
    "buffer_load_dwordx4 a[4:7], v1, s[36:39], s74 offen offset:1024 sc1\n"
    "s_mov_b32 s74, 0\n"
    "buffer_load_dwordx4 a[8:11], v1, s[36:39], s74 offen offset:2048 sc1\n"
    "s_mov_b32 s74, 0\n"
    "buffer_load_dwordx4 a[12:15], v1, s[36:39], s74 offen offset:3072 sc1\n"
    "s_mov_b32 s74, 0x1000\n"
    "buffer_load_dwordx4 a[16:19], v1, s[36:39], s74 offen offset:0 sc1\n"
    "s_mov_b32 s74, 0x1000\n"
    "buffer_load_dwordx4 a[20:23], v1, s[36:39], s74 offen offset:1024 sc1\n"
    "s_mov_b32 s74, 0x1000\n"
    "buffer_load_dwordx4 a[24:27], v1, s[36:39], s74 offen offset:2048 sc1\n"
    "s_mov_b32 s74, 0x1000\n"
    "buffer_load_dwordx4 a[28:31], v1, s[36:39], s74 offen offset:3072 sc1\n"
    "s_mov_b32 s74, 0x2000\n"
    "buffer_load_dwordx4 a[32:35], v1, s[36:39], s74 offen offset:0 sc1\n"
    "s_mov_b32 s74, 0x2000\n"
    "buffer_load_dwordx4 a[36:39], v1, s[36:39], s74 offen offset:1024 sc1\n"
    "s_mov_b32 s74, 0x2000\n"
    "buffer_load_dwordx4 a[40:43], v1, s[36:39], s74 offen offset:2048 sc1\n"
    "s_mov_b32 s74, 0x2000\n"
    "buffer_load_dwordx4 a[44:47], v1, s[36:39], s74 offen offset:3072 sc1\n"
    "s_mov_b32 s74, 0x3000\n"
    "buffer_load_dwordx4 a[48:51], v1, s[36:39], s74 offen offset:0 sc1\n"
    "s_mov_b32 s74, 0x3000\n"
    "buffer_load_dwordx4 a[52:55], v1, s[36:39], s74 offen offset:1024 sc1\n"
    "s_mov_b32 s74, 0x3000\n"
    "buffer_load_dwordx4 a[56:59], v1, s[36:39], s74 offen offset:2048 sc1\n"
    "s_mov_b32 s74, 0x3000\n"
    "buffer_load_dwordx4 a[60:63], v1, s[36:39], s74 offen offset:3072 sc1\n"
    "s_mov_b32 s74, 0x4000\n"
    "buffer_load_dwordx4 a[64:67], v1, s[36:39], s74 offen offset:0 sc1\n"
    "s_mov_b32 s74, 0x4000\n"
    "buffer_load_dwordx4 a[68:71], v1, s[36:39], s74 offen offset:1024 sc1\n"
    "s_mov_b32 s74, 0x4000\n"
    "buffer_load_dwordx4 a[72:75], v1, s[36:39], s74 offen offset:2048 sc1\n"
    "s_mov_b32 s74, 0x4000\n"
    "buffer_load_dwordx4 a[76:79], v1, s[36:39], s74 offen offset:3072 sc1\n"
    "s_mov_b32 s74, 0x5000\n"
    "buffer_load_dwordx4 a[80:83], v1, s[36:39], s74 offen offset:0 sc1\n"
    "s_mov_b32 s74, 0x5000\n"
    "buffer_load_dwordx4 a[84:87], v1, s[36:39], s74 offen offset:1024 sc1\n"
    "s_mov_b32 s74, 0x5000\n"
    "buffer_load_dwordx4 a[88:91], v1, s[36:39], s74 offen offset:2048 sc1\n"
    "s_mov_b32 s74, 0x5000\n"
    "buffer_load_dwordx4 a[92:95], v1, s[36:39], s74 offen offset:3072 sc1\n"
    "s_mov_b32 s74, 0x6000\n"
    "buffer_load_dwordx4 a[96:99], v1, s[36:39], s74 offen offset:0 sc1\n"
    "s_mov_b32 s74, 0x6000\n"
    "buffer_load_dwordx4 a[100:103], v1, s[36:39], s74 offen offset:1024 sc1\n"
    "s_mov_b32 s74, 0x6000\n"
    "buffer_load_dwordx4 a[104:107], v1, s[36:39], s74 offen offset:2048 sc1\n"
    "s_mov_b32 s74, 0x6000\n"
    "buffer_load_dwordx4 a[108:111], v1, s[36:39], s74 offen offset:3072 sc1\n"
    "s_waitcnt lgkmcnt(0)\n"
    "s_barrier\n"
    "s_waitcnt vmcnt(27)\n"
    "v_mfma_f32_32x32x16_bf16 v[128:143], a[112:115], a[0:3], 0\n"
    "ds_read_b128 a[176:179], v4 offset:0\n"
    "ds_read_b128 a[180:183], v4 offset:1024\n"
    "v_mfma_f32_32x32x16_bf16 v[144:159], a[116:119], a[0:3], 0\n"
    "ds_read_b128 a[184:187], v4 offset:2048\n"
    "ds_read_b128 a[188:191], v4 offset:3072\n"
    "s_mov_b32 m0, s60\n"
    "s_mov_b32 s70, 0x20000\n"
    "buffer_load_dwordx4 v2, s[40:43], s70 offen offset:0 lds\n"
    "v_mfma_f32_32x32x16_bf16 v[160:175], a[120:123], a[0:3], 0\n"
    "ds_read_b128 a[192:195], v4 offset:4096\n"
    "ds_read_b128 a[196:199], v4 offset:5120\n"
    "v_mfma_f32_32x32x16_bf16 v[176:191], a[124:127], a[0:3], 0\n"
    "ds_read_b128 a[200:203], v4 offset:6144\n"
    "ds_read_b128 a[204:207], v4 offset:7168\n"
    "s_waitcnt vmcnt(27)\n"
    "v_mfma_f32_32x32x16_bf16 v[128:143], a[128:131], a[4:7], v[128:143]\n"
    "ds_read_b128 a[208:211], v4 offset:8192\n"
    "v_mfma_f32_32x32x16_bf16 v[144:159], a[132:135], a[4:7], v[144:159]\n"
    "ds_read_b128 a[212:215], v4 offset:9216\n"
    "v_mfma_f32_32x32x16_bf16 v[160:175], a[136:139], a[4:7], v[160:175]\n"
    "ds_read_b128 a[216:219], v4 offset:10240\n"
    "v_mfma_f32_32x32x16_bf16 v[176:191], a[140:143], a[4:7], v[176:191]\n"
    "ds_read_b128 a[220:223], v4 offset:11264\n"
    "buffer_load_dwordx4 v2, s[40:43], s70 offen offset:1024 lds\n"
    "s_waitcnt vmcnt(27)\n"
    "v_mfma_f32_32x32x16_bf16 v[128:143], a[144:147], a[8:11], v[128:143]\n"
    "ds_read_b128 a[224:227], v4 offset:12288\n"
    "v_mfma_f32_32x32x16_bf16 v[144:159], a[148:151], a[8:11], v[144:159]\n"
    "ds_read_b128 a[228:231], v4 offset:13312\n"
    "v_mfma_f32_32x32x16_bf16 v[160:175], a[152:155], a[8:11], v[160:175]\n"
    "ds_read_b128 a[232:235], v4 offset:14336\n"
    "v_mfma_f32_32x32x16_bf16 v[176:191], a[156:159], a[8:11], v[176:191]\n"
    "ds_read_b128 a[236:239], v4 offset:15360\n"
    "s_waitcnt vmcnt(26)\n"
    "v_mfma_f32_32x32x16_bf16 v[128:143], a[160:163], a[12:15], v[128:143]\n"
    "v_mfma_f32_32x32x16_bf16 v[144:159], a[164:167], a[12:15], v[144:159]\n"
    "v_mfma_f32_32x32x16_bf16 v[160:175], a[168:171], a[12:15], v[160:175]\n"
    "ds_read_b128 a[112:115], v3 offset:16384\n"
    "buffer_load_dwordx4 v2, s[40:43], s70 offen offset:2048 lds\n"
    "v_mfma_f32_32x32x16_bf16 v[176:191], a[172:175], a[12:15], v[176:191]\n"
    "ds_read_b128 a[116:119], v3 offset:17408\n"
    "s_waitcnt vmcnt(26) lgkmcnt(3)\n"
    "v_mfma_f32_32x32x16_bf16 v[128:143], a[176:179], a[16:19], v[128:143]\n"
    "ds_read_b128 a[120:123], v3 offset:18432\n"
    "v_mfma_f32_32x32x16_bf16 v[144:159], a[180:183], a[16:19], v[144:159]\n"
    "ds_read_b128 a[124:127], v3 offset:19456\n"
    "v_mfma_f32_32x32x16_bf16 v[160:175], a[184:187], a[16:19], v[160:175]\n"
    "ds_read_b128 a[128:131], v3 offset:20480\n"
    "v_mfma_f32_32x32x16_bf16 v[176:191], a[188:191], a[16:19], v[176:191]\n"
    "ds_read_b128 a[132:135], v3 offset:21504\n"
    "s_waitcnt vmcnt(25)\n"
    "v_mfma_f32_32x32x16_bf16 v[128:143], a[192:195], a[20:23], v[128:143]\n"
    "ds_read_b128 a[136:139], v3 offset:22528\n"
    "v_mfma_f32_32x32x16_bf16 v[144:159], a[196:199], a[20:23], v[144:159]\n"
    "ds_read_b128 a[140:143], v3 offset:23552\n"
    "buffer_load_dwordx4 v2, s[40:43], s70 offen offset:3072 lds\n"
    "v_mfma_f32_32x32x16_bf16 v[160:175], a[200:203], a[20:23], v[160:175]\n"
    "ds_read_b128 a[144:147], v3 offset:24576\n"
    "v_mfma_f32_32x32x16_bf16 v[176:191], a[204:207], a[20:23], v[176:191]\n"
    "ds_read_b128 a[148:151], v3 offset:25600\n"
    "s_waitcnt vmcnt(25)\n"
    "v_mfma_f32_32x32x16_bf16 v[128:143], a[208:211], a[24:27], v[128:143]\n"
    "ds_read_b128 a[152:155], v3 offset:26624\n"
    "v_mfma_f32_32x32x16_bf16 v[144:159], a[212:215], a[24:27], v[144:159]\n"
    "ds_read_b128 a[156:159], v3 offset:27648\n"
    "v_mfma_f32_32x32x16_bf16 v[160:175], a[216:219], a[24:27], v[160:175]\n"
    "ds_read_b128 a[160:163], v3 offset:28672\n"
    "v_mfma_f32_32x32x16_bf16 v[176:191], a[220:223], a[24:27], v[176:191]\n"
    "ds_read_b128 a[164:167], v3 offset:29696\n"
    "s_waitcnt vmcnt(24)\n"
    "v_mfma_f32_32x32x16_bf16 v[128:143], a[224:227], a[28:31], v[128:143]\n"
    "ds_read_b128 a[168:171], v3 offset:30720\n"
    "ds_read_b128 a[172:175], v3 offset:31744\n"
    "v_mfma_f32_32x32x16_bf16 v[144:159], a[228:231], a[28:31], v[144:159]\n"
    "v_mfma_f32_32x32x16_bf16 v[160:175], a[232:235], a[28:31], v[160:175]\n"
    "s_waitcnt lgkmcnt(0)\n"
    "s_barrier\n"
    "v_mfma_f32_32x32x16_bf16 v[176:191], a[236:239], a[28:31], v[176:191]\n"
    "s_waitcnt vmcnt(23)\n"
    "v_mfma_f32_32x32x16_bf16 v[128:143], a[112:115], a[32:35], v[128:143]\n"
    "ds_read_b128 a[176:179], v4 offset:16384\n"
    "ds_read_b128 a[180:183], v4 offset:17408\n"
    "v_mfma_f32_32x32x16_bf16 v[144:159], a[116:119], a[32:35], v[144:159]\n"
    "ds_read_b128 a[184:187], v4 offset:18432\n"
    "ds_read_b128 a[188:191], v4 offset:19456\n"
    "s_mov_b32 m0, s64\n"
    "s_mov_b32 s70, 0x24000\n"
    "buffer_load_dwordx4 v2, s[40:43], s70 offen offset:0 lds\n"
    "v_mfma_f32_32x32x16_bf16 v[160:175], a[120:123], a[32:35], v[160:175]\n"
    "ds_read_b128 a[192:195], v4 offset:20480\n"
    "ds_read_b128 a[196:199], v4 offset:21504\n"
    "v_mfma_f32_32x32x16_bf16 v[176:191], a[124:127], a[32:35], v[176:191]\n"
    "ds_read_b128 a[200:203], v4 offset:22528\n"
    "ds_read_b128 a[204:207], v4 offset:23552\n"
    "s_waitcnt vmcnt(23)\n"
    "v_mfma_f32_32x32x16_bf16 v[128:143], a[128:131], a[36:39], v[128:143]\n"
    "ds_read_b128 a[208:211], v4 offset:24576\n"
    "buffer_load_dwordx4 v2, s[40:43], s70 offen offset:1024 lds\n"
    "v_mfma_f32_32x32x16_bf16 v[144:159], a[132:135], a[36:39], v[144:159]\n"
    "ds_read_b128 a[212:215], v4 offset:25600\n"
    "v_mfma_f32_32x32x16_bf16 v[160:175], a[136:139], a[36:39], v[160:175]\n"
    "ds_read_b128 a[216:219], v4 offset:26624\n"
    "v_mfma_f32_32x32x16_bf16 v[176:191], a[140:143], a[36:39], v[176:191]\n"
    "ds_read_b128 a[220:223], v4 offset:27648\n"
    "buffer_load_dwordx4 v2, s[40:43], s70 offen offset:2048 lds\n"
    "s_waitcnt vmcnt(24)\n"
    "v_mfma_f32_32x32x16_bf16 v[128:143], a[144:147], a[40:43], v[128:143]\n"
    "ds_read_b128 a[224:227], v4 offset:28672\n"
    "v_mfma_f32_32x32x16_bf16 v[144:159], a[148:151], a[40:43], v[144:159]\n"
    "ds_read_b128 a[228:231], v4 offset:29696\n"
    "v_mfma_f32_32x32x16_bf16 v[160:175], a[152:155], a[40:43], v[160:175]\n"
    "ds_read_b128 a[232:235], v4 offset:30720\n"
    "v_mfma_f32_32x32x16_bf16 v[176:191], a[156:159], a[40:43], v[176:191]\n"
    "ds_read_b128 a[236:239], v4 offset:31744\n"
    "buffer_load_dwordx4 v2, s[40:43], s70 offen offset:3072 lds\n"
    "s_waitcnt vmcnt(24)\n"
    "v_mfma_f32_32x32x16_bf16 v[128:143], a[160:163], a[44:47], v[128:143]\n"
    "v_mfma_f32_32x32x16_bf16 v[144:159], a[164:167], a[44:47], v[144:159]\n"
    "v_mfma_f32_32x32x16_bf16 v[160:175], a[168:171], a[44:47], v[160:175]\n"
    "ds_read_b128 a[112:115], v3 offset:32768\n"
    "s_mov_b32 m0, s61\n"
    "s_mov_b32 s70, 0x28000\n"
    "buffer_load_dwordx4 v2, s[40:43], s70 offen offset:0 lds\n"
    "v_mfma_f32_32x32x16_bf16 v[176:191], a[172:175], a[44:47], v[176:191]\n"
    "ds_read_b128 a[116:119], v3 offset:33792\n"
    "s_waitcnt vmcnt(24) lgkmcnt(2)\n"
    "v_mfma_f32_32x32x16_bf16 v[128:143], a[176:179], a[48:51], v[128:143]\n"
    "ds_read_b128 a[120:123], v3 offset:34816\n"
    "v_mfma_f32_32x32x16_bf16 v[144:159], a[180:183], a[48:51], v[144:159]\n"
    "ds_read_b128 a[124:127], v3 offset:35840\n"
    "buffer_load_dwordx4 v2, s[40:43], s70 offen offset:1024 lds\n"
    "v_mfma_f32_32x32x16_bf16 v[160:175], a[184:187], a[48:51], v[160:175]\n"
    "ds_read_b128 a[128:131], v3 offset:36864\n"
    "v_mfma_f32_32x32x16_bf16 v[176:191], a[188:191], a[48:51], v[176:191]\n"
    "ds_read_b128 a[132:135], v3 offset:37888\n"
    "s_waitcnt vmcnt(24)\n"
    "v_mfma_f32_32x32x16_bf16 v[128:143], a[192:195], a[52:55], v[128:143]\n"
    "ds_read_b128 a[136:139], v3 offset:38912\n"
    "v_mfma_f32_32x32x16_bf16 v[144:159], a[196:199], a[52:55], v[144:159]\n"
    "ds_read_b128 a[140:143], v3 offset:39936\n"
    "buffer_load_dwordx4 v2, s[40:43], s70 offen offset:2048 lds\n"
    "v_mfma_f32_32x32x16_bf16 v[160:175], a[200:203], a[52:55], v[160:175]\n"
    "ds_read_b128 a[144:147], v3 offset:40960\n"
    "v_mfma_f32_32x32x16_bf16 v[176:191], a[204:207], a[52:55], v[176:191]\n"
    "ds_read_b128 a[148:151], v3 offset:41984\n"
    "s_waitcnt vmcnt(24)\n"
    "v_mfma_f32_32x32x16_bf16 v[128:143], a[208:211], a[56:59], v[128:143]\n"
    "ds_read_b128 a[152:155], v3 offset:43008\n"
    "buffer_load_dwordx4 v2, s[40:43], s70 offen offset:3072 lds\n"
    "v_mfma_f32_32x32x16_bf16 v[144:159], a[212:215], a[56:59], v[144:159]\n"
    "ds_read_b128 a[156:159], v3 offset:44032\n"
    "v_mfma_f32_32x32x16_bf16 v[160:175], a[216:219], a[56:59], v[160:175]\n"
    "ds_read_b128 a[160:163], v3 offset:45056\n"
    "v_mfma_f32_32x32x16_bf16 v[176:191], a[220:223], a[56:59], v[176:191]\n"
    "ds_read_b128 a[164:167], v3 offset:46080\n"
    "s_waitcnt vmcnt(24)\n"
    "v_mfma_f32_32x32x16_bf16 v[128:143], a[224:227], a[60:63], v[128:143]\n"
    "ds_read_b128 a[168:171], v3 offset:47104\n"
    "ds_read_b128 a[172:175], v3 offset:48128\n"
    "v_mfma_f32_32x32x16_bf16 v[144:159], a[228:231], a[60:63], v[144:159]\n"
    "v_mfma_f32_32x32x16_bf16 v[160:175], a[232:235], a[60:63], v[160:175]\n"
    "s_waitcnt lgkmcnt(0)\n"
    "s_barrier\n"
    "v_mfma_f32_32x32x16_bf16 v[176:191], a[236:239], a[60:63], v[176:191]\n"
    "s_waitcnt vmcnt(23)\n"
    "v_mfma_f32_32x32x16_bf16 v[128:143], a[112:115], a[64:67], v[128:143]\n"
    "ds_read_b128 a[176:179], v4 offset:32768\n"
    "ds_read_b128 a[180:183], v4 offset:33792\n"
    "v_mfma_f32_32x32x16_bf16 v[144:159], a[116:119], a[64:67], v[144:159]\n"
    "ds_read_b128 a[184:187], v4 offset:34816\n"
    "ds_read_b128 a[188:191], v4 offset:35840\n"
    "s_mov_b32 m0, s65\n"
    "s_mov_b32 s70, 0x2c000\n"
    "buffer_load_dwordx4 v2, s[40:43], s70 offen offset:0 lds\n"
    "v_mfma_f32_32x32x16_bf16 v[160:175], a[120:123], a[64:67], v[160:175]\n"
    "ds_read_b128 a[192:195], v4 offset:36864\n"
    "ds_read_b128 a[196:199], v4 offset:37888\n"
    "v_mfma_f32_32x32x16_bf16 v[176:191], a[124:127], a[64:67], v[176:191]\n"
    "ds_read_b128 a[200:203], v4 offset:38912\n"
    "ds_read_b128 a[204:207], v4 offset:39936\n"
    "s_waitcnt vmcnt(23)\n"
    "v_mfma_f32_32x32x16_bf16 v[128:143], a[128:131], a[68:71], v[128:143]\n"
    "ds_read_b128 a[208:211], v4 offset:40960\n"
    "buffer_load_dwordx4 v2, s[40:43], s70 offen offset:1024 lds\n"
    "v_mfma_f32_32x32x16_bf16 v[144:159], a[132:135], a[68:71], v[144:159]\n"
    "ds_read_b128 a[212:215], v4 offset:41984\n"
    "v_mfma_f32_32x32x16_bf16 v[160:175], a[136:139], a[68:71], v[160:175]\n"
    "ds_read_b128 a[216:219], v4 offset:43008\n"
    "v_mfma_f32_32x32x16_bf16 v[176:191], a[140:143], a[68:71], v[176:191]\n"
    "ds_read_b128 a[220:223], v4 offset:44032\n"
    "buffer_load_dwordx4 v2, s[40:43], s70 offen offset:2048 lds\n"
    "s_waitcnt vmcnt(24)\n"
    "v_mfma_f32_32x32x16_bf16 v[128:143], a[144:147], a[72:75], v[128:143]\n"
    "ds_read_b128 a[224:227], v4 offset:45056\n"
    "v_mfma_f32_32x32x16_bf16 v[144:159], a[148:151], a[72:75], v[144:159]\n"
    "ds_read_b128 a[228:231], v4 offset:46080\n"
    "v_mfma_f32_32x32x16_bf16 v[160:175], a[152:155], a[72:75], v[160:175]\n"
    "ds_read_b128 a[232:235], v4 offset:47104\n"
    "v_mfma_f32_32x32x16_bf16 v[176:191], a[156:159], a[72:75], v[176:191]\n"
    "ds_read_b128 a[236:239], v4 offset:48128\n"
    "buffer_load_dwordx4 v2, s[40:43], s70 offen offset:3072 lds\n"
    "s_waitcnt vmcnt(24)\n"
    "v_mfma_f32_32x32x16_bf16 v[128:143], a[160:163], a[76:79], v[128:143]\n"
    "v_mfma_f32_32x32x16_bf16 v[144:159], a[164:167], a[76:79], v[144:159]\n"
    "v_mfma_f32_32x32x16_bf16 v[160:175], a[168:171], a[76:79], v[160:175]\n"
    "ds_read_b128 a[112:115], v3 offset:49152\n"
    "s_mov_b32 m0, s62\n"
    "s_mov_b32 s70, 0x30000\n"
    "buffer_load_dwordx4 v2, s[40:43], s70 offen offset:0 lds\n"
    "v_mfma_f32_32x32x16_bf16 v[176:191], a[172:175], a[76:79], v[176:191]\n"
    "ds_read_b128 a[116:119], v3 offset:50176\n"
    "s_waitcnt vmcnt(24) lgkmcnt(2)\n"
    "v_mfma_f32_32x32x16_bf16 v[128:143], a[176:179], a[80:83], v[128:143]\n"
    "ds_read_b128 a[120:123], v3 offset:51200\n"
    "v_mfma_f32_32x32x16_bf16 v[144:159], a[180:183], a[80:83], v[144:159]\n"
    "ds_read_b128 a[124:127], v3 offset:52224\n"
    "buffer_load_dwordx4 v2, s[40:43], s70 offen offset:1024 lds\n"
    "v_mfma_f32_32x32x16_bf16 v[160:175], a[184:187], a[80:83], v[160:175]\n"
    "ds_read_b128 a[128:131], v3 offset:53248\n"
    "v_mfma_f32_32x32x16_bf16 v[176:191], a[188:191], a[80:83], v[176:191]\n"
    "ds_read_b128 a[132:135], v3 offset:54272\n"
    "s_waitcnt vmcnt(24)\n"
    "v_mfma_f32_32x32x16_bf16 v[128:143], a[192:195], a[84:87], v[128:143]\n"
    "ds_read_b128 a[136:139], v3 offset:55296\n"
    "v_mfma_f32_32x32x16_bf16 v[144:159], a[196:199], a[84:87], v[144:159]\n"
    "ds_read_b128 a[140:143], v3 offset:56320\n"
    "buffer_load_dwordx4 v2, s[40:43], s70 offen offset:2048 lds\n"
    "v_mfma_f32_32x32x16_bf16 v[160:175], a[200:203], a[84:87], v[160:175]\n"
    "ds_read_b128 a[144:147], v3 offset:57344\n"
    "v_mfma_f32_32x32x16_bf16 v[176:191], a[204:207], a[84:87], v[176:191]\n"
    "ds_read_b128 a[148:151], v3 offset:58368\n"
    "s_waitcnt vmcnt(24)\n"
    "v_mfma_f32_32x32x16_bf16 v[128:143], a[208:211], a[88:91], v[128:143]\n"
    "ds_read_b128 a[152:155], v3 offset:59392\n"
    "buffer_load_dwordx4 v2, s[40:43], s70 offen offset:3072 lds\n"
    "v_mfma_f32_32x32x16_bf16 v[144:159], a[212:215], a[88:91], v[144:159]\n"
    "ds_read_b128 a[156:159], v3 offset:60416\n"
    "v_mfma_f32_32x32x16_bf16 v[160:175], a[216:219], a[88:91], v[160:175]\n"
    "ds_read_b128 a[160:163], v3 offset:61440\n"
    "v_mfma_f32_32x32x16_bf16 v[176:191], a[220:223], a[88:91], v[176:191]\n"
    "ds_read_b128 a[164:167], v3 offset:62464\n"
    "s_waitcnt vmcnt(24)\n"
    "v_mfma_f32_32x32x16_bf16 v[128:143], a[224:227], a[92:95], v[128:143]\n"
    "ds_read_b128 a[168:171], v3 offset:63488\n"
    "ds_read_b128 a[172:175], v3 offset:64512\n"
    "v_mfma_f32_32x32x16_bf16 v[144:159], a[228:231], a[92:95], v[144:159]\n"
    "v_mfma_f32_32x32x16_bf16 v[160:175], a[232:235], a[92:95], v[160:175]\n"
    "s_waitcnt vmcnt(16) lgkmcnt(0)\n"
    "s_barrier\n"
    "v_mfma_f32_32x32x16_bf16 v[176:191], a[236:239], a[92:95], v[176:191]\n"
    "v_mfma_f32_32x32x16_bf16 v[128:143], a[112:115], a[96:99], v[128:143]\n"
    "ds_read_b128 a[176:179], v4 offset:49152\n"
    "ds_read_b128 a[180:183], v4 offset:50176\n"
    "v_mfma_f32_32x32x16_bf16 v[144:159], a[116:119], a[96:99], v[144:159]\n"
    "ds_read_b128 a[184:187], v4 offset:51200\n"
    "ds_read_b128 a[188:191], v4 offset:52224\n"
    "s_mov_b32 m0, s66\n"
    "s_mov_b32 s70, 0x34000\n"
    "buffer_load_dwordx4 v2, s[40:43], s70 offen offset:0 lds\n"
    "v_mfma_f32_32x32x16_bf16 v[160:175], a[120:123], a[96:99], v[160:175]\n"
    "ds_read_b128 a[192:195], v4 offset:53248\n"
    "ds_read_b128 a[196:199], v4 offset:54272\n"
    "v_mfma_f32_32x32x16_bf16 v[176:191], a[124:127], a[96:99], v[176:191]\n"
    "ds_read_b128 a[200:203], v4 offset:55296\n"
    "ds_read_b128 a[204:207], v4 offset:56320\n"
    "v_mfma_f32_32x32x16_bf16 v[128:143], a[128:131], a[100:103], v[128:143]\n"
    "ds_read_b128 a[208:211], v4 offset:57344\n"
    "v_mfma_f32_32x32x16_bf16 v[144:159], a[132:135], a[100:103], v[144:159]\n"
    "ds_read_b128 a[212:215], v4 offset:58368\n"
    "v_mfma_f32_32x32x16_bf16 v[160:175], a[136:139], a[100:103], v[160:175]\n"
    "ds_read_b128 a[216:219], v4 offset:59392\n"
    "v_mfma_f32_32x32x16_bf16 v[176:191], a[140:143], a[100:103], v[176:191]\n"
    "ds_read_b128 a[220:223], v4 offset:60416\n"
    "buffer_load_dwordx4 v2, s[40:43], s70 offen offset:1024 lds\n"
    "v_mfma_f32_32x32x16_bf16 v[128:143], a[144:147], a[104:107], v[128:143]\n"
    "ds_read_b128 a[224:227], v4 offset:61440\n"
    "v_mfma_f32_32x32x16_bf16 v[144:159], a[148:151], a[104:107], v[144:159]\n"
    "ds_read_b128 a[228:231], v4 offset:62464\n"
    "v_mfma_f32_32x32x16_bf16 v[160:175], a[152:155], a[104:107], v[160:175]\n"
    "ds_read_b128 a[232:235], v4 offset:63488\n"
    "v_mfma_f32_32x32x16_bf16 v[176:191], a[156:159], a[104:107], v[176:191]\n"
    "ds_read_b128 a[236:239], v4 offset:64512\n"
    "v_mfma_f32_32x32x16_bf16 v[128:143], a[160:163], a[108:111], v[128:143]\n"
    "v_mfma_f32_32x32x16_bf16 v[144:159], a[164:167], a[108:111], v[144:159]\n"
    "v_mfma_f32_32x32x16_bf16 v[160:175], a[168:171], a[108:111], v[160:175]\n"
    "ds_read_b128 a[112:115], v3 offset:0\n"
    "buffer_load_dwordx4 v2, s[40:43], s70 offen offset:2048 lds\n"
    "v_mfma_f32_32x32x16_bf16 v[176:191], a[172:175], a[108:111], v[176:191]\n"
    "ds_read_b128 a[116:119], v3 offset:1024\n"
    "s_waitcnt lgkmcnt(3)\n"
    "v_mfma_f32_32x32x16_bf16 v[192:207], a[176:179], a[0:3], 0\n"
    "ds_read_b128 a[120:123], v3 offset:2048\n"
    "v_mfma_f32_32x32x16_bf16 v[208:223], a[180:183], a[0:3], 0\n"
    "ds_read_b128 a[124:127], v3 offset:3072\n"
    "v_mfma_f32_32x32x16_bf16 v[192:207], a[184:187], a[4:7], v[192:207]\n"
    "ds_read_b128 a[128:131], v3 offset:4096\n"
    "v_mfma_f32_32x32x16_bf16 v[208:223], a[188:191], a[4:7], v[208:223]\n"
    "ds_read_b128 a[132:135], v3 offset:5120\n"
    "v_mfma_f32_32x32x16_bf16 v[192:207], a[192:195], a[8:11], v[192:207]\n"
    "ds_read_b128 a[136:139], v3 offset:6144\n"
    "v_mfma_f32_32x32x16_bf16 v[208:223], a[196:199], a[8:11], v[208:223]\n"
    "ds_read_b128 a[140:143], v3 offset:7168\n"
    "buffer_load_dwordx4 v2, s[40:43], s70 offen offset:3072 lds\n"
    "v_mul_f32 v8, s73, v128\n"
    "v_exp_f32 v8, v8\n"
    "v_mul_f32 v9, s73, v129\n"
    "v_mfma_f32_32x32x16_bf16 v[192:207], a[200:203], a[12:15], v[192:207]\n"
    "ds_read_b128 a[144:147], v3 offset:8192\n"
    "v_mul_f32 v128, v128, v144\n"
    "v_exp_f32 v9, v9\n"
    "v_mul_f32 v10, s73, v130\n"
    "v_add_f32 v8, 1.0, v8\n"
    "v_mfma_f32_32x32x16_bf16 v[208:223], a[204:207], a[12:15], v[208:223]\n"
    "ds_read_b128 a[148:151], v3 offset:9216\n"
    "v_mul_f32 v129, v129, v145\n"
    "v_exp_f32 v10, v10\n"
    "v_mul_f32 v11, s73, v131\n"
    "v_rcp_f32 v8, v8\n"
    "v_mfma_f32_32x32x16_bf16 v[192:207], a[208:211], a[16:19], v[192:207]\n"
    "ds_read_b128 a[152:155], v3 offset:10240\n"
    "v_add_f32 v9, 1.0, v9\n"
    "v_mul_f32 v130, v130, v146\n"
    "v_exp_f32 v11, v11\n"
    "v_mul_f32 v12, s73, v132\n"
    "v_mfma_f32_32x32x16_bf16 v[208:223], a[212:215], a[16:19], v[208:223]\n"
    "ds_read_b128 a[156:159], v3 offset:11264\n"
    "v_rcp_f32 v9, v9\n"
    "v_add_f32 v10, 1.0, v10\n"
    "v_mul_f32 v131, v131, v147\n"
    "v_exp_f32 v12, v12\n"
    "v_mfma_f32_32x32x16_bf16 v[192:207], a[216:219], a[20:23], v[192:207]\n"
    "ds_read_b128 a[160:163], v3 offset:12288\n"
    "v_mul_f32 v13, s73, v133\n"
    "v_mul_f32 v128, v128, v8\n"
    "v_rcp_f32 v10, v10\n"
    "v_add_f32 v11, 1.0, v11\n"
    "v_mfma_f32_32x32x16_bf16 v[208:223], a[220:223], a[20:23], v[208:223]\n"
    "ds_read_b128 a[164:167], v3 offset:13312\n"
    "v_mul_f32 v132, v132, v148\n"
    "v_exp_f32 v13, v13\n"
    "v_mul_f32 v14, s73, v134\n"
    "v_mul_f32 v129, v129, v9\n"
    "v_mfma_f32_32x32x16_bf16 v[192:207], a[224:227], a[24:27], v[192:207]\n"
    "ds_read_b128 a[168:171], v3 offset:14336\n"
    "ds_read_b128 a[172:175], v3 offset:15360\n"
    "v_rcp_f32 v11, v11\n"
    "v_add_f32 v12, 1.0, v12\n"
    "v_mul_f32 v133, v133, v149\n"
    "v_mfma_f32_32x32x16_bf16 v[208:223], a[228:231], a[24:27], v[208:223]\n"
    "v_exp_f32 v14, v14\n"
    "v_mul_f32 v15, s73, v135\n"
    "v_mul_f32 v130, v130, v10\n"
    "v_rcp_f32 v12, v12\n"
    "v_mfma_f32_32x32x16_bf16 v[192:207], a[232:235], a[28:31], v[192:207]\n"
    "s_waitcnt vmcnt(12) lgkmcnt(0)\n"
    "s_barrier\n"
    "v_add_f32 v13, 1.0, v13\n"
    "v_mul_f32 v134, v134, v150\n"
    "v_exp_f32 v15, v15\n"
    "v_mfma_f32_32x32x16_bf16 v[208:223], a[236:239], a[28:31], v[208:223]\n"
    "v_mul_f32 v8, s73, v136\n"
    "v_cvt_pk_bf16_f32 v16, v128, v129\n"
    "v_mul_f32 v131, v131, v11\n"
    "v_rcp_f32 v13, v13\n"
    "v_mfma_f32_32x32x16_bf16 v[192:207], a[112:115], a[32:35], v[192:207]\n"
    "ds_read_b128 a[176:179], v4 offset:0\n"
    "ds_read_b128 a[180:183], v4 offset:1024\n"
    "v_add_f32 v14, 1.0, v14\n"
    "v_mul_f32 v135, v135, v151\n"
    "v_exp_f32 v8, v8\n"
    "v_mfma_f32_32x32x16_bf16 v[208:223], a[116:119], a[32:35], v[208:223]\n"
    "ds_read_b128 a[184:187], v4 offset:2048\n"
    "ds_read_b128 a[188:191], v4 offset:3072\n"
    "v_mul_f32 v9, s73, v137\n"
    "v_mul_f32 v132, v132, v12\n"
    "v_rcp_f32 v14, v14\n"
    "v_mfma_f32_32x32x16_bf16 v[192:207], a[120:123], a[36:39], v[192:207]\n"
    "ds_read_b128 a[192:195], v4 offset:4096\n"
    "ds_read_b128 a[196:199], v4 offset:5120\n"
    "v_add_f32 v15, 1.0, v15\n"
    "v_mul_f32 v136, v136, v152\n"
    "v_exp_f32 v9, v9\n"
    "v_mfma_f32_32x32x16_bf16 v[208:223], a[124:127], a[36:39], v[208:223]\n"
    "ds_read_b128 a[200:203], v4 offset:6144\n"
    "ds_read_b128 a[204:207], v4 offset:7168\n"
    "v_mul_f32 v10, s73, v138\n"
    "v_cvt_pk_bf16_f32 v17, v130, v131\n"
    "v_mul_f32 v133, v133, v13\n"
    "v_mfma_f32_32x32x16_bf16 v[192:207], a[128:131], a[40:43], v[192:207]\n"
    "ds_read_b128 a[208:211], v4 offset:8192\n"
    "v_rcp_f32 v15, v15\n"
    "v_add_f32 v8, 1.0, v8\n"
    "v_mul_f32 v137, v137, v153\n"
    "v_exp_f32 v10, v10\n"
    "v_mfma_f32_32x32x16_bf16 v[208:223], a[132:135], a[40:43], v[208:223]\n"
    "ds_read_b128 a[212:215], v4 offset:9216\n"
    "v_mul_f32 v11, s73, v139\n"
    "v_mul_f32 v134, v134, v14\n"
    "v_rcp_f32 v8, v8\n"
    "v_add_f32 v9, 1.0, v9\n"
    "v_mfma_f32_32x32x16_bf16 v[192:207], a[136:139], a[44:47], v[192:207]\n"
    "ds_read_b128 a[216:219], v4 offset:10240\n"
    "v_mul_f32 v138, v138, v154\n"
    "v_exp_f32 v11, v11\n"
    "v_mul_f32 v12, s73, v140\n"
    "v_cvt_pk_bf16_f32 v18, v132, v133\n"
    "v_mfma_f32_32x32x16_bf16 v[208:223], a[140:143], a[44:47], v[208:223]\n"
    "ds_read_b128 a[220:223], v4 offset:11264\n"
    "v_mul_f32 v135, v135, v15\n"
    "v_rcp_f32 v9, v9\n"
    "v_add_f32 v10, 1.0, v10\n"
    "v_mul_f32 v139, v139, v155\n"
    "v_mfma_f32_32x32x16_bf16 v[192:207], a[144:147], a[48:51], v[192:207]\n"
    "ds_read_b128 a[224:227], v4 offset:12288\n"
    "v_exp_f32 v12, v12\n"
    "v_mul_f32 v13, s73, v141\n"
    "v_mul_f32 v136, v136, v8\n"
    "v_rcp_f32 v10, v10\n"
    "v_mfma_f32_32x32x16_bf16 v[208:223], a[148:151], a[48:51], v[208:223]\n"
    "ds_read_b128 a[228:231], v4 offset:13312\n"
    "v_add_f32 v11, 1.0, v11\n"
    "v_mul_f32 v140, v140, v156\n"
    "v_exp_f32 v13, v13\n"
    "v_mul_f32 v14, s73, v142\n"
    "v_mfma_f32_32x32x16_bf16 v[192:207], a[152:155], a[52:55], v[192:207]\n"
    "ds_read_b128 a[232:235], v4 offset:14336\n"
    "v_cvt_pk_bf16_f32 v19, v134, v135\n"
    "buffer_store_dwordx4 v[16:19], v1, s[44:47], 0 offen offset:0 sc1\n"
    "v_mul_f32 v137, v137, v9\n"
    "v_rcp_f32 v11, v11\n"
    "v_mfma_f32_32x32x16_bf16 v[208:223], a[156:159], a[52:55], v[208:223]\n"
    "ds_read_b128 a[236:239], v4 offset:15360\n"
    "v_add_f32 v12, 1.0, v12\n"
    "v_mul_f32 v141, v141, v157\n"
    "v_exp_f32 v14, v14\n"
    "v_mul_f32 v15, s73, v143\n"
    "v_mfma_f32_32x32x16_bf16 v[192:207], a[160:163], a[56:59], v[192:207]\n"
    "v_mul_f32 v138, v138, v10\n"
    "v_rcp_f32 v12, v12\n"
    "v_add_f32 v13, 1.0, v13\n"
    "v_mul_f32 v142, v142, v158\n"
    "v_mfma_f32_32x32x16_bf16 v[208:223], a[164:167], a[56:59], v[208:223]\n"
    "v_exp_f32 v15, v15\n"
    "v_cvt_pk_bf16_f32 v20, v136, v137\n"
    "v_mul_f32 v139, v139, v11\n"
    "v_rcp_f32 v13, v13\n"
    "v_mfma_f32_32x32x16_bf16 v[192:207], a[168:171], a[60:63], v[192:207]\n"
    "ds_read_b128 a[112:115], v3 offset:16384\n"
    "v_add_f32 v14, 1.0, v14\n"
    "v_mul_f32 v143, v143, v159\n"
    "v_mul_f32 v140, v140, v12\n"
    "v_rcp_f32 v14, v14\n"
    "v_mfma_f32_32x32x16_bf16 v[208:223], a[172:175], a[60:63], v[208:223]\n"
    "ds_read_b128 a[116:119], v3 offset:17408\n"
    "v_add_f32 v15, 1.0, v15\n"
    "v_cvt_pk_bf16_f32 v21, v138, v139\n"
    "v_mul_f32 v141, v141, v13\n"
    "v_rcp_f32 v15, v15\n"
    "s_waitcnt lgkmcnt(1)\n"
    "v_mfma_f32_32x32x16_bf16 v[192:207], a[176:179], a[64:67], v[192:207]\n"
    "ds_read_b128 a[120:123], v3 offset:18432\n"
    "v_mul_f32 v142, v142, v14\n"
    "v_cvt_pk_bf16_f32 v22, v140, v141\n"
    "v_mul_f32 v143, v143, v15\n"
    "v_cvt_pk_bf16_f32 v23, v142, v143\n"
    "v_mfma_f32_32x32x16_bf16 v[208:223], a[180:183], a[64:67], v[208:223]\n"
    "ds_read_b128 a[124:127], v3 offset:19456\n"
    "buffer_store_dwordx4 v[20:23], v1, s[44:47], 0 offen offset:1024 sc1\n"
    "v_mul_f32 v8, s73, v160\n"
    "v_exp_f32 v8, v8\n"
    "v_mul_f32 v9, s73, v161\n"
    "v_mfma_f32_32x32x16_bf16 v[192:207], a[184:187], a[68:71], v[192:207]\n"
    "ds_read_b128 a[128:131], v3 offset:20480\n"
    "v_mul_f32 v160, v160, v176\n"
    "v_exp_f32 v9, v9\n"
    "v_mul_f32 v10, s73, v162\n"
    "v_add_f32 v8, 1.0, v8\n"
    "v_mfma_f32_32x32x16_bf16 v[208:223], a[188:191], a[68:71], v[208:223]\n"
    "ds_read_b128 a[132:135], v3 offset:21504\n"
    "v_mul_f32 v161, v161, v177\n"
    "v_exp_f32 v10, v10\n"
    "v_mul_f32 v11, s73, v163\n"
    "v_rcp_f32 v8, v8\n"
    "v_mfma_f32_32x32x16_bf16 v[192:207], a[192:195], a[72:75], v[192:207]\n"
    "ds_read_b128 a[136:139], v3 offset:22528\n"
    "v_add_f32 v9, 1.0, v9\n"
    "v_mul_f32 v162, v162, v178\n"
    "v_exp_f32 v11, v11\n"
    "v_mul_f32 v12, s73, v164\n"
    "v_mfma_f32_32x32x16_bf16 v[208:223], a[196:199], a[72:75], v[208:223]\n"
    "ds_read_b128 a[140:143], v3 offset:23552\n"
    "v_rcp_f32 v9, v9\n"
    "v_add_f32 v10, 1.0, v10\n"
    "v_mul_f32 v163, v163, v179\n"
    "v_exp_f32 v12, v12\n"
    "v_mfma_f32_32x32x16_bf16 v[192:207], a[200:203], a[76:79], v[192:207]\n"
    "ds_read_b128 a[144:147], v3 offset:24576\n"
    "v_mul_f32 v13, s73, v165\n"
    "v_mul_f32 v160, v160, v8\n"
    "v_rcp_f32 v10, v10\n"
    "v_add_f32 v11, 1.0, v11\n"
    "v_mfma_f32_32x32x16_bf16 v[208:223], a[204:207], a[76:79], v[208:223]\n"
    "ds_read_b128 a[148:151], v3 offset:25600\n"
    "v_mul_f32 v164, v164, v180\n"
    "v_exp_f32 v13, v13\n"
    "v_mul_f32 v14, s73, v166\n"
    "v_mul_f32 v161, v161, v9\n"
    "v_mfma_f32_32x32x16_bf16 v[192:207], a[208:211], a[80:83], v[192:207]\n"
    "ds_read_b128 a[152:155], v3 offset:26624\n"
    "v_rcp_f32 v11, v11\n"
    "v_add_f32 v12, 1.0, v12\n"
    "v_mul_f32 v165, v165, v181\n"
    "v_exp_f32 v14, v14\n"
    "v_mfma_f32_32x32x16_bf16 v[208:223], a[212:215], a[80:83], v[208:223]\n"
    "ds_read_b128 a[156:159], v3 offset:27648\n"
    "v_mul_f32 v15, s73, v167\n"
    "v_mul_f32 v162, v162, v10\n"
    "v_rcp_f32 v12, v12\n"
    "v_add_f32 v13, 1.0, v13\n"
    "v_mfma_f32_32x32x16_bf16 v[192:207], a[216:219], a[84:87], v[192:207]\n"
    "ds_read_b128 a[160:163], v3 offset:28672\n"
    "v_mul_f32 v166, v166, v182\n"
    "v_exp_f32 v15, v15\n"
    "v_mul_f32 v8, s73, v168\n"
    "v_cvt_pk_bf16_f32 v24, v160, v161\n"
    "v_mfma_f32_32x32x16_bf16 v[208:223], a[220:223], a[84:87], v[208:223]\n"
    "ds_read_b128 a[164:167], v3 offset:29696\n"
    "v_mul_f32 v163, v163, v11\n"
    "v_rcp_f32 v13, v13\n"
    "v_add_f32 v14, 1.0, v14\n"
    "v_mul_f32 v167, v167, v183\n"
    "v_mfma_f32_32x32x16_bf16 v[192:207], a[224:227], a[88:91], v[192:207]\n"
    "ds_read_b128 a[168:171], v3 offset:30720\n"
    "ds_read_b128 a[172:175], v3 offset:31744\n"
    "v_exp_f32 v8, v8\n"
    "v_mul_f32 v9, s73, v169\n"
    "v_mul_f32 v164, v164, v12\n"
    "v_mfma_f32_32x32x16_bf16 v[208:223], a[228:231], a[88:91], v[208:223]\n"
    "v_rcp_f32 v14, v14\n"
    "v_add_f32 v15, 1.0, v15\n"
    "v_mul_f32 v168, v168, v184\n"
    "v_exp_f32 v9, v9\n"
    "v_mfma_f32_32x32x16_bf16 v[192:207], a[232:235], a[92:95], v[192:207]\n"
    "s_waitcnt vmcnt(6) lgkmcnt(0)\n"
    "s_barrier\n"
    "v_mul_f32 v10, s73, v170\n"
    "v_cvt_pk_bf16_f32 v25, v162, v163\n"
    "v_mul_f32 v165, v165, v13\n"
    "v_mfma_f32_32x32x16_bf16 v[208:223], a[236:239], a[92:95], v[208:223]\n"
    "v_rcp_f32 v15, v15\n"
    "v_add_f32 v8, 1.0, v8\n"
    "v_mul_f32 v169, v169, v185\n"
    "v_exp_f32 v10, v10\n"
    "v_mfma_f32_32x32x16_bf16 v[192:207], a[112:115], a[96:99], v[192:207]\n"
    "ds_read_b128 a[176:179], v4 offset:16384\n"
    "ds_read_b128 a[180:183], v4 offset:17408\n"
    "v_mul_f32 v11, s73, v171\n"
    "v_mul_f32 v166, v166, v14\n"
    "v_rcp_f32 v8, v8\n"
    "v_mfma_f32_32x32x16_bf16 v[208:223], a[116:119], a[96:99], v[208:223]\n"
    "ds_read_b128 a[184:187], v4 offset:18432\n"
    "ds_read_b128 a[188:191], v4 offset:19456\n"
    "v_add_f32 v9, 1.0, v9\n"
    "v_mul_f32 v170, v170, v186\n"
    "v_exp_f32 v11, v11\n"
    "v_mfma_f32_32x32x16_bf16 v[192:207], a[120:123], a[100:103], v[192:207]\n"
    "ds_read_b128 a[192:195], v4 offset:20480\n"
    "ds_read_b128 a[196:199], v4 offset:21504\n"
    "v_mul_f32 v12, s73, v172\n"
    "v_cvt_pk_bf16_f32 v26, v164, v165\n"
    "v_mul_f32 v167, v167, v15\n"
    "v_mfma_f32_32x32x16_bf16 v[208:223], a[124:127], a[100:103], v[208:223]\n"
    "ds_read_b128 a[200:203], v4 offset:22528\n"
    "ds_read_b128 a[204:207], v4 offset:23552\n"
    "v_rcp_f32 v9, v9\n"
    "v_add_f32 v10, 1.0, v10\n"
    "v_mul_f32 v171, v171, v187\n"
    "v_mfma_f32_32x32x16_bf16 v[192:207], a[128:131], a[104:107], v[192:207]\n"
    "ds_read_b128 a[208:211], v4 offset:24576\n"
    "v_exp_f32 v12, v12\n"
    "v_mul_f32 v13, s73, v173\n"
    "v_mul_f32 v168, v168, v8\n"
    "v_rcp_f32 v10, v10\n"
    "v_mfma_f32_32x32x16_bf16 v[208:223], a[132:135], a[104:107], v[208:223]\n"
    "ds_read_b128 a[212:215], v4 offset:25600\n"
    "v_add_f32 v11, 1.0, v11\n"
    "v_mul_f32 v172, v172, v188\n"
    "v_exp_f32 v13, v13\n"
    "v_mul_f32 v14, s73, v174\n"
    "v_mfma_f32_32x32x16_bf16 v[192:207], a[136:139], a[108:111], v[192:207]\n"
    "ds_read_b128 a[216:219], v4 offset:26624\n"
    "v_cvt_pk_bf16_f32 v27, v166, v167\n"
    "buffer_store_dwordx4 v[24:27], v1, s[44:47], 0 offen offset:2048 sc1\n"
    "v_mul_f32 v169, v169, v9\n"
    "v_rcp_f32 v11, v11\n"
    "v_mfma_f32_32x32x16_bf16 v[208:223], a[140:143], a[108:111], v[208:223]\n"
    "ds_read_b128 a[220:223], v4 offset:27648\n"
    "v_add_f32 v12, 1.0, v12\n"
    "v_mul_f32 v173, v173, v189\n"
    "v_exp_f32 v14, v14\n"
    "v_mul_f32 v15, s73, v175\n"
    "v_mfma_f32_32x32x16_bf16 v[224:239], a[144:147], a[0:3], 0\n"
    "ds_read_b128 a[224:227], v4 offset:28672\n"
    "v_mul_f32 v170, v170, v10\n"
    "v_rcp_f32 v12, v12\n"
    "v_add_f32 v13, 1.0, v13\n"
    "v_mul_f32 v174, v174, v190\n"
    "v_mfma_f32_32x32x16_bf16 v[240:255], a[148:151], a[0:3], 0\n"
    "ds_read_b128 a[228:231], v4 offset:29696\n"
    "v_exp_f32 v15, v15\n"
    "v_cvt_pk_bf16_f32 v28, v168, v169\n"
    "v_mul_f32 v171, v171, v11\n"
    "v_rcp_f32 v13, v13\n"
    "v_mfma_f32_32x32x16_bf16 v[224:239], a[152:155], a[4:7], v[224:239]\n"
    "ds_read_b128 a[232:235], v4 offset:30720\n"
    "s_mov_b32 s96, 0x3000\n"
    "buffer_load_dwordx4 a[0:3], v1, s[48:51], s96 offen offset:0\n"
    "v_add_f32 v14, 1.0, v14\n"
    "v_mul_f32 v175, v175, v191\n"
    "v_mul_f32 v172, v172, v12\n"
    "v_rcp_f32 v14, v14\n"
    "v_mfma_f32_32x32x16_bf16 v[240:255], a[156:159], a[4:7], v[240:255]\n"
    "ds_read_b128 a[236:239], v4 offset:31744\n"
    "v_add_f32 v15, 1.0, v15\n"
    "v_cvt_pk_bf16_f32 v29, v170, v171\n"
    "v_mul_f32 v173, v173, v13\n"
    "v_rcp_f32 v15, v15\n"
    "v_mfma_f32_32x32x16_bf16 v[224:239], a[160:163], a[8:11], v[224:239]\n"
    "s_mov_b32 s97, 0x1f000\n"
    "buffer_load_dwordx4 a[4:7], v1, s[48:51], s97 offen offset:0\n"
    "v_mul_f32 v174, v174, v14\n"
    "v_cvt_pk_bf16_f32 v30, v172, v173\n"
    "v_mul_f32 v175, v175, v15\n"
    "v_cvt_pk_bf16_f32 v31, v174, v175\n"
    "v_mfma_f32_32x32x16_bf16 v[240:255], a[164:167], a[8:11], v[240:255]\n"
    "buffer_store_dwordx4 v[28:31], v1, s[44:47], 0 offen offset:3072 sc1\n"
    "v_mul_f32 v8, s73, v192\n"
    "v_exp_f32 v8, v8\n"
    "v_mul_f32 v9, s73, v193\n"
    "v_mfma_f32_32x32x16_bf16 v[224:239], a[168:171], a[12:15], v[224:239]\n"
    "ds_read_b128 a[112:115], v3 offset:32768\n"
    "s_mov_b32 s96, 0x3000\n"
    "buffer_load_dwordx4 a[8:11], v1, s[48:51], s96 offen offset:1024\n"
    "v_mul_f32 v192, v192, v208\n"
    "v_exp_f32 v9, v9\n"
    "v_mul_f32 v10, s73, v194\n"
    "v_add_f32 v8, 1.0, v8\n"
    "v_mfma_f32_32x32x16_bf16 v[240:255], a[172:175], a[12:15], v[240:255]\n"
    "ds_read_b128 a[116:119], v3 offset:33792\n"
    "v_mul_f32 v193, v193, v209\n"
    "v_exp_f32 v10, v10\n"
    "v_mul_f32 v11, s73, v195\n"
    "v_rcp_f32 v8, v8\n"
    "s_waitcnt lgkmcnt(1)\n"
    "v_mfma_f32_32x32x16_bf16 v[224:239], a[176:179], a[16:19], v[224:239]\n"
    "ds_read_b128 a[120:123], v3 offset:34816\n"
    "s_mov_b32 s97, 0x1f000\n"
    "buffer_load_dwordx4 a[12:15], v1, s[48:51], s97 offen offset:1024\n"
    "v_add_f32 v9, 1.0, v9\n"
    "v_mul_f32 v194, v194, v210\n"
    "v_exp_f32 v11, v11\n"
    "v_mul_f32 v12, s73, v196\n"
    "v_mfma_f32_32x32x16_bf16 v[240:255], a[180:183], a[16:19], v[240:255]\n"
    "ds_read_b128 a[124:127], v3 offset:35840\n"
    "v_rcp_f32 v9, v9\n"
    "v_add_f32 v10, 1.0, v10\n"
    "v_mul_f32 v195, v195, v211\n"
    "v_exp_f32 v12, v12\n"
    "v_mfma_f32_32x32x16_bf16 v[224:239], a[184:187], a[20:23], v[224:239]\n"
    "ds_read_b128 a[128:131], v3 offset:36864\n"
    "s_mov_b32 s96, 0x3000\n"
    "buffer_load_dwordx4 a[16:19], v1, s[48:51], s96 offen offset:2048\n"
    "v_mul_f32 v13, s73, v197\n"
    "v_mul_f32 v192, v192, v8\n"
    "v_rcp_f32 v10, v10\n"
    "v_add_f32 v11, 1.0, v11\n"
    "v_mfma_f32_32x32x16_bf16 v[240:255], a[188:191], a[20:23], v[240:255]\n"
    "ds_read_b128 a[132:135], v3 offset:37888\n"
    "v_mul_f32 v196, v196, v212\n"
    "v_exp_f32 v13, v13\n"
    "v_mul_f32 v14, s73, v198\n"
    "v_mul_f32 v193, v193, v9\n"
    "v_mfma_f32_32x32x16_bf16 v[224:239], a[192:195], a[24:27], v[224:239]\n"
    "ds_read_b128 a[136:139], v3 offset:38912\n"
    "s_mov_b32 s97, 0x1f000\n"
    "buffer_load_dwordx4 a[20:23], v1, s[48:51], s97 offen offset:2048\n"
    "v_rcp_f32 v11, v11\n"
    "v_add_f32 v12, 1.0, v12\n"
    "v_mul_f32 v197, v197, v213\n"
    "v_exp_f32 v14, v14\n"
    "v_mfma_f32_32x32x16_bf16 v[240:255], a[196:199], a[24:27], v[240:255]\n"
    "ds_read_b128 a[140:143], v3 offset:39936\n"
    "v_mul_f32 v15, s73, v199\n"
    "v_mul_f32 v194, v194, v10\n"
    "v_rcp_f32 v12, v12\n"
    "v_add_f32 v13, 1.0, v13\n"
    "v_mfma_f32_32x32x16_bf16 v[224:239], a[200:203], a[28:31], v[224:239]\n"
    "ds_read_b128 a[144:147], v3 offset:40960\n"
    "s_mov_b32 s96, 0x3000\n"
    "buffer_load_dwordx4 a[24:27], v1, s[48:51], s96 offen offset:3072\n"
    "v_mul_f32 v198, v198, v214\n"
    "v_exp_f32 v15, v15\n"
    "v_mul_f32 v8, s73, v200\n"
    "v_cvt_pk_bf16_f32 v16, v192, v193\n"
    "v_mfma_f32_32x32x16_bf16 v[240:255], a[204:207], a[28:31], v[240:255]\n"
    "ds_read_b128 a[148:151], v3 offset:41984\n"
    "v_mul_f32 v195, v195, v11\n"
    "v_rcp_f32 v13, v13\n"
    "v_add_f32 v14, 1.0, v14\n"
    "v_mul_f32 v199, v199, v215\n"
    "v_mfma_f32_32x32x16_bf16 v[224:239], a[208:211], a[32:35], v[224:239]\n"
    "ds_read_b128 a[152:155], v3 offset:43008\n"
    "s_mov_b32 s97, 0x1f000\n"
    "buffer_load_dwordx4 a[28:31], v1, s[48:51], s97 offen offset:3072\n"
    "v_exp_f32 v8, v8\n"
    "v_mul_f32 v9, s73, v201\n"
    "v_mul_f32 v196, v196, v12\n"
    "v_rcp_f32 v14, v14\n"
    "v_mfma_f32_32x32x16_bf16 v[240:255], a[212:215], a[32:35], v[240:255]\n"
    "ds_read_b128 a[156:159], v3 offset:44032\n"
    "v_add_f32 v15, 1.0, v15\n"
    "v_mul_f32 v200, v200, v216\n"
    "v_exp_f32 v9, v9\n"
    "v_mul_f32 v10, s73, v202\n"
    "v_mfma_f32_32x32x16_bf16 v[224:239], a[216:219], a[36:39], v[224:239]\n"
    "ds_read_b128 a[160:163], v3 offset:45056\n"
    "s_mov_b32 s96, 0x4000\n"
    "buffer_load_dwordx4 a[32:35], v1, s[48:51], s96 offen offset:0\n"
    "v_cvt_pk_bf16_f32 v17, v194, v195\n"
    "v_mul_f32 v197, v197, v13\n"
    "v_rcp_f32 v15, v15\n"
    "v_add_f32 v8, 1.0, v8\n"
    "v_mfma_f32_32x32x16_bf16 v[240:255], a[220:223], a[36:39], v[240:255]\n"
    "ds_read_b128 a[164:167], v3 offset:46080\n"
    "v_mul_f32 v201, v201, v217\n"
    "v_exp_f32 v10, v10\n"
    "v_mul_f32 v11, s73, v203\n"
    "v_mul_f32 v198, v198, v14\n"
    "v_mfma_f32_32x32x16_bf16 v[224:239], a[224:227], a[40:43], v[224:239]\n"
    "ds_read_b128 a[168:171], v3 offset:47104\n"
    "ds_read_b128 a[172:175], v3 offset:48128\n"
    "s_mov_b32 s97, 0x20000\n"
    "buffer_load_dwordx4 a[36:39], v1, s[48:51], s97 offen offset:0\n"
    "v_rcp_f32 v8, v8\n"
    "v_add_f32 v9, 1.0, v9\n"
    "v_mul_f32 v202, v202, v218\n"
    "v_mfma_f32_32x32x16_bf16 v[240:255], a[228:231], a[40:43], v[240:255]\n"
    "v_exp_f32 v11, v11\n"
    "v_mul_f32 v12, s73, v204\n"
    "v_cvt_pk_bf16_f32 v18, v196, v197\n"
    "v_mul_f32 v199, v199, v15\n"
    "v_mfma_f32_32x32x16_bf16 v[224:239], a[232:235], a[44:47], v[224:239]\n"
    "s_waitcnt vmcnt(14) lgkmcnt(0)\n"
    "s_barrier\n"
    "s_mov_b32 s96, 0x4000\n"
    "buffer_load_dwordx4 a[40:43], v1, s[48:51], s96 offen offset:1024\n"
    "v_rcp_f32 v9, v9\n"
    "v_add_f32 v10, 1.0, v10\n"
    "v_mul_f32 v203, v203, v219\n"
    "v_mfma_f32_32x32x16_bf16 v[240:255], a[236:239], a[44:47], v[240:255]\n"
    "v_exp_f32 v12, v12\n"
    "v_mul_f32 v13, s73, v205\n"
    "v_mul_f32 v200, v200, v8\n"
    "v_rcp_f32 v10, v10\n"
    "v_mfma_f32_32x32x16_bf16 v[224:239], a[112:115], a[48:51], v[224:239]\n"
    "ds_read_b128 a[176:179], v4 offset:32768\n"
    "ds_read_b128 a[180:183], v4 offset:33792\n"
    "s_mov_b32 s97, 0x20000\n"
    "buffer_load_dwordx4 a[44:47], v1, s[48:51], s97 offen offset:1024\n"
    "v_add_f32 v11, 1.0, v11\n"
    "v_mul_f32 v204, v204, v220\n"
    "v_exp_f32 v13, v13\n"
    "v_mfma_f32_32x32x16_bf16 v[240:255], a[116:119], a[48:51], v[240:255]\n"
    "ds_read_b128 a[184:187], v4 offset:34816\n"
    "ds_read_b128 a[188:191], v4 offset:35840\n"
    "v_mul_f32 v14, s73, v206\n"
    "v_cvt_pk_bf16_f32 v19, v198, v199\n"
    "buffer_store_dwordx4 v[16:19], v1, s[44:47], s72 offen offset:0 sc1\n"
    "v_mfma_f32_32x32x16_bf16 v[224:239], a[120:123], a[52:55], v[224:239]\n"
    "ds_read_b128 a[192:195], v4 offset:36864\n"
    "ds_read_b128 a[196:199], v4 offset:37888\n"
    "s_mov_b32 s96, 0x4000\n"
    "buffer_load_dwordx4 a[48:51], v1, s[48:51], s96 offen offset:2048\n"
    "v_mul_f32 v201, v201, v9\n"
    "v_rcp_f32 v11, v11\n"
    "v_add_f32 v12, 1.0, v12\n"
    "v_mfma_f32_32x32x16_bf16 v[240:255], a[124:127], a[52:55], v[240:255]\n"
    "ds_read_b128 a[200:203], v4 offset:38912\n"
    "ds_read_b128 a[204:207], v4 offset:39936\n"
    "v_mul_f32 v205, v205, v221\n"
    "v_exp_f32 v14, v14\n"
    "v_mul_f32 v15, s73, v207\n"
    "v_mfma_f32_32x32x16_bf16 v[224:239], a[128:131], a[56:59], v[224:239]\n"
    "ds_read_b128 a[208:211], v4 offset:40960\n"
    "s_mov_b32 s97, 0x20000\n"
    "buffer_load_dwordx4 a[52:55], v1, s[48:51], s97 offen offset:2048\n"
    "v_mul_f32 v202, v202, v10\n"
    "v_rcp_f32 v12, v12\n"
    "v_add_f32 v13, 1.0, v13\n"
    "v_mul_f32 v206, v206, v222\n"
    "v_mfma_f32_32x32x16_bf16 v[240:255], a[132:135], a[56:59], v[240:255]\n"
    "ds_read_b128 a[212:215], v4 offset:41984\n"
    "v_exp_f32 v15, v15\n"
    "v_cvt_pk_bf16_f32 v20, v200, v201\n"
    "v_mul_f32 v203, v203, v11\n"
    "v_rcp_f32 v13, v13\n"
    "v_mfma_f32_32x32x16_bf16 v[224:239], a[136:139], a[60:63], v[224:239]\n"
    "ds_read_b128 a[216:219], v4 offset:43008\n"
    "s_mov_b32 s96, 0x4000\n"
    "buffer_load_dwordx4 a[56:59], v1, s[48:51], s96 offen offset:3072\n"
    "v_add_f32 v14, 1.0, v14\n"
    "v_mul_f32 v207, v207, v223\n"
    "v_mul_f32 v204, v204, v12\n"
    "v_rcp_f32 v14, v14\n"
    "v_mfma_f32_32x32x16_bf16 v[240:255], a[140:143], a[60:63], v[240:255]\n"
    "ds_read_b128 a[220:223], v4 offset:44032\n"
    "v_add_f32 v15, 1.0, v15\n"
    "v_cvt_pk_bf16_f32 v21, v202, v203\n"
    "v_mul_f32 v205, v205, v13\n"
    "v_rcp_f32 v15, v15\n"
    "v_mfma_f32_32x32x16_bf16 v[224:239], a[144:147], a[64:67], v[224:239]\n"
    "ds_read_b128 a[224:227], v4 offset:45056\n"
    "s_mov_b32 s97, 0x20000\n"
    "buffer_load_dwordx4 a[60:63], v1, s[48:51], s97 offen offset:3072\n"
    "v_mul_f32 v206, v206, v14\n"
    "v_cvt_pk_bf16_f32 v22, v204, v205\n"
    "v_mul_f32 v207, v207, v15\n"
    "v_cvt_pk_bf16_f32 v23, v206, v207\n"
    "v_mfma_f32_32x32x16_bf16 v[240:255], a[148:151], a[64:67], v[240:255]\n"
    "ds_read_b128 a[228:231], v4 offset:46080\n"
    "buffer_store_dwordx4 v[20:23], v1, s[44:47], s72 offen offset:1024 sc1\n"
    "v_mfma_f32_32x32x16_bf16 v[224:239], a[152:155], a[68:71], v[224:239]\n"
    "ds_read_b128 a[232:235], v4 offset:47104\n"
    "s_mov_b32 s96, 0x5000\n"
    "buffer_load_dwordx4 a[64:67], v1, s[48:51], s96 offen offset:0\n"
    "v_mfma_f32_32x32x16_bf16 v[240:255], a[156:159], a[68:71], v[240:255]\n"
    "ds_read_b128 a[236:239], v4 offset:48128\n"
    "v_mfma_f32_32x32x16_bf16 v[224:239], a[160:163], a[72:75], v[224:239]\n"
    "s_mov_b32 s97, 0x21000\n"
    "buffer_load_dwordx4 a[68:71], v1, s[48:51], s97 offen offset:0\n"
    "v_mfma_f32_32x32x16_bf16 v[240:255], a[164:167], a[72:75], v[240:255]\n"
    "v_mfma_f32_32x32x16_bf16 v[224:239], a[168:171], a[76:79], v[224:239]\n"
    "s_mov_b32 s96, 0x5000\n"
    "buffer_load_dwordx4 a[72:75], v1, s[48:51], s96 offen offset:1024\n"
    "v_mfma_f32_32x32x16_bf16 v[240:255], a[172:175], a[76:79], v[240:255]\n"
    "s_waitcnt lgkmcnt(1)\n"
    "v_mfma_f32_32x32x16_bf16 v[224:239], a[176:179], a[80:83], v[224:239]\n"
    "s_mov_b32 s97, 0x21000\n"
    "buffer_load_dwordx4 a[76:79], v1, s[48:51], s97 offen offset:1024\n"
    "v_mfma_f32_32x32x16_bf16 v[240:255], a[180:183], a[80:83], v[240:255]\n"
    "v_mfma_f32_32x32x16_bf16 v[224:239], a[184:187], a[84:87], v[224:239]\n"
    "s_mov_b32 s96, 0x5000\n"
    "buffer_load_dwordx4 a[80:83], v1, s[48:51], s96 offen offset:2048\n"
    "v_mfma_f32_32x32x16_bf16 v[240:255], a[188:191], a[84:87], v[240:255]\n"
    "v_mfma_f32_32x32x16_bf16 v[224:239], a[192:195], a[88:91], v[224:239]\n"
    "s_mov_b32 s97, 0x21000\n"
    "buffer_load_dwordx4 a[84:87], v1, s[48:51], s97 offen offset:2048\n"
    "v_mfma_f32_32x32x16_bf16 v[240:255], a[196:199], a[88:91], v[240:255]\n"
    "v_mfma_f32_32x32x16_bf16 v[224:239], a[200:203], a[92:95], v[224:239]\n"
    "s_mov_b32 s96, 0x5000\n"
    "buffer_load_dwordx4 a[88:91], v1, s[48:51], s96 offen offset:3072\n"
    "v_mfma_f32_32x32x16_bf16 v[240:255], a[204:207], a[92:95], v[240:255]\n"
    "v_mfma_f32_32x32x16_bf16 v[224:239], a[208:211], a[96:99], v[224:239]\n"
    "s_mov_b32 s97, 0x21000\n"
    "buffer_load_dwordx4 a[92:95], v1, s[48:51], s97 offen offset:3072\n"
    "v_mfma_f32_32x32x16_bf16 v[240:255], a[212:215], a[96:99], v[240:255]\n"
    "v_mfma_f32_32x32x16_bf16 v[224:239], a[216:219], a[100:103], v[224:239]\n"
    "s_mov_b32 s96, 0x6000\n"
    "buffer_load_dwordx4 a[96:99], v1, s[48:51], s96 offen offset:0\n"
    "v_mfma_f32_32x32x16_bf16 v[240:255], a[220:223], a[100:103], v[240:255]\n"
    "v_mfma_f32_32x32x16_bf16 v[224:239], a[224:227], a[104:107], v[224:239]\n"
    "s_mov_b32 s97, 0x22000\n"
    "buffer_load_dwordx4 a[100:103], v1, s[48:51], s97 offen offset:0\n"
    "v_mfma_f32_32x32x16_bf16 v[240:255], a[228:231], a[104:107], v[240:255]\n"
    "v_mfma_f32_32x32x16_bf16 v[224:239], a[232:235], a[108:111], v[224:239]\n"
    "s_waitcnt lgkmcnt(0)\n"
    "v_mfma_f32_32x32x16_bf16 v[240:255], a[236:239], a[108:111], v[240:255]\n"
    "s_mov_b64 s[76:77], %[actl]\n"
    "s_mov_b32 s78, 0x23000\n"
    "s_mov_b32 s79, 0x20000\n"
    "s_nop 8\n"
    "v_mul_f32 v8, s73, v224\n"
    "v_exp_f32 v8, v8\n"
    "v_mul_f32 v9, s73, v225\n"
    "v_mul_f32 v224, v224, v240\n"
    "v_exp_f32 v9, v9\n"
    "v_mul_f32 v10, s73, v226\n"
    "v_add_f32 v8, 1.0, v8\n"
    "v_mul_f32 v225, v225, v241\n"
    "v_exp_f32 v10, v10\n"
    "v_mul_f32 v11, s73, v227\n"
    "v_rcp_f32 v8, v8\n"
    "v_add_f32 v9, 1.0, v9\n"
    "v_mul_f32 v226, v226, v242\n"
    "v_exp_f32 v11, v11\n"
    "v_mul_f32 v12, s73, v228\n"
    "v_rcp_f32 v9, v9\n"
    "v_add_f32 v10, 1.0, v10\n"
    "v_mul_f32 v227, v227, v243\n"
    "v_exp_f32 v12, v12\n"
    "v_mul_f32 v13, s73, v229\n"
    "v_mul_f32 v224, v224, v8\n"
    "v_rcp_f32 v10, v10\n"
    "v_add_f32 v11, 1.0, v11\n"
    "v_mul_f32 v228, v228, v244\n"
    "v_exp_f32 v13, v13\n"
    "v_mul_f32 v14, s73, v230\n"
    "v_mul_f32 v225, v225, v9\n"
    "v_rcp_f32 v11, v11\n"
    "v_add_f32 v12, 1.0, v12\n"
    "v_mul_f32 v229, v229, v245\n"
    "v_exp_f32 v14, v14\n"
    "v_mul_f32 v15, s73, v231\n"
    "v_mul_f32 v226, v226, v10\n"
    "v_rcp_f32 v12, v12\n"
    "v_add_f32 v13, 1.0, v13\n"
    "v_mul_f32 v230, v230, v246\n"
    "v_exp_f32 v15, v15\n"
    "v_mul_f32 v8, s73, v232\n"
    "v_cvt_pk_bf16_f32 v24, v224, v225\n"
    "v_mul_f32 v227, v227, v11\n"
    "v_rcp_f32 v13, v13\n"
    "v_add_f32 v14, 1.0, v14\n"
    "v_mul_f32 v231, v231, v247\n"
    "v_exp_f32 v8, v8\n"
    "v_mul_f32 v9, s73, v233\n"
    "v_mul_f32 v228, v228, v12\n"
    "v_rcp_f32 v14, v14\n"
    "v_add_f32 v15, 1.0, v15\n"
    "v_mul_f32 v232, v232, v248\n"
    "v_exp_f32 v9, v9\n"
    "v_mul_f32 v10, s73, v234\n"
    "v_cvt_pk_bf16_f32 v25, v226, v227\n"
    "v_mul_f32 v229, v229, v13\n"
    "v_rcp_f32 v15, v15\n"
    "v_add_f32 v8, 1.0, v8\n"
    "v_mul_f32 v233, v233, v249\n"
    "v_exp_f32 v10, v10\n"
    "v_mul_f32 v11, s73, v235\n"
    "v_mul_f32 v230, v230, v14\n"
    "v_rcp_f32 v8, v8\n"
    "v_add_f32 v9, 1.0, v9\n"
    "v_mul_f32 v234, v234, v250\n"
    "v_exp_f32 v11, v11\n"
    "v_mul_f32 v12, s73, v236\n"
    "v_cvt_pk_bf16_f32 v26, v228, v229\n"
    "v_mul_f32 v231, v231, v15\n"
    "v_rcp_f32 v9, v9\n"
    "v_add_f32 v10, 1.0, v10\n"
    "v_mul_f32 v235, v235, v251\n"
    "v_exp_f32 v12, v12\n"
    "v_mul_f32 v13, s73, v237\n"
    "v_mul_f32 v232, v232, v8\n"
    "v_rcp_f32 v10, v10\n"
    "v_add_f32 v11, 1.0, v11\n"
    "v_mul_f32 v236, v236, v252\n"
    "v_exp_f32 v13, v13\n"
    "v_mul_f32 v14, s73, v238\n"
    "v_cvt_pk_bf16_f32 v27, v230, v231\n"
    "buffer_store_dwordx4 v[24:27], v1, s[44:47], s72 offen offset:2048 sc1\n"
    "v_mul_f32 v233, v233, v9\n"
    "v_rcp_f32 v11, v11\n"
    "v_add_f32 v12, 1.0, v12\n"
    "v_mul_f32 v237, v237, v253\n"
    "v_exp_f32 v14, v14\n"
    "v_mul_f32 v15, s73, v239\n"
    "v_mul_f32 v234, v234, v10\n"
    "v_rcp_f32 v12, v12\n"
    "v_add_f32 v13, 1.0, v13\n"
    "v_mul_f32 v238, v238, v254\n"
    "v_exp_f32 v15, v15\n"
    "v_cvt_pk_bf16_f32 v28, v232, v233\n"
    "v_mul_f32 v235, v235, v11\n"
    "v_rcp_f32 v13, v13\n"
    "v_add_f32 v14, 1.0, v14\n"
    "v_mul_f32 v239, v239, v255\n"
    "v_mul_f32 v236, v236, v12\n"
    "v_rcp_f32 v14, v14\n"
    "v_add_f32 v15, 1.0, v15\n"
    "v_cvt_pk_bf16_f32 v29, v234, v235\n"
    "v_mul_f32 v237, v237, v13\n"
    "v_rcp_f32 v15, v15\n"
    "v_mul_f32 v238, v238, v14\n"
    "v_cvt_pk_bf16_f32 v30, v236, v237\n"
    "v_mul_f32 v239, v239, v15\n"
    "v_cvt_pk_bf16_f32 v31, v238, v239\n"
    "buffer_store_dwordx4 v[28:31], v1, s[44:47], s72 offen offset:3072 sc1\n"
    "s_mov_b32 s96, 0x6000\n"
    "buffer_load_dwordx4 a[104:107], v1, s[48:51], s96 offen offset:1024\n"
    "s_mov_b32 s97, 0x22000\n"
    "buffer_load_dwordx4 a[108:111], v1, s[48:51], s97 offen offset:1024\n"
    "s_mov_b32 s96, 0x6000\n"
    "buffer_load_dwordx4 a[112:115], v1, s[48:51], s96 offen offset:2048\n"
    "s_mov_b32 s97, 0x22000\n"
    "buffer_load_dwordx4 a[116:119], v1, s[48:51], s97 offen offset:2048\n"
    "s_waitcnt vmcnt(4)\n"
    "s_barrier\n"
    "v_mov_b32 v5, %[o3]\n"
    "v_mov_b32 v6, 1\n"
    "s_cmp_eq_u32 %[w], 0\n"
    "s_cbranch_scc0 LSG_%=\n"
    "s_mov_b64 exec, 1\n"
    "global_atomic_add v5, v6, %[sync]\n"
    "s_mov_b64 exec, -1\n"
    "s_nop 2\n"
    "LSG_%=:\n"
    "s_mov_b32 s96, 0x6000\n"
    "buffer_load_dwordx4 a[120:123], v1, s[48:51], s96 offen offset:3072\n"
    "s_mov_b32 s97, 0x22000\n"
    "buffer_load_dwordx4 a[124:127], v1, s[48:51], s97 offen offset:3072\n"
    "s_mov_b64 s[52:53], %[x1]\n"
    "s_mov_b32 s54, 0x800\n"
    "s_mov_b32 s55, 0x20000\n"
    "s_mov_b64 s[56:57], %[gb]\n"
    "s_mov_b32 s58, 0x80\n"
    "s_mov_b32 s59, 0x20000\n"
    "v_and_b32 v3, 31, v0\n"
    "v_lshlrev_b32 v3, 4, v3\n"
    "v_lshrrev_b32 v7, 5, v0\n"
    "v_lshl_add_u32 v3, v7, 11, v3\n"
    "v_lshlrev_b32 v7, 6, v7\n"
    "s_mov_b32 s54, 0x1000\n"
    "v_add_u32 v2, %[lw], v1\n"
    "s_mov_b32 s68, 0\n"
    "v_mov_b32 v8, %[o2d]\n"
    "v_mov_b32 v9, %[o3d]\n"
    "LPB_%=:\n"
    "global_load_dword v5, v8, %[sync] sc1\n"
    "global_load_dword v6, v9, %[sync] sc1\n"
    "s_waitcnt vmcnt(0)\n"
    "v_readfirstlane_b32 s71, v5\n"
    "v_readfirstlane_b32 s75, v6\n"
    "s_nop 3\n"
    "s_lshl_b32 s75, s75, 8\n"
    "s_or_b32 s71, s71, s75\n"
    "s_cmp_eq_u32 s71, 3600\n"
    "s_cbranch_scc1 LPBD_%=\n"
    "s_sleep 1\n"
    "s_add_u32 s68, s68, 1\n"
    "s_cmp_lt_u32 s68, 0x40000\n"
    "s_cbranch_scc1 LPB_%=\n"
    "LPBD_%=:\n"
    "s_mov_b32 s69, 0\n"
    "s_mov_b32 s74, 0x1c000\n"
    "buffer_load_dwordx4 v[160:163], v1, s[76:79], s69 offen offset:0 sc1\n"
    "buffer_load_dwordx4 v[164:167], v1, s[76:79], s74 offen offset:0 sc1\n"
    "buffer_load_dwordx4 v[168:171], v1, s[76:79], s69 offen offset:1024 sc1\n"
    "buffer_load_dwordx4 v[172:175], v1, s[76:79], s74 offen offset:1024 sc1\n"
    "buffer_load_dwordx4 v[176:179], v1, s[76:79], s69 offen offset:2048 sc1\n"
    "buffer_load_dwordx4 v[180:183], v1, s[76:79], s74 offen offset:2048 sc1\n"
    "buffer_load_dwordx4 v[184:187], v1, s[76:79], s69 offen offset:3072 sc1\n"
    "buffer_load_dwordx4 v[188:191], v1, s[76:79], s74 offen offset:3072 sc1\n"
    "s_mov_b32 s69, 0x1000\n"
    "s_mov_b32 s74, 0x1d000\n"
    "buffer_load_dwordx4 v[192:195], v1, s[76:79], s69 offen offset:0 sc1\n"
    "buffer_load_dwordx4 v[196:199], v1, s[76:79], s74 offen offset:0 sc1\n"
    "buffer_load_dwordx4 v[200:203], v1, s[76:79], s69 offen offset:1024 sc1\n"
    "buffer_load_dwordx4 v[204:207], v1, s[76:79], s74 offen offset:1024 sc1\n"
    "buffer_load_dwordx4 v[208:211], v1, s[76:79], s69 offen offset:2048 sc1\n"
    "buffer_load_dwordx4 v[212:215], v1, s[76:79], s74 offen offset:2048 sc1\n"
    "buffer_load_dwordx4 v[216:219], v1, s[76:79], s69 offen offset:3072 sc1\n"
    "buffer_load_dwordx4 v[220:223], v1, s[76:79], s74 offen offset:3072 sc1\n"
    "s_waitcnt vmcnt(15)\n"
    "v_mfma_f32_32x32x16_bf16 a[192:207], v[32:35], v[160:163], 0\n"
    "v_mfma_f32_32x32x16_bf16 a[208:223], v[36:39], v[160:163], 0\n"
    "s_waitcnt vmcnt(14)\n"
    "v_mfma_f32_32x32x16_bf16 a[224:239], v[32:35], v[164:167], 0\n"
    "v_mfma_f32_32x32x16_bf16 a[240:255], v[36:39], v[164:167], 0\n"
    "s_mov_b32 s69, 0x2000\n"
    "s_mov_b32 s74, 0x1e000\n"
    "buffer_load_dwordx4 v[224:227], v1, s[76:79], s69 offen offset:0 sc1\n"
    "buffer_load_dwordx4 v[228:231], v1, s[76:79], s74 offen offset:0 sc1\n"
    "s_waitcnt vmcnt(15)\n"
    "v_mfma_f32_32x32x16_bf16 a[192:207], v[40:43], v[168:171], a[192:207]\n"
    "v_mfma_f32_32x32x16_bf16 a[208:223], v[44:47], v[168:171], a[208:223]\n"
    "s_waitcnt vmcnt(14)\n"
    "v_mfma_f32_32x32x16_bf16 a[224:239], v[40:43], v[172:175], a[224:239]\n"
    "v_mfma_f32_32x32x16_bf16 a[240:255], v[44:47], v[172:175], a[240:255]\n"
    "buffer_load_dwordx4 v[232:235], v1, s[76:79], s69 offen offset:1024 sc1\n"
    "buffer_load_dwordx4 v[236:239], v1, s[76:79], s74 offen offset:1024 sc1\n"
    "s_waitcnt vmcnt(15)\n"
    "v_mfma_f32_32x32x16_bf16 a[192:207], v[48:51], v[176:179], a[192:207]\n"
    "v_mfma_f32_32x32x16_bf16 a[208:223], v[52:55], v[176:179], a[208:223]\n"
    "s_waitcnt vmcnt(14)\n"
    "v_mfma_f32_32x32x16_bf16 a[224:239], v[48:51], v[180:183], a[224:239]\n"
    "v_mfma_f32_32x32x16_bf16 a[240:255], v[52:55], v[180:183], a[240:255]\n"
    "buffer_load_dwordx4 v[240:243], v1, s[76:79], s69 offen offset:2048 sc1\n"
    "buffer_load_dwordx4 v[244:247], v1, s[76:79], s74 offen offset:2048 sc1\n"
    "s_waitcnt vmcnt(15)\n"
    "v_mfma_f32_32x32x16_bf16 a[192:207], v[56:59], v[184:187], a[192:207]\n"
    "v_mfma_f32_32x32x16_bf16 a[208:223], v[60:63], v[184:187], a[208:223]\n"
    "s_waitcnt vmcnt(14)\n"
    "v_mfma_f32_32x32x16_bf16 a[224:239], v[56:59], v[188:191], a[224:239]\n"
    "v_mfma_f32_32x32x16_bf16 a[240:255], v[60:63], v[188:191], a[240:255]\n"
    "buffer_load_dwordx4 v[248:251], v1, s[76:79], s69 offen offset:3072 sc1\n"
    "buffer_load_dwordx4 v[252:255], v1, s[76:79], s74 offen offset:3072 sc1\n"
    "s_waitcnt vmcnt(15)\n"
    "v_mfma_f32_32x32x16_bf16 a[192:207], v[64:67], v[192:195], a[192:207]\n"
    "v_mfma_f32_32x32x16_bf16 a[208:223], v[68:71], v[192:195], a[208:223]\n"
    "s_waitcnt vmcnt(14)\n"
    "v_mfma_f32_32x32x16_bf16 a[224:239], v[64:67], v[196:199], a[224:239]\n"
    "v_mfma_f32_32x32x16_bf16 a[240:255], v[68:71], v[196:199], a[240:255]\n"
    "s_mov_b32 s69, 0x3000\n"
    "s_mov_b32 s74, 0x1f000\n"
    "buffer_load_dwordx4 a[128:131], v1, s[76:79], s69 offen offset:0 sc1\n"
    "buffer_load_dwordx4 a[132:135], v1, s[76:79], s74 offen offset:0 sc1\n"
    "s_waitcnt vmcnt(15)\n"
    "v_mfma_f32_32x32x16_bf16 a[192:207], v[72:75], v[200:203], a[192:207]\n"
    "v_mfma_f32_32x32x16_bf16 a[208:223], v[76:79], v[200:203], a[208:223]\n"
    "s_waitcnt vmcnt(14)\n"
    "v_mfma_f32_32x32x16_bf16 a[224:239], v[72:75], v[204:207], a[224:239]\n"
    "v_mfma_f32_32x32x16_bf16 a[240:255], v[76:79], v[204:207], a[240:255]\n"
    "buffer_load_dwordx4 a[136:139], v1, s[76:79], s69 offen offset:1024 sc1\n"
    "buffer_load_dwordx4 a[140:143], v1, s[76:79], s74 offen offset:1024 sc1\n"
    "s_waitcnt vmcnt(15)\n"
    "v_mfma_f32_32x32x16_bf16 a[192:207], v[80:83], v[208:211], a[192:207]\n"
    "v_mfma_f32_32x32x16_bf16 a[208:223], v[84:87], v[208:211], a[208:223]\n"
    "s_waitcnt vmcnt(14)\n"
    "v_mfma_f32_32x32x16_bf16 a[224:239], v[80:83], v[212:215], a[224:239]\n"
    "v_mfma_f32_32x32x16_bf16 a[240:255], v[84:87], v[212:215], a[240:255]\n"
    "buffer_load_dwordx4 a[144:147], v1, s[76:79], s69 offen offset:2048 sc1\n"
    "buffer_load_dwordx4 a[148:151], v1, s[76:79], s74 offen offset:2048 sc1\n"
    "s_waitcnt vmcnt(15)\n"
    "v_mfma_f32_32x32x16_bf16 a[192:207], v[88:91], v[216:219], a[192:207]\n"
    "v_mfma_f32_32x32x16_bf16 a[208:223], v[92:95], v[216:219], a[208:223]\n"
    "s_waitcnt vmcnt(14)\n"
    "v_mfma_f32_32x32x16_bf16 a[224:239], v[88:91], v[220:223], a[224:239]\n"
    "v_mfma_f32_32x32x16_bf16 a[240:255], v[92:95], v[220:223], a[240:255]\n"
    "buffer_load_dwordx4 a[152:155], v1, s[76:79], s69 offen offset:3072 sc1\n"
    "buffer_load_dwordx4 a[156:159], v1, s[76:79], s74 offen offset:3072 sc1\n"
    "s_waitcnt vmcnt(15)\n"
    "v_mfma_f32_32x32x16_bf16 a[192:207], v[96:99], v[224:227], a[192:207]\n"
    "v_mfma_f32_32x32x16_bf16 a[208:223], v[100:103], v[224:227], a[208:223]\n"
    "s_waitcnt vmcnt(14)\n"
    "v_mfma_f32_32x32x16_bf16 a[224:239], v[96:99], v[228:231], a[224:239]\n"
    "v_mfma_f32_32x32x16_bf16 a[240:255], v[100:103], v[228:231], a[240:255]\n"
    "s_mov_b32 s69, 0x4000\n"
    "s_mov_b32 s74, 0x20000\n"
    "buffer_load_dwordx4 a[160:163], v1, s[76:79], s69 offen offset:0 sc1\n"
    "buffer_load_dwordx4 a[164:167], v1, s[76:79], s74 offen offset:0 sc1\n"
    "s_waitcnt vmcnt(15)\n"
    "v_mfma_f32_32x32x16_bf16 a[192:207], v[104:107], v[232:235], a[192:207]\n"
    "v_mfma_f32_32x32x16_bf16 a[208:223], v[108:111], v[232:235], a[208:223]\n"
    "s_waitcnt vmcnt(14)\n"
    "v_mfma_f32_32x32x16_bf16 a[224:239], v[104:107], v[236:239], a[224:239]\n"
    "v_mfma_f32_32x32x16_bf16 a[240:255], v[108:111], v[236:239], a[240:255]\n"
    "buffer_load_dwordx4 a[168:171], v1, s[76:79], s69 offen offset:1024 sc1\n"
    "buffer_load_dwordx4 a[172:175], v1, s[76:79], s74 offen offset:1024 sc1\n"
    "s_waitcnt vmcnt(15)\n"
    "v_mfma_f32_32x32x16_bf16 a[192:207], v[112:115], v[240:243], a[192:207]\n"
    "v_mfma_f32_32x32x16_bf16 a[208:223], v[116:119], v[240:243], a[208:223]\n"
    "s_waitcnt vmcnt(14)\n"
    "v_mfma_f32_32x32x16_bf16 a[224:239], v[112:115], v[244:247], a[224:239]\n"
    "v_mfma_f32_32x32x16_bf16 a[240:255], v[116:119], v[244:247], a[240:255]\n"
    "buffer_load_dwordx4 a[176:179], v1, s[76:79], s69 offen offset:2048 sc1\n"
    "buffer_load_dwordx4 a[180:183], v1, s[76:79], s74 offen offset:2048 sc1\n"
    "s_waitcnt vmcnt(15)\n"
    "v_mfma_f32_32x32x16_bf16 a[192:207], v[120:123], v[248:251], a[192:207]\n"
    "v_mfma_f32_32x32x16_bf16 a[208:223], v[124:127], v[248:251], a[208:223]\n"
    "s_waitcnt vmcnt(14)\n"
    "v_mfma_f32_32x32x16_bf16 a[224:239], v[120:123], v[252:255], a[224:239]\n"
    "v_mfma_f32_32x32x16_bf16 a[240:255], v[124:127], v[252:255], a[240:255]\n"
    "buffer_load_dwordx4 a[184:187], v1, s[76:79], s69 offen offset:3072 sc1\n"
    "buffer_load_dwordx4 a[188:191], v1, s[76:79], s74 offen offset:3072 sc1\n"
    "s_waitcnt vmcnt(15)\n"
    "v_mfma_f32_32x32x16_bf16 a[192:207], a[0:3], a[128:131], a[192:207]\n"
    "v_mfma_f32_32x32x16_bf16 a[208:223], a[4:7], a[128:131], a[208:223]\n"
    "s_waitcnt vmcnt(14)\n"
    "v_mfma_f32_32x32x16_bf16 a[224:239], a[0:3], a[132:135], a[224:239]\n"
    "v_mfma_f32_32x32x16_bf16 a[240:255], a[4:7], a[132:135], a[240:255]\n"
    "s_mov_b32 s69, 0x5000\n"
    "s_mov_b32 s74, 0x21000\n"
    "buffer_load_dwordx4 v[160:163], v1, s[76:79], s69 offen offset:0 sc1\n"
    "buffer_load_dwordx4 v[164:167], v1, s[76:79], s74 offen offset:0 sc1\n"
    "s_waitcnt vmcnt(15)\n"
    "v_mfma_f32_32x32x16_bf16 a[192:207], a[8:11], a[136:139], a[192:207]\n"
    "v_mfma_f32_32x32x16_bf16 a[208:223], a[12:15], a[136:139], a[208:223]\n"
    "s_waitcnt vmcnt(14)\n"
    "v_mfma_f32_32x32x16_bf16 a[224:239], a[8:11], a[140:143], a[224:239]\n"
    "v_mfma_f32_32x32x16_bf16 a[240:255], a[12:15], a[140:143], a[240:255]\n"
    "buffer_load_dwordx4 v[168:171], v1, s[76:79], s69 offen offset:1024 sc1\n"
    "buffer_load_dwordx4 v[172:175], v1, s[76:79], s74 offen offset:1024 sc1\n"
    "s_waitcnt vmcnt(15)\n"
    "v_mfma_f32_32x32x16_bf16 a[192:207], a[16:19], a[144:147], a[192:207]\n"
    "v_mfma_f32_32x32x16_bf16 a[208:223], a[20:23], a[144:147], a[208:223]\n"
    "s_waitcnt vmcnt(14)\n"
    "v_mfma_f32_32x32x16_bf16 a[224:239], a[16:19], a[148:151], a[224:239]\n"
    "v_mfma_f32_32x32x16_bf16 a[240:255], a[20:23], a[148:151], a[240:255]\n"
    "buffer_load_dwordx4 v[176:179], v1, s[76:79], s69 offen offset:2048 sc1\n"
    "buffer_load_dwordx4 v[180:183], v1, s[76:79], s74 offen offset:2048 sc1\n"
    "s_waitcnt vmcnt(15)\n"
    "v_mfma_f32_32x32x16_bf16 a[192:207], a[24:27], a[152:155], a[192:207]\n"
    "v_mfma_f32_32x32x16_bf16 a[208:223], a[28:31], a[152:155], a[208:223]\n"
    "s_waitcnt vmcnt(14)\n"
    "v_mfma_f32_32x32x16_bf16 a[224:239], a[24:27], a[156:159], a[224:239]\n"
    "v_mfma_f32_32x32x16_bf16 a[240:255], a[28:31], a[156:159], a[240:255]\n"
    "buffer_load_dwordx4 v[184:187], v1, s[76:79], s69 offen offset:3072 sc1\n"
    "buffer_load_dwordx4 v[188:191], v1, s[76:79], s74 offen offset:3072 sc1\n"
    "s_waitcnt vmcnt(15)\n"
    "v_mfma_f32_32x32x16_bf16 a[192:207], a[32:35], a[160:163], a[192:207]\n"
    "v_mfma_f32_32x32x16_bf16 a[208:223], a[36:39], a[160:163], a[208:223]\n"
    "s_waitcnt vmcnt(14)\n"
    "v_mfma_f32_32x32x16_bf16 a[224:239], a[32:35], a[164:167], a[224:239]\n"
    "v_mfma_f32_32x32x16_bf16 a[240:255], a[36:39], a[164:167], a[240:255]\n"
    "s_mov_b32 s69, 0x6000\n"
    "s_mov_b32 s74, 0x22000\n"
    "buffer_load_dwordx4 v[192:195], v1, s[76:79], s69 offen offset:0 sc1\n"
    "buffer_load_dwordx4 v[196:199], v1, s[76:79], s74 offen offset:0 sc1\n"
    "s_waitcnt vmcnt(15)\n"
    "v_mfma_f32_32x32x16_bf16 a[192:207], a[40:43], a[168:171], a[192:207]\n"
    "v_mfma_f32_32x32x16_bf16 a[208:223], a[44:47], a[168:171], a[208:223]\n"
    "s_waitcnt vmcnt(14)\n"
    "v_mfma_f32_32x32x16_bf16 a[224:239], a[40:43], a[172:175], a[224:239]\n"
    "v_mfma_f32_32x32x16_bf16 a[240:255], a[44:47], a[172:175], a[240:255]\n"
    "buffer_load_dwordx4 v[200:203], v1, s[76:79], s69 offen offset:1024 sc1\n"
    "buffer_load_dwordx4 v[204:207], v1, s[76:79], s74 offen offset:1024 sc1\n"
    "s_waitcnt vmcnt(15)\n"
    "v_mfma_f32_32x32x16_bf16 a[192:207], a[48:51], a[176:179], a[192:207]\n"
    "v_mfma_f32_32x32x16_bf16 a[208:223], a[52:55], a[176:179], a[208:223]\n"
    "s_waitcnt vmcnt(14)\n"
    "v_mfma_f32_32x32x16_bf16 a[224:239], a[48:51], a[180:183], a[224:239]\n"
    "v_mfma_f32_32x32x16_bf16 a[240:255], a[52:55], a[180:183], a[240:255]\n"
    "buffer_load_dwordx4 v[208:211], v1, s[76:79], s69 offen offset:2048 sc1\n"
    "buffer_load_dwordx4 v[212:215], v1, s[76:79], s74 offen offset:2048 sc1\n"
    "s_waitcnt vmcnt(15)\n"
    "v_mfma_f32_32x32x16_bf16 a[192:207], a[56:59], a[184:187], a[192:207]\n"
    "v_mfma_f32_32x32x16_bf16 a[208:223], a[60:63], a[184:187], a[208:223]\n"
    "s_waitcnt vmcnt(14)\n"
    "v_mfma_f32_32x32x16_bf16 a[224:239], a[56:59], a[188:191], a[224:239]\n"
    "v_mfma_f32_32x32x16_bf16 a[240:255], a[60:63], a[188:191], a[240:255]\n"
    "buffer_load_dwordx4 v[216:219], v1, s[76:79], s69 offen offset:3072 sc1\n"
    "buffer_load_dwordx4 v[220:223], v1, s[76:79], s74 offen offset:3072 sc1\n"
    "s_waitcnt vmcnt(15)\n"
    "v_mfma_f32_32x32x16_bf16 a[192:207], a[64:67], v[160:163], a[192:207]\n"
    "v_mfma_f32_32x32x16_bf16 a[208:223], a[68:71], v[160:163], a[208:223]\n"
    "s_waitcnt vmcnt(14)\n"
    "v_mfma_f32_32x32x16_bf16 a[224:239], a[64:67], v[164:167], a[224:239]\n"
    "v_mfma_f32_32x32x16_bf16 a[240:255], a[68:71], v[164:167], a[240:255]\n"
    "buffer_load_dwordx4 v[128:131], v3, s[52:55], 0 offen offset:0 sc1\n"
    "buffer_load_dwordx4 v[132:135], v3, s[52:55], 0 offen offset:512 sc1\n"
    "buffer_load_dwordx4 v[136:139], v3, s[52:55], 0 offen offset:1024 sc1\n"
    "buffer_load_dwordx4 v[140:143], v3, s[52:55], 0 offen offset:1536 sc1\n"
    "buffer_load_dwordx4 v[144:147], v7, s[56:59], 0 offen offset:0\n"
    "buffer_load_dwordx4 v[148:151], v7, s[56:59], 0 offen offset:16\n"
    "buffer_load_dwordx4 v[152:155], v7, s[56:59], 0 offen offset:32\n"
    "buffer_load_dwordx4 v[156:159], v7, s[56:59], 0 offen offset:48\n"
    "s_waitcnt vmcnt(21)\n"
    "v_mfma_f32_32x32x16_bf16 a[192:207], a[72:75], v[168:171], a[192:207]\n"
    "v_mfma_f32_32x32x16_bf16 a[208:223], a[76:79], v[168:171], a[208:223]\n"
    "s_waitcnt vmcnt(20)\n"
    "v_mfma_f32_32x32x16_bf16 a[224:239], a[72:75], v[172:175], a[224:239]\n"
    "v_mfma_f32_32x32x16_bf16 a[240:255], a[76:79], v[172:175], a[240:255]\n"
    "s_waitcnt vmcnt(19)\n"
    "v_mfma_f32_32x32x16_bf16 a[192:207], a[80:83], v[176:179], a[192:207]\n"
    "v_mfma_f32_32x32x16_bf16 a[208:223], a[84:87], v[176:179], a[208:223]\n"
    "s_waitcnt vmcnt(18)\n"
    "v_mfma_f32_32x32x16_bf16 a[224:239], a[80:83], v[180:183], a[224:239]\n"
    "v_mfma_f32_32x32x16_bf16 a[240:255], a[84:87], v[180:183], a[240:255]\n"
    "s_waitcnt vmcnt(17)\n"
    "v_mfma_f32_32x32x16_bf16 a[192:207], a[88:91], v[184:187], a[192:207]\n"
    "v_mfma_f32_32x32x16_bf16 a[208:223], a[92:95], v[184:187], a[208:223]\n"
    "s_waitcnt vmcnt(16)\n"
    "v_mfma_f32_32x32x16_bf16 a[224:239], a[88:91], v[188:191], a[224:239]\n"
    "v_mfma_f32_32x32x16_bf16 a[240:255], a[92:95], v[188:191], a[240:255]\n"
    "s_waitcnt vmcnt(15)\n"
    "v_mfma_f32_32x32x16_bf16 a[192:207], a[96:99], v[192:195], a[192:207]\n"
    "v_mfma_f32_32x32x16_bf16 a[208:223], a[100:103], v[192:195], a[208:223]\n"
    "s_waitcnt vmcnt(14)\n"
    "v_mfma_f32_32x32x16_bf16 a[224:239], a[96:99], v[196:199], a[224:239]\n"
    "v_mfma_f32_32x32x16_bf16 a[240:255], a[100:103], v[196:199], a[240:255]\n"
    "s_waitcnt vmcnt(13)\n"
    "v_mfma_f32_32x32x16_bf16 a[192:207], a[104:107], v[200:203], a[192:207]\n"
    "v_mfma_f32_32x32x16_bf16 a[208:223], a[108:111], v[200:203], a[208:223]\n"
    "s_waitcnt vmcnt(12)\n"
    "v_mfma_f32_32x32x16_bf16 a[224:239], a[104:107], v[204:207], a[224:239]\n"
    "v_mfma_f32_32x32x16_bf16 a[240:255], a[108:111], v[204:207], a[240:255]\n"
    "s_waitcnt vmcnt(11)\n"
    "v_mfma_f32_32x32x16_bf16 a[192:207], a[112:115], v[208:211], a[192:207]\n"
    "v_mfma_f32_32x32x16_bf16 a[208:223], a[116:119], v[208:211], a[208:223]\n"
    "s_waitcnt vmcnt(10)\n"
    "v_mfma_f32_32x32x16_bf16 a[224:239], a[112:115], v[212:215], a[224:239]\n"
    "v_mfma_f32_32x32x16_bf16 a[240:255], a[116:119], v[212:215], a[240:255]\n"
    "s_waitcnt vmcnt(9)\n"
    "v_mfma_f32_32x32x16_bf16 a[192:207], a[120:123], v[216:219], a[192:207]\n"
    "v_mfma_f32_32x32x16_bf16 a[208:223], a[124:127], v[216:219], a[208:223]\n"
    "s_waitcnt vmcnt(8)\n"
    "v_mfma_f32_32x32x16_bf16 a[224:239], a[120:123], v[220:223], a[224:239]\n"
    "v_mfma_f32_32x32x16_bf16 a[240:255], a[124:127], v[220:223], a[240:255]\n"
    "s_nop 12\n"
    "ds_write_b128 v2, a[192:195] offset:0\n"
    "ds_write_b128 v2, a[196:199] offset:1024\n"
    "ds_write_b128 v2, a[200:203] offset:2048\n"
    "ds_write_b128 v2, a[204:207] offset:3072\n"
    "ds_write_b128 v2, a[208:211] offset:4096\n"
    "ds_write_b128 v2, a[212:215] offset:5120\n"
    "ds_write_b128 v2, a[216:219] offset:6144\n"
    "ds_write_b128 v2, a[220:223] offset:7168\n"
    "ds_write_b128 v2, a[224:227] offset:8192\n"
    "ds_write_b128 v2, a[228:231] offset:9216\n"
    "ds_write_b128 v2, a[232:235] offset:10240\n"
    "ds_write_b128 v2, a[236:239] offset:11264\n"
    "ds_write_b128 v2, a[240:243] offset:12288\n"
    "ds_write_b128 v2, a[244:247] offset:13312\n"
    "ds_write_b128 v2, a[248:251] offset:14336\n"
    "ds_write_b128 v2, a[252:255] offset:15360\n"
    "s_waitcnt vmcnt(0) lgkmcnt(0)\n"
      : "=&{v[128:131]}"(x0), "=&{v[132:135]}"(x1v), "=&{v[136:139]}"(x2), "=&{v[140:143]}"(x3),
        "=&{v[144:147]}"(g0), "=&{v[148:151]}"(g1), "=&{v[152:155]}"(g2), "=&{v[156:159]}"(g3)
      : [hf] "s"(ws + WS_H2F + (size_t)(gq * 4 + w) * (NS * 1024)), [wgu] "s"(ws + WS_WGU + (size_t)fc * 229376),
        [act] "s"(ws + WS_ACT + ((size_t)(gq * 4 + w) * 112 + fc * 8) * 1024), [w] "s"(w), [lb] "s"(lb), [lw] "s"(lb + w * 16384),
        [actl] "s"(ws + WS_ACT + ((size_t)(2 * rb) * 112 + 28 * w) * 1024), [wd] "s"(ws + WS_WD + ((size_t)(2 * nb) * 112 + 28 * w) * 1024),
        [x1] "s"(ws + WS_X1 + ((size_t)(2 * rb + (w >> 1)) * NS + 4 * nb + 2 * (w & 1)) * 2048), [gb] "s"(ws + (5 * D + 16 * (4 * nb + 2 * (w & 1))) * 4),
        [sync] "s"(ws + WS_SYNC), [o2] "s"((16 + gq) * 256), [o3] "s"((32 + gq) * 256), [o2d] "s"((16 + qd) * 256), [o3d] "s"((32 + qd) * 256)
      : "v0", "v1", "v2", "v3", "v4", "v5", "v6", "v7", "v8", "v9", "v10", "v11", "v12", "v13", "v14", "v15", "v16", "v17", "v18", "v19", "v20", "v21", "v22", "v23", "v24", "v25", "v26", "v27", "v28", "v29", "v30", "v31", "v32", "v33", "v34", "v35", "v36", "v37", "v38", "v39", "v40", "v41", "v42", "v43", "v44", "v45", "v46", "v47", "v48", "v49", "v50", "v51", "v52", "v53", "v54", "v55", "v56", "v57", "v58", "v59", "v60", "v61", "v62", "v63", "v64", "v65", "v66", "v67", "v68", "v69", "v70", "v71", "v72", "v73", "v74", "v75", "v76", "v77", "v78", "v79", "v80", "v81", "v82", "v83", "v84", "v85", "v86", "v87", "v88", "v89", "v90", "v91", "v92", "v93", "v94", "v95", "v96", "v97", "v98", "v99", "v100", "v101", "v102", "v103", "v104", "v105", "v106", "v107", "v108", "v109", "v110", "v111", "v112", "v113", "v114", "v115", "v116", "v117", "v118", "v119", "v120", "v121", "v122", "v123", "v124", "v125", "v126", "v127", "v160", "v161", "v162", "v163", "v164", "v165", "v166", "v167", "v168", "v169", "v170", "v171", "v172", "v173", "v174", "v175", "v176", "v177", "v178", "v179", "v180", "v181", "v182", "v183", "v184", "v185", "v186", "v187", "v188", "v189", "v190", "v191", "v192", "v193", "v194", "v195", "v196", "v197", "v198", "v199", "v200", "v201", "v202", "v203", "v204", "v205", "v206", "v207", "v208", "v209", "v210", "v211", "v212", "v213", "v214", "v215", "v216", "v217", "v218", "v219", "v220", "v221", "v222", "v223", "v224", "v225", "v226", "v227", "v228", "v229", "v230", "v231", "v232", "v233", "v234", "v235", "v236", "v237", "v238", "v239", "v240", "v241", "v242", "v243", "v244", "v245", "v246", "v247", "v248", "v249", "v250", "v251", "v252", "v253", "v254", "v255", "a0", "a1", "a2", "a3", "a4", "a5", "a6", "a7", "a8", "a9", "a10", "a11", "a12", "a13", "a14", "a15", "a16", "a17", "a18", "a19", "a20", "a21", "a22", "a23", "a24", "a25", "a26", "a27", "a28", "a29", "a30", "a31", "a32", "a33", "a34", "a35", "a36", "a37", "a38", "a39", "a40", "a41", "a42", "a43", "a44", "a45", "a46", "a47", "a48", "a49", "a50", "a51", "a52", "a53", "a54", "a55", "a56", "a57", "a58", "a59", "a60", "a61", "a62", "a63", "a64", "a65", "a66", "a67", "a68", "a69", "a70", "a71", "a72", "a73", "a74", "a75", "a76", "a77", "a78", "a79", "a80", "a81", "a82", "a83", "a84", "a85", "a86", "a87", "a88", "a89", "a90", "a91", "a92", "a93", "a94", "a95", "a96", "a97", "a98", "a99", "a100", "a101", "a102", "a103", "a104", "a105", "a106", "a107", "a108", "a109", "a110", "a111", "a112", "a113", "a114", "a115", "a116", "a117", "a118", "a119", "a120", "a121", "a122", "a123", "a124", "a125", "a126", "a127", "a128", "a129", "a130", "a131", "a132", "a133", "a134", "a135", "a136", "a137", "a138", "a139", "a140", "a141", "a142", "a143", "a144", "a145", "a146", "a147", "a148", "a149", "a150", "a151", "a152", "a153", "a154", "a155", "a156", "a157", "a158", "a159", "a160", "a161", "a162", "a163", "a164", "a165", "a166", "a167", "a168", "a169", "a170", "a171", "a172", "a173", "a174", "a175", "a176", "a177", "a178", "a179", "a180", "a181", "a182", "a183", "a184", "a185", "a186", "a187", "a188", "a189", "a190", "a191", "a192", "a193", "a194", "a195", "a196", "a197", "a198", "a199", "a200", "a201", "a202", "a203", "a204", "a205", "a206", "a207", "a208", "a209", "a210", "a211", "a212", "a213", "a214", "a215", "a216", "a217", "a218", "a219", "a220", "a221", "a222", "a223", "a224", "a225", "a226", "a227", "a228", "a229", "a230", "a231", "a232", "a233", "a234", "a235", "a236", "a237", "a238", "a239", "a240", "a241", "a242", "a243", "a244", "a245", "a246", "a247", "a248", "a249", "a250", "a251", "a252", "a253", "a254", "a255", "s36", "s37", "s38", "s39", "s40", "s41", "s42", "s43", "s44", "s45", "s46", "s47", "s48", "s49", "s50", "s51", "s52", "s53", "s54", "s55", "s56", "s57", "s58", "s59", "s60", "s61", "s62", "s63", "s64", "s65", "s66", "s67", "s68", "s69", "s70", "s71", "s72", "s73", "s74", "s75", "s76", "s77", "s78", "s79", "s80", "s81", "s82", "s83", "s84", "s85", "s86", "s87", "s88", "s89", "s90", "s91", "s92", "s93", "s94", "s95", "s96", "s97", "s98", "s99", "vcc", "scc", "m0", "memory");
    __syncthreads();
    char* outp;
    asm volatile("s_load_dwordx2 %0, %1, 0x48\n s_waitcnt lgkmcnt(0)\n" : "=&s"(outp) : "s"(ka));
    int lane;
    asm volatile("v_mbcnt_lo_u32_b32 %0, -1, 0\n v_mbcnt_hi_u32_b32 %0, -1, %0\n" : "=v"(lane));
    const int rr = lane & 31, hh = lane >> 5, qo = 2 * rb + (w >> 1), s = 4 * nb + 2 * (w & 1) + hh;
    f32x4 y[4];
#pragma unroll
    for (int gI = 0; gI < 4; ++gI) {
      const char* p = lds + (4 * w + gI) * 1024 + lane * 16;
      y[gI] = (*(const f32x4*)p + *(const f32x4*)(p + 16384)) + (*(const f32x4*)(p + 32768) + *(const f32x4*)(p + 49152));
    }
    const f32x4 o0 = x0 + g0 * y[0], o1 = x1v + g1 * y[1], o2 = x2 + g2 * y[2], o3 = x3 + g3 * y[3];
    u32x4 a, bb;
    a[0] = cvtpk(o0[0], o0[1]); a[1] = cvtpk(o0[2], o0[3]); a[2] = cvtpk(o1[0], o1[1]); a[3] = cvtpk(o1[2], o1[3]);
    bb[0] = cvtpk(o2[0], o2[1]); bb[1] = cvtpk(o2[2], o2[3]); bb[2] = cvtpk(o3[0], o3[1]); bb[3] = cvtpk(o3[2], o3[3]);
    const __amdgpu_buffer_rsrc_t ro = __builtin_amdgcn_make_buffer_rsrc((void*)outp, 0, S * D * 2, 0x00020000);
    const int oo = ((qo * 32 + rr) * D + 16 * s) * 2;
    __builtin_amdgcn_raw_buffer_store_b128(a, ro, oo, 0, 0); __builtin_amdgcn_raw_buffer_store_b128(bb, ro, oo + 16, 0, 0);
  }

}


extern "C" void kernel_launch(void* const* d_in, const int* in_sizes, int n_in, void* d_out, int out_size, void* d_ws, size_t ws_size, hipStream_t stream) {
  const bf16* x = (const bf16*)d_in[0]; const bf16* Kc = (const bf16*)d_in[1]; const bf16* Vc = (const bf16*)d_in[2];
  const bf16* wq = (const bf16*)d_in[3]; const bf16* wo = (const bf16*)d_in[4];
  const bf16* wg = (const bf16*)d_in[6]; const bf16* wu = (const bf16*)d_in[7]; const bf16* wd = (const bf16*)d_in[8];
  const bf16* c = (const bf16*)d_in[9]; const bf16* wmod = (const bf16*)d_in[10];
  char* ws = (char*)d_ws;
  float* mod = (float*)(ws + WS_MOD); bf16* hf = (bf16*)(ws + WS_HF); bf16* h2f = (bf16*)(ws + WS_H2F); float* x1 = (float*)(ws + WS_X1);
  bf16* yp = (bf16*)(ws + WS_YP); bf16* act = (bf16*)(ws + WS_ACT); bf16* wgu = (bf16*)(ws + WS_WGU); bf16* wdf = (bf16*)(ws + WS_WD);
  k_front<<<NMODB + NLNB + NREL, 256, 0, stream>>>(c, wmod, mod, wg, wu, wd, wgu, wdf, x, hf, (unsigned*)(ws + WS_SYNC));
  k_attn<<<256, 256, 0, stream>>>(hf, wq, Kc, Vc, wo, yp, (const float*)(ws + WS_AB), x, ws, (bf16*)d_out);
  return;
}
```
